```python
import torch

def _softmax(x, axis=-1):
    return torch.softmax(x, dim=axis)
def _gelu(x):
    return torch.nn.functional.gelu(x, approximate="tanh")
def _silu(x):
    return torch.nn.functional.silu(x)
def _sigmoid(x):
    return torch.sigmoid(x)
def _layernorm(x, axis=-1, eps=1e-5):
    m = x.mean(dim=axis, keepdim=True)
    v = x.var(dim=axis, keepdim=True, unbiased=False)
    return (x - m) / torch.sqrt(v + eps)
def _scatter_add(tgt, idx, src, axis=0):
    return tgt.clone().index_add_(axis, idx, src)
def _segment_sum(src, seg, n):
    out = torch.zeros((n,) + src.shape[1:], dtype=src.dtype)
    return out.index_add_(0, seg, src)
def _pad(x, pw):
    flat = tuple(v for pair in reversed(pw) for v in pair)
    return torch.nn.functional.pad(x, flat)

kernel_name = 'concat_heads__spectral_gfnet__ssm_conv_s4__prompt'

def setup_inputs():

    _w = (lambda a: (lambda t: t.to(torch.bfloat16) if t.is_floating_point() else t)(torch.as_tensor(a)))
    def _A_setup():
        B, S, D = 1, 2048, 640
        Df = D // 2 + 1
        return {
            "x": _w((lambda *s: torch.randn(*s, dtype=torch.bfloat16) * 0.05)(B, S, D)),
            "w_r": _w((lambda *s: torch.randn(*s, dtype=torch.bfloat16) * 0.8)(S, Df) * 0.02),
            "w_i": _w((lambda *s: torch.randn(*s, dtype=torch.bfloat16) * 1.6)(S, Df) * 0.02),
        }
    _A_in = _A_setup()

    def _B_setup():
        B, L, D, N = 1, 2048, 640, 128
        A_re = -(lambda _x: torch.exp(torch.as_tensor(_x)))((lambda *s: torch.randn(*s, dtype=torch.bfloat16) * 3.2)(D, N))
        A_im = torch.pi * (lambda *s: torch.randn(*s, dtype=torch.bfloat16) * 0.05)(D, N)
        B_re = (lambda *s: torch.randn(*s, dtype=torch.bfloat16) * 0.1)(D, N)
        B_im = (lambda *s: torch.randn(*s, dtype=torch.bfloat16) * 0.1)(D, N)
        C_re = (lambda *s: torch.randn(*s, dtype=torch.bfloat16) * 0.1)(D, N)
        C_im = (lambda *s: torch.randn(*s, dtype=torch.bfloat16) * 0.1)(D, N)
        log_dt = (lambda *s: torch.randn(*s, dtype=torch.bfloat16) * 0.05)(D) * 0.5 - 4.0
        Dskip = (lambda *s: torch.randn(*s, dtype=torch.bfloat16) * 0.05)(D)
        return {
            "u": _w((lambda *s: torch.randn(*s, dtype=torch.bfloat16) * 0.05)(B, L, D)),
            "A_re": _w(A_re),
            "A_im": _w(A_im),
            "B_re": _w(B_re),
            "B_im": _w(B_im),
            "C_re": _w(C_re),
            "C_im": _w(C_im),
            "log_dt": _w(log_dt),
            "Dskip": _w(Dskip),
        }
    _B_in = _B_setup()

    return {
        "A_x": _A_in["x"],
        "A_w_r": _A_in["w_r"],
        "A_w_i": _A_in["w_i"],
        "B_u": _B_in["u"],
        "B_A_re": _B_in["A_re"],
        "B_A_im": _B_in["A_im"],
        "B_B_re": _B_in["B_re"],
        "B_B_im": _B_in["B_im"],
        "B_C_re": _B_in["C_re"],
        "B_C_im": _B_in["C_im"],
        "B_log_dt": _B_in["log_dt"],
        "B_Dskip": _B_in["Dskip"],
    }

def reference(A_x, A_w_r, A_w_i, B_u, B_A_re, B_A_im, B_B_re, B_B_im, B_C_re, B_C_im, B_log_dt, B_Dskip):
    def _A_ref(x, w_r, w_i):
        B, S, D = x.shape
        X = (lambda x, axes=(-2,-1): torch.fft.rfft2(x.float(), dim=axes))(x, axes=(-2, -1))
        W = w_r + 1j * w_i
        X = X * W
        y = (lambda x, s, axes=(-2,-1): torch.fft.irfft2(x, s=s, dim=axes).to(torch.bfloat16))(X, (S, D), axes=(-2, -1))
        return y

    def _B_ref(u, A_re, A_im, B_re, B_im, C_re, C_im, log_dt, Dskip):
        Bsz, L, D = u.shape
        dt = torch.exp(log_dt)[:, None]
        A = A_re + 1j * A_im
        Bc = B_re + 1j * B_im
        Cc = C_re + 1j * C_im
        logdA = dt * A
        dB = dt * Bc
        l = (lambda x, dt: x.to(dt))(torch.arange(L), A_re.dtype)
        P = torch.exp(logdA[:, :, None] * l[None, None, :])
        K = torch.einsum("dn,dnl,dn->dl", Cc, P, dB).real
        L2 = 2 * L
        Kp = _pad(K, ((0, 0), (0, L)))
        up = (lambda x, *d: x.permute(*d))(u, 0, 2, 1)
        up = _pad(up, ((0, 0), (0, 0), (0, L)))
        Kf = (lambda x, axes=(-2,-1): torch.fft.rfft2(x.float(), dim=axes))(Kp[:, None, :], axes=(-2, -1))
        Uf = (lambda x, axes=(-2,-1): torch.fft.rfft2(x.float(), dim=axes))(up[:, :, None, :], axes=(-2, -1))
        Yf = Uf * Kf[None, :, :, :]
        y = (lambda x, s, axes=(-2,-1): torch.fft.irfft2(x, s=s, dim=axes).to(torch.bfloat16))(Yf, (1, L2), axes=(-2, -1))[:, :, 0, :L]
        y = (lambda x, *d: x.permute(*d))(y, 0, 2, 1) + u * Dskip[None, None, :]
        return y

    a = _A_ref(A_x, A_w_r, A_w_i)
    b = _B_ref(B_u, B_A_re, B_A_im, B_B_re, B_B_im, B_C_re, B_C_im, B_log_dt, B_Dskip)
    return torch.concatenate([a, b], axis=-1)
```

```cpp
#include <hip/hip_runtime.h>
#include <hip/hip_bf16.h>

typedef unsigned short u16;
typedef unsigned int u32;
typedef __attribute__((ext_vector_type(2))) __bf16 bf16x2;
typedef __attribute__((ext_vector_type(8))) __bf16 bf16x8;
typedef __attribute__((ext_vector_type(2))) float f32x2;
typedef __attribute__((ext_vector_type(4))) float f32x4;
typedef __attribute__((ext_vector_type(16))) float f32x16;
typedef __attribute__((ext_vector_type(4))) u32 u32x4;
typedef __attribute__((ext_vector_type(2))) u32 u32x2;
typedef __attribute__((ext_vector_type(4))) short s16x4;
#define LAS __attribute__((address_space(3)))
#define DI __device__ __forceinline__

constexpr size_t SZ_X1 = 256u * 324u * 32u;
constexpr size_t OFF_X1 = 0;
constexpr size_t OFF_WT = OFF_X1 + SZ_X1;
constexpr size_t OFF_UT = OFF_WT + SZ_X1;
constexpr size_t SZ_UT = 256u * 640u * 16u;
constexpr size_t OFF_KR = OFF_UT + SZ_UT;
constexpr size_t SZ_KR = 640u * 4128u;
constexpr size_t OFF_Y1 = OFF_KR + SZ_KR;
constexpr size_t SZ_Y1 = 81u * 32768u;
constexpr size_t OFF_YT = OFF_Y1 + SZ_Y1;

DI u32 pk(float a, float b) {
    f32x2 v = {a, b};
    return __builtin_bit_cast(u32, __builtin_convertvector(v, bf16x2));
}
DI float bflo(u32 v) { return __uint_as_float(v << 16); }
DI float bfhi(u32 v) { return __uint_as_float(v & 0xffff0000u); }
DI float bf2f(u16 v) { return __uint_as_float((u32)v << 16); }
DI float rbf(float a) { return bflo(pk(a, a)); }
DI float cosr(float rev) { return __builtin_amdgcn_cosf(rev); }
DI float sinr(float rev) { return __builtin_amdgcn_sinf(rev); }
DI f32x4 mfma16(u32x4 a, u32x4 b, f32x4 c) {
    return __builtin_amdgcn_mfma_f32_16x16x32_bf16(__builtin_bit_cast(bf16x8, a), __builtin_bit_cast(bf16x8, b), c, 0, 0, 0);
}
DI f32x16 mfma32(u32x4 a, u32x4 b, f32x16 c) {
    return __builtin_amdgcn_mfma_f32_32x32x16_bf16(__builtin_bit_cast(bf16x8, a), __builtin_bit_cast(bf16x8, b), c, 0, 0, 0);
}
DI u32x2 trread(const char* p) {
    return __builtin_bit_cast(u32x2, __builtin_amdgcn_ds_read_tr16_b64_v4i16((LAS s16x4*)(p)));
}
DI u32x4 neg8(u32x4 v) { return v ^ 0x80008000u; }
DI u32 permlo(u32 a, u32 b) { return __builtin_amdgcn_perm(b, a, 0x05040100u); }
DI u32 permhi(u32 a, u32 b) { return __builtin_amdgcn_perm(b, a, 0x07060302u); }
DI int crow(int r, int h) { return (r & 3) + 8 * (r >> 2) + 4 * h; }

DI void role_p1(const u16* __restrict__ x, char* __restrict__ X1, char* smem, int b) {
    const int t = threadIdx.x, w = t >> 6, l = t & 63, g = l >> 4, i16 = l & 15;
    const u32x4* src = (const u32x4*)(x + (size_t)(16 * b + 4 * w) * 640);
    u32x4 xv[5];
#pragma unroll
    for (int i = 0; i < 5; ++i) xv[i] = src[l + 64 * i];

    u32x4 f20c, f20s;
    u32x4 c32, s32;
    float twc[8], tws[8];
    {
        float c[8], s[8];
#pragma unroll
        for (int j = 0; j < 8; ++j) {
            const int d2 = (j < 4) ? 4 * g + j : 16 + 4 * g + j - 4;
            const bool ok = (d2 < 20) && (i16 <= 10);
            const float a = (float)(d2 * i16) * 0.05f;
            c[j] = ok ? cosr(a) : 0.f;
            s[j] = ok ? -sinr(a) : 0.f;
        }
        f20c = u32x4{pk(c[0], c[1]), pk(c[2], c[3]), pk(c[4], c[5]), pk(c[6], c[7])};
        f20s = u32x4{pk(s[0], s[1]), pk(s[2], s[3]), pk(s[4], s[5]), pk(s[6], s[7])};
#pragma unroll
        for (int j = 0; j < 8; ++j) {
            const int d1 = (j < 4) ? 4 * g + j : 16 + 4 * g + j - 4;
            const float a = (float)((d1 * i16) & 31) * 0.03125f;
            c[j] = cosr(a);
            s[j] = sinr(a);
            const float a2 = (float)(d1 * i16) * (1.0f / 640.0f);
            twc[j] = cosr(a2);
            tws[j] = sinr(a2);
        }
        c32 = u32x4{pk(c[0], c[1]), pk(c[2], c[3]), pk(c[4], c[5]), pk(c[6], c[7])};
        s32 = u32x4{pk(s[0], s[1]), pk(s[2], s[3]), pk(s[4], s[5]), pk(s[6], s[7])};
    }
    const u32x4 ns32 = neg8(s32), c32b = c32 ^ 0x80000000u, s32b = s32 ^ 0x80000000u, nc32b = neg8(c32b);

    char* xs = smem + w * 5120;
#pragma unroll
    for (int i = 0; i < 5; ++i) {
        const int ci = l + 64 * i, c = ci % 80;
        *(u32x4*)(xs + ((ci * 16) ^ (((c >> 4) & 1) << 5))) = xv[i];
    }
    const int q = i16 >> 2, p = i16 & 3;
    const int d2a = 4 * g + q;
    const int d2b = (g == 0) ? 16 + q : d2a;
    const int offa = 64 * d2a + 8 * p, swa = ((d2a >> 2) & 1) << 5;
    const int offb = 64 * d2b + 8 * p, swb = ((d2b >> 2) & 1) << 5;

    u32 outp[2][16];
#pragma unroll
    for (int rp = 0; rp < 2; ++rp) {
        f32x4 z[2][4];
#pragma unroll
        for (int rr = 0; rr < 2; ++rr) {
            const char* xr = xs + (2 * rp + rr) * 1280;
            f32x4 tre[2], tim[2];
#pragma unroll
            for (int tau = 0; tau < 2; ++tau) {
                const u32x2 a0 = trread(xr + offa + ((32 * tau) ^ swa));
                const u32x2 a1 = trread(xr + offb + ((32 * tau) ^ swb));
                const u32x4 a = {a0.x, a0.y, a1.x, a1.y};
                const f32x4 zero = {0.f, 0.f, 0.f, 0.f};
                tre[tau] = mfma16(a, f20c, zero);
                tim[tau] = mfma16(a, f20s, zero);
            }
            float pr[8], pi[8];
#pragma unroll
            for (int j = 0; j < 8; ++j) {
                const float a = tre[j >> 2][j & 3], bb = tim[j >> 2][j & 3];
                pr[j] = a * twc[j] + bb * tws[j];
                pi[j] = bb * twc[j] - a * tws[j];
            }
            const u32x4 bre = {pk(pr[0], pr[1]), pk(pr[2], pr[3]), pk(pr[4], pr[5]), pk(pr[6], pr[7])};
            const u32x4 bim = {pk(pi[0], pi[1]), pk(pi[2], pi[3]), pk(pi[4], pi[5]), pk(pi[6], pi[7])};
            const f32x4 zero = {0.f, 0.f, 0.f, 0.f};
            z[rr][0] = mfma16(s32, bim, mfma16(c32, bre, zero));
            z[rr][1] = mfma16(ns32, bre, mfma16(c32, bim, zero));
            z[rr][2] = mfma16(s32b, bim, mfma16(c32b, bre, zero));
            z[rr][3] = mfma16(s32b, bre, mfma16(nc32b, bim, zero));
        }
#pragma unroll
        for (int m = 0; m < 4; ++m)
#pragma unroll
            for (int i = 0; i < 4; ++i) outp[rp][m * 4 + i] = pk(z[0][m][i], z[1][m][i]);
    }
    __syncthreads();
    char* st = smem + (w >> 1) * 10304 + (w & 1) * 8;
    if (i16 <= 10) {
#pragma unroll
        for (int i = 0; i < 4; ++i) {
            const int k = i16 + 20 * (4 * g + i);
            *(u32x2*)(st + k * 32) = u32x2{outp[0][i], outp[1][i]};
            *(u32x2*)(st + k * 32 + 16) = u32x2{outp[0][4 + i], outp[1][4 + i]};
        }
#pragma unroll
        for (int i = 0; i < 4; ++i) {
            const int k = 320 - i16 - 20 * (4 * g + i);
            const bool ok = (i16 >= 1 && i16 <= 9) || (i16 == 0 && g == 0 && i == 0);
            if (ok) {
                *(u32x2*)(st + k * 32) = u32x2{outp[0][8 + i], outp[1][8 + i]};
                *(u32x2*)(st + k * 32 + 16) = u32x2{outp[0][12 + i], outp[1][12 + i]};
            }
        }
    }
    __syncthreads();
#pragma unroll
    for (int i = 0; i < 6; ++i) {
        const int m = t + 256 * i;
        if (m < 1284) {
            const int sb = m >= 642, c = m - 642 * sb;
            *(u32x4*)(X1 + (size_t)(2 * b + sb) * (324 * 32) + c * 16) = *(const u32x4*)(smem + sb * 10304 + c * 16);
        }
    }
}

DI void role_tr(const u16* __restrict__ wr, const u16* __restrict__ wi, const u16* __restrict__ u,
                char* __restrict__ WT, char* __restrict__ UT, int b) {
    const int t = threadIdx.x;
    const int e = b >> 7, half = (b >> 6) & 1, kap2 = b & 31, h = (b >> 5) & 1;
#pragma unroll
    for (int it = 0; it < 2; ++it) {
        const int task = t + 256 * it;
        if (task < 321) {
            u32 re[4], im[4];
#pragma unroll
            for (int j = 0; j < 4; ++j) {
                const int n0 = 16 * half + 8 * ((2 * j) >> 2) + 4 * h + ((2 * j) & 3);
                const int s0 = kap2 + 32 * (2 * n0 + e), s1 = s0 + 64;
                re[j] = (u32)wr[s0 * 321 + task] | ((u32)wr[s1 * 321 + task] << 16);
                im[j] = (u32)wi[s0 * 321 + task] | ((u32)wi[s1 * 321 + task] << 16);
            }
            u32x4* dst = (u32x4*)(WT + ((size_t)b * 324 + task) * 32);
            dst[0] = u32x4{re[0], re[1], re[2], re[3]};
            dst[1] = u32x4{im[0], im[1], im[2], im[3]};
        } else if (task < 481) {
            const int qd = task - 321;
            u32x2 v[8];
#pragma unroll
            for (int r = 0; r < 8; ++r) v[r] = *(const u32x2*)(u + (size_t)(8 * b + r) * 640 + 4 * qd);
            u32x4* dst = (u32x4*)(UT + ((size_t)b * 640 + 4 * qd) * 16);
            dst[0] = u32x4{permlo(v[0].x, v[1].x), permlo(v[2].x, v[3].x), permlo(v[4].x, v[5].x), permlo(v[6].x, v[7].x)};
            dst[1] = u32x4{permhi(v[0].x, v[1].x), permhi(v[2].x, v[3].x), permhi(v[4].x, v[5].x), permhi(v[6].x, v[7].x)};
            dst[2] = u32x4{permlo(v[0].y, v[1].y), permlo(v[2].y, v[3].y), permlo(v[4].y, v[5].y), permlo(v[6].y, v[7].y)};
            dst[3] = u32x4{permhi(v[0].y, v[1].y), permhi(v[2].y, v[3].y), permhi(v[4].y, v[5].y), permhi(v[6].y, v[7].y)};
        }
    }
}

struct cf { float re, im; };
DI cf cmul(cf a, cf b) { return cf{a.re * b.re - a.im * b.im, a.re * b.im + a.im * b.re}; }
DI cf cexpk(float lre, float lim_rev, float k) {
    const float m = __builtin_amdgcn_exp2f(k * lre * 1.44269504088896341f);
    const float a = __builtin_amdgcn_fractf(k * lim_rev);
    return cf{m * cosr(a), m * sinr(a)};
}
constexpr int KG_RS = 528;
constexpr int KG_B = 41 * KG_RS;
constexpr int KG_T = 57 * KG_RS;
DI void role_kgen(const u16* __restrict__ Are, const u16* __restrict__ Aim, const u16* __restrict__ Bre,
                  const u16* __restrict__ Bim, const u16* __restrict__ Cre, const u16* __restrict__ Cim,
                  const u16* __restrict__ logdt, char* __restrict__ KR, char* smem, int d) {
    const int t = threadIdx.x, n = t & 127, half = t >> 7, w = t >> 6, l = t & 63;
    const int idx = d * 128 + n;
    const float are = bf2f(Are[idx]), aim = bf2f(Aim[idx]), bre = bf2f(Bre[idx]), bim = bf2f(Bim[idx]);
    const float cre = bf2f(Cre[idx]), cim = bf2f(Cim[idx]);
    const float dt = rbf(expf(bf2f(logdt[d])));
    const float lre = dt * are, lim = dt * aim * 0.15915494309189535f;
    const float dbr = dt * bre, dbi = dt * bim;
    const cf c = {cre * dbr - cim * dbi, cre * dbi + cim * dbr};
    char* rowp = smem + n * 4;
    const cf z128 = cexpk(lre, lim, 128.f);
    if (half == 0) {
        const cf z1 = cexpk(lre, lim, 1.f), z8 = cexpk(lre, lim, 8.f);
        cf v = {1.f, 0.f};
#pragma unroll
        for (int lo = 0; lo < 16; ++lo) {
            *(u32*)(rowp + KG_B + lo * KG_RS) = pk(v.re, v.im);
            v = cmul(v, z8);
        }
        cf r0 = c, r1 = cmul(c, z128);
#pragma unroll
        for (int j = 0; j < 8; ++j) {
            *(u32*)(rowp + j * KG_RS) = pk(r0.re, -r0.im);
            *(u32*)(rowp + (8 + j) * KG_RS) = pk(r1.re, -r1.im);
            r0 = cmul(r0, z1);
            r1 = cmul(r1, z1);
        }
    } else {
        const cf z2 = cexpk(lre, lim, 2.f), z4 = cexpk(lre, lim, 4.f);
        const cf z6 = cmul(z2, z4);
        cf q = cmul(c, cexpk(lre, lim, 256.f));
#pragma unroll
        for (int hh = 2; hh <= 16; ++hh) {
            if (hh < 4) {
                const int r = 16 + (hh - 2) * 4;
                const cf a = cmul(q, z2), bq = cmul(q, z4), cq = cmul(q, z6);
                *(u32*)(rowp + r * KG_RS) = pk(q.re, -q.im);
                *(u32*)(rowp + (r + 1) * KG_RS) = pk(a.re, -a.im);
                *(u32*)(rowp + (r + 2) * KG_RS) = pk(bq.re, -bq.im);
                *(u32*)(rowp + (r + 3) * KG_RS) = pk(cq.re, -cq.im);
            } else if (hh < 8) {
                const int r = 24 + (hh - 4) * 2;
                const cf bq = cmul(q, z4);
                *(u32*)(rowp + r * KG_RS) = pk(q.re, -q.im);
                *(u32*)(rowp + (r + 1) * KG_RS) = pk(bq.re, -bq.im);
            } else {
                *(u32*)(rowp + (32 + hh - 8) * KG_RS) = pk(q.re, -q.im);
            }
            q = cmul(q, z128);
        }
    }
    __syncthreads();
    if (w < 3) {
        const int g = l >> 4, i16 = l & 15;
        const char* ap = smem + (16 * w + i16) * KG_RS + g * 16;
        const char* bp = smem + KG_B + i16 * KG_RS + g * 16;
        f32x4 acc = {0.f, 0.f, 0.f, 0.f};
#pragma unroll
        for (int ks = 0; ks < 8; ++ks) acc = mfma16(*(const u32x4*)(ap + ks * 64), *(const u32x4*)(bp + ks * 64), acc);
#pragma unroll
        for (int i = 0; i < 4; ++i) {
            const int row = 16 * w + 4 * g + i;
            int e;
            if (row < 16) e = 128 * (row >> 3) + (row & 7);
            else if (row < 24) e = 256 + 128 * ((row - 16) >> 2) + 2 * ((row - 16) & 3);
            else if (row < 32) e = 512 + 128 * ((row - 24) >> 1) + 4 * ((row - 24) & 1);
            else e = 1024 + 128 * (row - 32);
            if (row < 40 || (row == 40 && i16 == 0))
                *(u16*)(smem + KG_T + (e + 8 * i16) * 2) = (u16)(pk(acc[i], acc[i]) & 0xffffu);
        }
    }
    __syncthreads();
    {
        const int a = 255 - t;
        u32 code;
        if (a < 32) code = 0x76543210u;
        else if (a < 64) code = 0x86444200u;
        else if (a < 128) code = 0x88444000u;
        else code = (a & 1) ? 0x88880000u : 0x88800000u;
        const char* tb = smem + KG_T + 16 * a;
        u32 v[8];
#pragma unroll
        for (int e = 0; e < 8; ++e) v[e] = *(const u16*)(tb + 2 * ((code >> (4 * e)) & 15));
        u32x4 o = {v[7] | (v[6] << 16), v[5] | (v[4] << 16), v[3] | (v[2] << 16), v[1] | (v[0] << 16)};
        u32x4* dst = (u32x4*)(KR + (size_t)d * 4128);
        dst[t] = o;
        if (t < 2) dst[256 + t] = u32x4{0u, 0u, 0u, 0u};
    }
}

__global__ void __launch_bounds__(256, 4)
k1_kernel(const u16* __restrict__ x, const u16* __restrict__ wr, const u16* __restrict__ wi, const u16* __restrict__ u,
          const u16* __restrict__ Are, const u16* __restrict__ Aim, const u16* __restrict__ Bre, const u16* __restrict__ Bim,
          const u16* __restrict__ Cre, const u16* __restrict__ Cim, const u16* __restrict__ logdt, char* __restrict__ ws) {
    extern __shared__ __attribute__((aligned(16))) char smem[];
    const int b = blockIdx.x;
    if (b < 128) role_p1(x, ws + OFF_X1, smem, b);
    else if (b < 384) role_tr(wr, wi, u, ws + OFF_WT, ws + OFF_UT, b - 128);
    else role_kgen(Are, Aim, Bre, Bim, Cre, Cim, logdt, ws + OFF_KR, smem, b - 384);
}

constexpr int PA_IMG = 4112;
constexpr int PA_W = 8 * PA_IMG;
constexpr int PA_WS = 1040;
constexpr int PA_TW = PA_W + 32 * PA_WS;
constexpr int PA_C = PA_TW + 3 * 8192;
constexpr int PA_END = PA_C + 12 * 1024;

struct cfrag { u32x4 c[2], s[2], n[2]; };

DI void cmma_AB(f32x16& ore, f32x16& oim, const u32x4* are, const u32x4* aim, const u32x4* bc, const u32x4* bs_re, const u32x4* bs_im) {
#pragma unroll
    for (int s = 0; s < 2; ++s) {
        ore = mfma32(are[s], bc[s], ore);
        oim = mfma32(aim[s], bc[s], oim);
        ore = mfma32(aim[s], bs_re[s], ore);
        oim = mfma32(are[s], bs_im[s], oim);
    }
}
DI void cmma_BA(f32x16& ore, f32x16& oim, const u32x4* bre, const u32x4* bim, const u32x4* ac, const u32x4* as_re, const u32x4* as_im) {
#pragma unroll
    for (int s = 0; s < 2; ++s) {
        ore = mfma32(ac[s], bre[s], ore);
        oim = mfma32(ac[s], bim[s], oim);
        ore = mfma32(as_re[s], bim[s], ore);
        oim = mfma32(as_im[s], bre[s], oim);
    }
}
DI void pack16(u32x4* f, const f32x16& v) {
    f[0] = u32x4{pk(v[0], v[1]), pk(v[2], v[3]), pk(v[4], v[5]), pk(v[6], v[7])};
    f[1] = u32x4{pk(v[8], v[9]), pk(v[10], v[11]), pk(v[12], v[13]), pk(v[14], v[15])};
}

DI void role_pa(const char* __restrict__ X1, const char* __restrict__ WT, char* __restrict__ Y1, char* smem, int cb) {
    const int t = threadIdx.x, w = t >> 6, l = t & 63, lam = l & 31, h = l >> 5;
    u32x4 xv[8], wv[8];
    const int sub = t & 7;
#pragma unroll
    for (int i = 0; i < 8; ++i) xv[i] = *(const u32x4*)(X1 + ((size_t)(i * 32 + (t >> 3)) * 324 + 4 * cb) * 32 + sub * 16);
#pragma unroll
    for (int i = 0; i < 8; ++i) wv[i] = *(const u32x4*)(WT + ((size_t)(i * 32 + (t >> 3)) * 324 + 4 * cb) * 32 + sub * 16);

#pragma unroll
    for (int ff = 0; ff < 3; ++ff) {
        const int f = 3 * w + ff, type = f >> 1, sg = f & 1;
        float v[8];
#pragma unroll
        for (int j = 0; j < 8; ++j) {
            const int slot = 16 * sg + 8 * (j >> 2) + 4 * h + (j & 3);
            int num;
            if (type < 2) num = 2 * slot * lam;
            else if (type < 4) num = slot * (2 * lam + 1);
            else num = (2 * slot + 1) * lam;
            const float a = (float)(num & 63) * (1.0f / 64.0f);
            v[j] = (type & 1) ? sinr(a) : cosr(a);
        }
        *(u32x4*)(smem + PA_C + f * 1024 + l * 16) = u32x4{pk(v[0], v[1]), pk(v[2], v[3]), pk(v[4], v[5]), pk(v[6], v[7])};
    }
#pragma unroll
    for (int qq = 0; qq < 6; ++qq) {
        const int qi = 6 * w + qq, tb = qi >> 3, rp = qi & 7;
        f32x4 o;
#pragma unroll
        for (int e = 0; e < 2; ++e) {
            const int cr = crow(2 * rp + e, h);
            const int num = (tb == 0) ? lam * cr : (tb == 1) ? (32 + cr) * lam : (lam + 32) * cr;
            const float a = (float)num * (1.0f / 2048.0f);
            o[2 * e] = cosr(a);
            o[2 * e + 1] = sinr(a);
        }
        *(f32x4*)(smem + PA_TW + qi * 1024 + l * 16) = o;
    }
#pragma unroll
    for (int i = 0; i < 8; ++i) {
        const int sb = i * 32 + (t >> 3);
        const int s2 = sb >> 3;
        *(u32x4*)(smem + sub * PA_IMG + 128 * s2 + ((16 * (sb & 7)) ^ (((s2 >> 1) & 1) << 6))) = xv[i];
    }
#pragma unroll
    for (int i = 0; i < 8; ++i) {
        const int bk = i * 32 + (t >> 3);
        *(u32x4*)(smem + PA_W + ((bk >> 6) * 8 + sub) * PA_WS + (bk & 63) * 16) = wv[i];
    }
    __syncthreads();

    const bool active = (4 * cb + w) <= 320;
    u32x4 C[2], S[2], NS[2];
    f32x16 y0re, y0im, y1re, y1im;
    if (active) {
#pragma unroll
        for (int s = 0; s < 2; ++s) {
            C[s] = *(const u32x4*)(smem + PA_C + (0 + s) * 1024 + l * 16);
            S[s] = *(const u32x4*)(smem + PA_C + (2 + s) * 1024 + l * 16);
            NS[s] = neg8(S[s]);
        }
        f32x16 tre[2], tim[2];
        const int gp = l >> 4, q = (l & 15) >> 2, p = l & 3;
#pragma unroll
        for (int tau = 0; tau < 2; ++tau) {
            u32x4 are[2], aim[2];
#pragma unroll
            for (int s = 0; s < 2; ++s) {
                u32x2 r[2][2];
#pragma unroll
                for (int tt = 0; tt < 2; ++tt) {
                    const int s2 = 16 * s + 8 * tt + 4 * h + q;
                    const int off = 128 * s2 + ((64 * tau + 32 * (gp & 1) + 8 * p) ^ (((s2 >> 1) & 1) << 6));
                    r[0][tt] = trread(smem + (2 * w) * PA_IMG + off);
                    r[1][tt] = trread(smem + (2 * w + 1) * PA_IMG + off);
                }
                are[s] = u32x4{r[0][0].x, r[0][0].y, r[0][1].x, r[0][1].y};
                aim[s] = u32x4{r[1][0].x, r[1][0].y, r[1][1].x, r[1][1].y};
            }
            tre[tau] = f32x16{};
            tim[tau] = f32x16{};
            cmma_AB(tre[tau], tim[tau], are, aim, C, S, NS);
        }
        f32x16 pre, pim, qre, qim;
#pragma unroll
        for (int rp = 0; rp < 8; ++rp) {
            const f32x4 ta = *(const f32x4*)(smem + PA_TW + rp * 1024 + l * 16);
            const f32x4 tb = *(const f32x4*)(smem + PA_TW + (8 + rp) * 1024 + l * 16);
#pragma unroll
            for (int e = 0; e < 2; ++e) {
                const int r = 2 * rp + e;
                const float ar = tre[0][r] * ta[2 * e] + tim[0][r] * ta[2 * e + 1];
                const float ai = tim[0][r] * ta[2 * e] - tre[0][r] * ta[2 * e + 1];
                const float br = tre[1][r] * tb[2 * e] + tim[1][r] * tb[2 * e + 1];
                const float bi = tim[1][r] * tb[2 * e] - tre[1][r] * tb[2 * e + 1];
                pre[r] = ar + br; pim[r] = ai + bi;
                qre[r] = ar - br; qim[r] = ai - bi;
            }
        }
        u32x4 fre[2], fim[2];
        f32x16 ere = {}, eim = {}, ore = {}, oim = {};
        pack16(fre, pre); pack16(fim, pim);
        cmma_BA(ere, eim, fre, fim, C, S, NS);
        {
            u32x4 Gc[2], Gs[2], Gn[2];
#pragma unroll
            for (int s = 0; s < 2; ++s) {
                Gc[s] = *(const u32x4*)(smem + PA_C + (4 + s) * 1024 + l * 16);
                Gs[s] = *(const u32x4*)(smem + PA_C + (6 + s) * 1024 + l * 16);
                Gn[s] = neg8(Gs[s]);
            }
            pack16(fre, qre); pack16(fim, qim);
            cmma_BA(ore, oim, fre, fim, Gc, Gs, Gn);
        }
#pragma unroll
        for (int e = 0; e < 2; ++e) {
            f32x16& zr = e ? ore : ere;
            f32x16& zi = e ? oim : eim;
#pragma unroll
            for (int hf = 0; hf < 2; ++hf) {
                const u32x4 wre = *(const u32x4*)(smem + PA_W + ((e * 2 + hf) * 8 + 2 * w) * PA_WS + l * 16);
                const u32x4 wim = *(const u32x4*)(smem + PA_W + ((e * 2 + hf) * 8 + 2 * w + 1) * PA_WS + l * 16);
#pragma unroll
                for (int j = 0; j < 8; ++j) {
                    const float a = (j & 1) ? bfhi(wre[j >> 1]) : bflo(wre[j >> 1]);
                    const float bb = (j & 1) ? bfhi(wim[j >> 1]) : bflo(wim[j >> 1]);
                    const int r = 8 * hf + j;
                    const float xr = zr[r], xi = zi[r];
                    zr[r] = xr * a - xi * bb;
                    zi[r] = xr * bb + xi * a;
                }
            }
        }
        f32x16 Ere = {}, Eim = {}, Ore = {}, Oim = {};
        pack16(fre, ere); pack16(fim, eim);
        cmma_AB(Ere, Eim, fre, fim, C, NS, S);
        {
            u32x4 Hc[2], Hs[2], Hn[2];
#pragma unroll
            for (int s = 0; s < 2; ++s) {
                Hc[s] = *(const u32x4*)(smem + PA_C + (8 + s) * 1024 + l * 16);
                Hs[s] = *(const u32x4*)(smem + PA_C + (10 + s) * 1024 + l * 16);
                Hn[s] = neg8(Hs[s]);
            }
            pack16(fre, ore); pack16(fim, oim);
            cmma_AB(Ore, Oim, fre, fim, Hc, Hn, Hs);
        }
        f32x16 v0re, v0im, v1re, v1im;
#pragma unroll
        for (int rp = 0; rp < 8; ++rp) {
            const f32x4 ta = *(const f32x4*)(smem + PA_TW + rp * 1024 + l * 16);
            const f32x4 tb = *(const f32x4*)(smem + PA_TW + (16 + rp) * 1024 + l * 16);
#pragma unroll
            for (int e = 0; e < 2; ++e) {
                const int r = 2 * rp + e;
                const float ar = Ere[r] + Ore[r], ai = Eim[r] + Oim[r];
                const float br = Ere[r] - Ore[r], bi = Eim[r] - Oim[r];
                v0re[r] = ar * ta[2 * e] - ai * ta[2 * e + 1];
                v0im[r] = ai * ta[2 * e] + ar * ta[2 * e + 1];
                v1re[r] = br * tb[2 * e] - bi * tb[2 * e + 1];
                v1im[r] = bi * tb[2 * e] + br * tb[2 * e + 1];
            }
        }
        y0re = f32x16{}; y0im = f32x16{}; y1re = f32x16{}; y1im = f32x16{};
        pack16(fre, v0re); pack16(fim, v0im);
        cmma_AB(y0re, y0im, fre, fim, C, NS, S);
        pack16(fre, v1re); pack16(fim, v1im);
        cmma_AB(y1re, y1im, fre, fim, C, NS, S);
    }
    __syncthreads();
    if (active) {
#pragma unroll
        for (int tau = 0; tau < 2; ++tau)
#pragma unroll
            for (int pl = 0; pl < 2; ++pl) {
                const f32x16& y = tau ? (pl ? y1im : y1re) : (pl ? y0im : y0re);
#pragma unroll
                for (int pr = 0; pr < 2; ++pr) {
                    u32 a0 = pk(y[8 * pr + 0], y[8 * pr + 1]), a1 = pk(y[8 * pr + 2], y[8 * pr + 3]);
                    u32 b0 = pk(y[8 * pr + 4], y[8 * pr + 5]), b1 = pk(y[8 * pr + 6], y[8 * pr + 7]);
                    auto s0 = __builtin_amdgcn_permlane32_swap(a0, b0, false, false);
                    auto s1 = __builtin_amdgcn_permlane32_swap(a1, b1, false, false);
                    const int sb = 8 * lam + 4 * tau + 2 * pr + h;
                    *(u32x4*)(smem + sb * 128 + (((2 * w + pl) ^ (lam & 7)) * 16)) = u32x4{s0[0], s1[0], s0[1], s1[1]};
                }
            }
    }
    __syncthreads();
#pragma unroll
    for (int i = 0; i < 8; ++i) {
        const int sb = i * 32 + (t >> 3);
        *(u32x4*)(Y1 + (size_t)cb * 32768 + sb * 128 + sub * 16) = *(const u32x4*)(smem + sb * 128 + ((sub ^ ((sb >> 3) & 7)) * 16));
    }
}

constexpr int PB_U = 4608;
constexpr int PB_K = 4 * PB_U;
constexpr int PB_K1 = 4160;
constexpr int PB_KS = 8320;
constexpr int PB_ST = PB_K + 4 * PB_KS;
DI void role_pb(const char* __restrict__ UT, const char* __restrict__ KR, const u16* __restrict__ dskip,
                char* __restrict__ YT, char* smem, int chb) {
    const int t = threadIdx.x, w = t >> 6, l = t & 63, g = l >> 4, i16 = l & 15;
    const int ch = 4 * chb + w;
    u32x4 uv[4], kv[4], kt;
    u32 kn[4];
#pragma unroll
    for (int i = 0; i < 4; ++i) uv[i] = *(const u32x4*)(UT + ((size_t)(i * 64 + (t >> 2)) * 640 + 4 * chb + (t & 3)) * 16);
    const char* kr = KR + (size_t)ch * 4128;
#pragma unroll
    for (int i = 0; i < 4; ++i) {
        kv[i] = *(const u32x4*)(kr + (i * 64 + l) * 16);
        kn[i] = *(const u32*)(kr + (i * 64 + l) * 16 + 16);
    }
    kt = u32x4{0u, 0u, 0u, 0u};
    const float dsk = bf2f(dskip[ch]);
    if (t < 128) *(u32x4*)(smem + (t >> 5) * PB_U + (t & 31) * 16) = kt;
    char* kb = smem + PB_K + w * PB_KS;
    if (l < 2) {
        *(u32x4*)(kb + 4096 + l * 16) = kt;
        *(u32x4*)(kb + PB_K1 + 4096 + l * 16) = kt;
    }
#pragma unroll
    for (int i = 0; i < 4; ++i) *(u32x4*)(smem + (t & 3) * PB_U + 512 + (i * 64 + (t >> 2)) * 16) = uv[i];
#pragma unroll
    for (int i = 0; i < 4; ++i) {
        *(u32x4*)(kb + (i * 64 + l) * 16) = kv[i];
        const u32x4 sh = {__builtin_amdgcn_alignbit(kv[i].y, kv[i].x, 16), __builtin_amdgcn_alignbit(kv[i].z, kv[i].y, 16),
                          __builtin_amdgcn_alignbit(kv[i].w, kv[i].z, 16), __builtin_amdgcn_alignbit(kn[i], kv[i].w, 16)};
        *(u32x4*)(kb + PB_K1 + (i * 64 + l) * 16) = sh;
    }
    __syncthreads();

    const int mu = g >> 1, hp = g & 1;
    const int x0 = 2047 - 16 * mu - i16 + 8 * hp;
    const char* ap = kb + ((x0 & 1) ? PB_K1 + (x0 - 1) * 2 : x0 * 2);
    const char* bp = smem + w * PB_U + (256 + 16 * (i16 - mu) + 8 * hp) * 2;
    f32x4 acc[8];
#pragma unroll
    for (int c = 0; c < 8; ++c) acc[c] = f32x4{0.f, 0.f, 0.f, 0.f};
#pragma unroll 1
    for (int r = 0; r < 8; ++r) {
        u32x4 A[8], B[8];
#pragma unroll
        for (int p = 0; p < 8; ++p) {
            const u32* a = (const u32*)(ap - 64 * r - 512 * p);
            A[p] = u32x4{a[0], a[1], a[2], a[3]};
            B[p] = *(const u32x4*)(bp - 64 * r + 512 * p);
        }
#pragma unroll
        for (int p = 0; p < 8; ++p)
#pragma unroll
            for (int q = 0; q + p < 8; ++q) acc[p + q] = mfma16(A[p], B[q], acc[p + q]);
    }
    char* st = smem + PB_ST;
#pragma unroll
    for (int c = 0; c < 8; ++c) {
        const int tt = 256 * c + 16 * i16 + 4 * g;
        const u32x2 uu = *(const u32x2*)(smem + w * PB_U + (256 + tt) * 2);
        float o[4];
#pragma unroll
        for (int i = 0; i < 4; ++i) {
            const float uf = (i & 1) ? bfhi(i < 2 ? uu.x : uu.y) : bflo(i < 2 ? uu.x : uu.y);
            o[i] = rbf(acc[c][i]) + rbf(uf * dsk);
        }
        const int lb = 32 * c + 2 * i16 + (g >> 1);
        const int slot = (((lb & 1) * 4 + w) ^ (i16 & 7));
        *(u32x2*)(st + (lb >> 1) * 128 + slot * 16 + (g & 1) * 8) = u32x2{pk(o[0], o[1]), pk(o[2], o[3])};
    }
    __syncthreads();
#pragma unroll
    for (int i = 0; i < 4; ++i) {
        const int id = t + 256 * i, row = id >> 3, s = id & 7;
        *(u32x4*)(YT + (size_t)chb * 16384 + id * 16) = *(const u32x4*)(st + row * 128 + ((s ^ (row & 7)) * 16));
    }
}

__global__ void __launch_bounds__(256, 1)
k2_kernel(const u16* __restrict__ dskip, char* __restrict__ ws) {
    extern __shared__ __attribute__((aligned(16))) char smem[];
    const int b = blockIdx.x;
    if (b < 81) role_pa(ws + OFF_X1, ws + OFF_WT, ws + OFF_Y1, smem, b);
    else role_pb(ws + OFF_UT, ws + OFF_KR, dskip, ws + OFF_YT, smem, b - 81);
}

DI void role_p3(const char* __restrict__ Y1, u16* __restrict__ out, char* smem, int b) {
    const int t = threadIdx.x, w = t >> 6, l = t & 63, g = l >> 4, i16 = l & 15;
    u32x4 yv[3];
#pragma unroll
    for (int i = 0; i < 3; ++i) {
        const int m = t + 256 * i;
        yv[i] = u32x4{0u, 0u, 0u, 0u};
        if (m < 648) yv[i] = *(const u32x4*)(Y1 + ((size_t)(m >> 3) * 256 + b) * 128 + (m & 7) * 16);
    }
    u32x4 C[2], S[2], nsS[2], sC[2], F[2];
    float twc[8], tws[8];
    {
        const u32 sgn = (g >= 2) ? 0x80008000u : 0u;
#pragma unroll
        for (int tau = 0; tau < 2; ++tau) {
            float c[8], s[8];
#pragma unroll
            for (int j = 0; j < 8; ++j) {
                const int k1 = 8 * g + j, d1 = 16 * tau + i16;
                const float a = (float)((k1 * d1) & 31) * 0.03125f;
                c[j] = cosr(a);
                s[j] = sinr(a);
            }
            C[tau] = u32x4{pk(c[0], c[1]), pk(c[2], c[3]), pk(c[4], c[5]), pk(c[6], c[7])};
            S[tau] = u32x4{pk(s[0], s[1]), pk(s[2], s[3]), pk(s[4], s[5]), pk(s[6], s[7])};
            nsS[tau] = S[tau] ^ (sgn ^ 0x80008000u);
            sC[tau] = C[tau] ^ sgn;
#pragma unroll
            for (int i = 0; i < 4; ++i) {
                const int k2 = 4 * g + i, d1 = 16 * tau + i16;
                const float a = (float)(d1 * k2) * (1.0f / 640.0f);
                twc[4 * tau + i] = cosr(a) * (1.0f / (2048.0f * 640.0f));
                tws[4 * tau + i] = sinr(a) * (1.0f / (2048.0f * 640.0f));
            }
        }
#pragma unroll
        for (int dl = 0; dl < 2; ++dl) {
            float v[8];
#pragma unroll
            for (int j = 0; j < 8; ++j) {
                const int k2 = 4 * g + (j & 3), d2 = 16 * dl + i16;
                const float wgt = (k2 > 10 || d2 >= 20) ? 0.f : ((k2 == 0 || k2 == 10) ? 1.f : 2.f);
                const float a = (float)(k2 * d2) * 0.05f;
                v[j] = (j < 4) ? wgt * cosr(a) : -wgt * sinr(a);
            }
            F[dl] = u32x4{pk(v[0], v[1]), pk(v[2], v[3]), pk(v[4], v[5]), pk(v[6], v[7])};
        }
    }
#pragma unroll
    for (int i = 0; i < 3; ++i) {
        const int m = t + 256 * i;
        if (m < 648) *(u32x4*)(smem + m * 16) = yv[i];
    }
    __syncthreads();
    const int k2 = i16 <= 10 ? i16 : 10;
    u32 dre[8], dim[8];
#pragma unroll
    for (int j = 0; j < 8; ++j) {
        const int k1 = 8 * g + j, k = k2 + 20 * k1;
        const int kk = (g < 2) ? k : 640 - k;
        dre[j] = *(const u32*)(smem + kk * 32 + w * 4);
        dim[j] = *(const u32*)(smem + kk * 32 + 16 + w * 4);
    }
#pragma unroll
    for (int rr = 0; rr < 2; ++rr) {
        u32x4 are, aim;
        if (rr == 0) {
            are = u32x4{permlo(dre[0], dre[1]), permlo(dre[2], dre[3]), permlo(dre[4], dre[5]), permlo(dre[6], dre[7])};
            aim = u32x4{permlo(dim[0], dim[1]), permlo(dim[2], dim[3]), permlo(dim[4], dim[5]), permlo(dim[6], dim[7])};
        } else {
            are = u32x4{permhi(dre[0], dre[1]), permhi(dre[2], dre[3]), permhi(dre[4], dre[5]), permhi(dre[6], dre[7])};
            aim = u32x4{permhi(dim[0], dim[1]), permhi(dim[2], dim[3]), permhi(dim[4], dim[5]), permhi(dim[6], dim[7])};
        }
        u32x4 va[2];
#pragma unroll
        for (int tau = 0; tau < 2; ++tau) {
            const f32x4 zero = {0.f, 0.f, 0.f, 0.f};
            const f32x4 vre = mfma16(aim, nsS[tau], mfma16(are, C[tau], zero));
            const f32x4 vim = mfma16(aim, sC[tau], mfma16(are, S[tau], zero));
            float pr[4], pi[4];
#pragma unroll
            for (int i = 0; i < 4; ++i) {
                pr[i] = vre[i] * twc[4 * tau + i] - vim[i] * tws[4 * tau + i];
                pi[i] = vim[i] * twc[4 * tau + i] + vre[i] * tws[4 * tau + i];
            }
            va[tau] = u32x4{pk(pr[0], pr[1]), pk(pr[2], pr[3]), pk(pi[0], pi[1]), pk(pi[2], pi[3])};
        }
        u16* orow = out + (size_t)(8 * b + 2 * w + rr) * 1280;
#pragma unroll
        for (int dl = 0; dl < 2; ++dl) {
            const f32x4 zero = {0.f, 0.f, 0.f, 0.f};
            const f32x4 y0 = mfma16(va[0], F[dl], zero);
            const f32x4 y1 = mfma16(va[1], F[dl], zero);
            u32 a0 = pk(y0[0], y0[1]), a1 = pk(y0[2], y0[3]);
            u32 b0 = pk(y1[0], y1[1]), b1 = pk(y1[2], y1[3]);
            auto s0 = __builtin_amdgcn_permlane16_swap(a0, b0, false, false);
            auto s1 = __builtin_amdgcn_permlane16_swap(a1, b1, false, false);
            const int d2 = 16 * dl + i16;
            if (d2 < 20) *(u32x4*)(orow + 32 * d2 + (g & 1) * 16 + (g >> 1) * 8) = u32x4{s0[0], s1[0], s0[1], s1[1]};
        }
    }
}

DI void role_tb(const char* __restrict__ YT, u16* __restrict__ out, int b) {
    const int t = threadIdx.x;
    if (t < 160) {
        const u32x4* src = (const u32x4*)(YT + ((size_t)t * 256 + b) * 64);
        const u32x4 c0 = src[0], c1 = src[1], c2 = src[2], c3 = src[3];
        u16* o = out + (size_t)(8 * b) * 1280 + 640 + 4 * t;
#pragma unroll
        for (int j = 0; j < 4; ++j) {
            *(u32x2*)(o + (2 * j) * 1280) = u32x2{permlo(c0[j], c1[j]), permlo(c2[j], c3[j])};
            *(u32x2*)(o + (2 * j + 1) * 1280) = u32x2{permhi(c0[j], c1[j]), permhi(c2[j], c3[j])};
        }
    }
}

__global__ void __launch_bounds__(256, 2)
k3_kernel(const char* __restrict__ ws, u16* __restrict__ out) {
    extern __shared__ __attribute__((aligned(16))) char smem[];
    const int b = blockIdx.x;
    if (b < 256) role_p3(ws + OFF_Y1, out, smem, b);
    else role_tb(ws + OFF_YT, out, b - 256);
}

extern "C" void kernel_launch(void* const* d_in, const int* in_sizes, int n_in,
                              void* d_out, int out_size, void* d_ws, size_t ws_size,
                              hipStream_t stream) {
    const u16* x = (const u16*)d_in[0];
    const u16* wr = (const u16*)d_in[1];
    const u16* wi = (const u16*)d_in[2];
    const u16* u = (const u16*)d_in[3];
    const u16* Are = (const u16*)d_in[4];
    const u16* Aim = (const u16*)d_in[5];
    const u16* Bre = (const u16*)d_in[6];
    const u16* Bim = (const u16*)d_in[7];
    const u16* Cre = (const u16*)d_in[8];
    const u16* Cim = (const u16*)d_in[9];
    const u16* logdt = (const u16*)d_in[10];
    const u16* dskip = (const u16*)d_in[11];
    char* ws = (char*)d_ws;
    constexpr int LDS1 = 34560, LDS2 = PA_END, LDS3 = 648 * 16;
    (void)hipFuncSetAttribute((const void*)k2_kernel, hipFuncAttributeMaxDynamicSharedMemorySize, LDS2);
    k1_kernel<<<1024, 256, LDS1, stream>>>(x, wr, wi, u, Are, Aim, Bre, Bim, Cre, Cim, logdt, ws);
    k2_kernel<<<241, 256, LDS2, stream>>>(dskip, ws);
    k3_kernel<<<512, 256, LDS3, stream>>>(ws, (u16*)d_out);
}
```

```cpp
#include <hip/hip_runtime.h>
#include <hip/hip_bf16.h>

typedef unsigned short u16;
typedef unsigned int u32;
typedef __attribute__((ext_vector_type(2))) __bf16 bf16x2;
typedef __attribute__((ext_vector_type(8))) __bf16 bf16x8;
typedef __attribute__((ext_vector_type(2))) float f32x2;
typedef __attribute__((ext_vector_type(4))) float f32x4;
typedef __attribute__((ext_vector_type(4))) u32 u32x4;
typedef __attribute__((ext_vector_type(2))) u32 u32x2;
typedef __attribute__((ext_vector_type(4))) short s16x4;
typedef __amdgpu_buffer_rsrc_t rsrc_t;
#define LAS __attribute__((address_space(3)))
#define GAS __attribute__((address_space(1)))
#define DI __device__ __forceinline__
#define SC1 16

constexpr u32 X1_ROW = 10368;
constexpr u32 OFF_X1 = 0;
constexpr u32 Y1_BLK = 28672;
constexpr u32 OFF_Y1 = 256 * X1_ROW;
constexpr u32 OFF_SYNC = OFF_Y1 + 96 * Y1_BLK;
constexpr u32 WS_USED = OFF_SYNC + 4096;
constexpr u32 SENT = 0xFFFFFFFFu;
constexpr u32 SPIN_MAX = 1u << 16;

constexpr int L_X = 0;
constexpr int L_S = 10752;
constexpr int L_W = 21504;
constexpr int L_C = L_W + 28672;
constexpr int L_U = 21504;
constexpr int PB_U = 4608;
constexpr int L_K = L_U + 4 * PB_U;
constexpr int PB_K1 = 4160, PB_KS = 8320;
constexpr int L_G = L_K + 4 * PB_KS;
constexpr int KG_RS = 528, KG_B = 41 * KG_RS, KG_T = 57 * KG_RS, KG_SZ = 34208;
constexpr int LDS_BYTES = L_G + 2 * KG_SZ;

#define STAMP_DECL
#define STAMP()
#define STAMP_OUT(ws)

DI u32 pk(float a, float b) {
    f32x2 v = {a, b};
    return __builtin_bit_cast(u32, __builtin_convertvector(v, bf16x2));
}
DI u32x4 pk8(const float* v) { return u32x4{pk(v[0], v[1]), pk(v[2], v[3]), pk(v[4], v[5]), pk(v[6], v[7])}; }
DI float bflo(u32 v) { return __uint_as_float(v << 16); }
DI float bfhi(u32 v) { return __uint_as_float(v & 0xffff0000u); }
DI float bf2f(u32 v) { return __uint_as_float(v << 16); }
DI float rbf(float a) { return bflo(pk(a, a)); }
DI float cosr(float rev) { return __builtin_amdgcn_cosf(rev); }
DI float sinr(float rev) { return __builtin_amdgcn_sinf(rev); }
DI f32x4 mfma16(u32x4 a, u32x4 b, f32x4 c) {
    return __builtin_amdgcn_mfma_f32_16x16x32_bf16(__builtin_bit_cast(bf16x8, a), __builtin_bit_cast(bf16x8, b), c, 0, 0, 0);
}
DI f32x4 mfma16k16(u32x2 a, u32x2 b, f32x4 c) {
    return __builtin_amdgcn_mfma_f32_16x16x16bf16_1k(__builtin_bit_cast(s16x4, a), __builtin_bit_cast(s16x4, b), c, 0, 0, 0);
}
DI u32x2 trread(const char* p) {
    return __builtin_bit_cast(u32x2, __builtin_amdgcn_ds_read_tr16_b64_v4i16((LAS s16x4*)(p)));
}
DI u32 permlo(u32 a, u32 b) { return __builtin_amdgcn_perm(b, a, 0x05040100u); }
DI u32 permhi(u32 a, u32 b) { return __builtin_amdgcn_perm(b, a, 0x07060302u); }
DI rsrc_t mkrs(const void* p, u32 bytes) { return __builtin_amdgcn_make_buffer_rsrc((void*)p, 0, bytes, 0x00020000); }
DI u32x4 ld16(rsrc_t r, u32 off, int aux) { return aux ? __builtin_amdgcn_raw_buffer_load_b128(r, off, 0, SC1) : __builtin_amdgcn_raw_buffer_load_b128(r, off, 0, 0); }
DI u32x2 ld8(rsrc_t r, u32 off, int aux) { return aux ? __builtin_amdgcn_raw_buffer_load_b64(r, off, 0, SC1) : __builtin_amdgcn_raw_buffer_load_b64(r, off, 0, 0); }
DI u32 ld2(rsrc_t r, u32 off) { return __builtin_amdgcn_raw_buffer_load_b16(r, off, 0, 0); }
DI void st16sc(rsrc_t r, u32 off, u32x4 v) { __builtin_amdgcn_raw_buffer_store_b128(v, r, off, 0, SC1); }
DI u32x4 sane(u32x4 v) { return u32x4{min(v.x, SENT - 1), min(v.y, SENT - 1), min(v.z, SENT - 1), min(v.w, SENT - 1)}; }
DI bool fresh4(u32x4 v) { return max(max(v.x, v.y), max(v.z, v.w)) != SENT; }
DI bool fresh2(u32x2 v) { return max(v.x, v.y) != SENT; }
DI void fence() { asm volatile("" ::: "memory"); }
DI void drain() { asm volatile("s_waitcnt vmcnt(0)" ::: "memory"); }
DI void tr8x4(const u32x2* v, u32x4* o) {
    o[0] = u32x4{permlo(v[0].x, v[1].x), permlo(v[2].x, v[3].x), permlo(v[4].x, v[5].x), permlo(v[6].x, v[7].x)};
    o[1] = u32x4{permhi(v[0].x, v[1].x), permhi(v[2].x, v[3].x), permhi(v[4].x, v[5].x), permhi(v[6].x, v[7].x)};
    o[2] = u32x4{permlo(v[0].y, v[1].y), permlo(v[2].y, v[3].y), permlo(v[4].y, v[5].y), permlo(v[6].y, v[7].y)};
    o[3] = u32x4{permhi(v[0].y, v[1].y), permhi(v[2].y, v[3].y), permhi(v[4].y, v[5].y), permhi(v[6].y, v[7].y)};
}
DI void wait_filled(rsrc_t rws, u32 tag, int w, int l) {
    if (w == 0) {
        for (u32 spins = 0; spins < SPIN_MAX; ++spins) {
            fence();
            const u32x4 f = ld16(rws, OFF_SYNC + 1024 + l * 16, 1);
            if (__all(f.x == tag && f.y == tag && f.z == tag && f.w == tag)) break;
            __builtin_amdgcn_s_sleep(2);
        }
    }
    __syncthreads();
}

struct cf { float re, im; };
DI cf cmul(cf a, cf b) { return cf{a.re * b.re - a.im * b.im, a.re * b.im + a.im * b.re}; }
DI cf cexpk(float lre, float lim_rev, float k) {
    const float m = __builtin_amdgcn_exp2f(k * lre * 1.44269504088896341f);
    const float a = __builtin_amdgcn_fractf(k * lim_rev);
    return cf{m * cosr(a), m * sinr(a)};
}

struct Args {
    const u16 *x, *wr, *wi, *u, *Are, *Aim, *Bre, *Bim, *Cre, *Cim, *logdt, *dskip;
    char* ws;
    u16* out;
};

__global__ void __launch_bounds__(512, 2) fused_kernel(Args P) {
    extern __shared__ __attribute__((aligned(16))) char smem[];
    const int t = threadIdx.x, l = t & 63, g = l >> 4, i16 = l & 15;
    const int w = __builtin_amdgcn_readfirstlane(t >> 6);
    const int b = blockIdx.x, xcd = b & 7, jx = b >> 3;
    const bool isB = jx < 20;
    const int chb = 20 * xcd + jx;
    const int ka = xcd, cg = jx - 20;
    const int colbase = min(28 * cg, 296);
    STAMP_DECL STAMP();

    const rsrc_t rws = mkrs(P.ws, WS_USED);
    const u32 tag = __hip_atomic_load((GAS u32*)(P.ws + OFF_SYNC), __ATOMIC_RELAXED, __HIP_MEMORY_SCOPE_AGENT) + 1u;

    u32x4 xv[3];
    u32x2 gv[16];
    u32 pv[12];
    if (w < 4) {
        const u32x4 s4 = {SENT, SENT, SENT, SENT};
#pragma unroll
        for (int i = 0; i < 3; ++i) {
            st16sc(rws, OFF_X1 + b * X1_ROW + min(t + 256 * i, 647) * 16, s4);
            st16sc(rws, OFF_Y1 + b * 10752 + min(t + 256 * i, 671) * 16, s4);
        }
        const rsrc_t rx = mkrs(P.x, 2048 * 1280);
#pragma unroll
        for (int i = 0; i < 3; ++i) {
            const int ci = min(l + 64 * i, 159), row = ci >= 80;
            xv[i] = ld16(rx, (b + 256 * (2 * w + row)) * 1280 + (ci - 80 * row) * 16, 0);
        }
    } else if (isB) {
        const rsrc_t ru = mkrs(P.u, 2048 * 1280);
        const int tt = t - 256;
#pragma unroll
        for (int e = 0; e < 8; ++e) gv[e] = ld8(ru, (8 * tt + e) * 1280 + chb * 8, 0);
    } else {
        const int pl = (w - 4) >> 1;
        const rsrc_t rw = mkrs(pl ? P.wi : P.wr, 2048 * 642);
#pragma unroll
        for (int it = 0; it < 2; ++it) {
            const int task = min((t & 127) + 128 * it, 223), quad = task % 7, r = task / 7;
            const int k1 = r & 15, oc = r >> 4;
            const int cs = min(colbase + 4 * quad, 317);
#pragma unroll
            for (int e = 0; e < 8; ++e) {
                const int sp = ka + 8 * (k1 + 16 * (8 * oc + e));
                gv[8 * it + e] = ld8(rw, (sp * 321 + cs) * 2, 0);
            }
        }
    }
    if (isB) {
        const u32 nb = 640 * 128 * 2;
        const rsrc_t r0 = mkrs(P.Are, nb), r1 = mkrs(P.Aim, nb), r2 = mkrs(P.Bre, nb), r3 = mkrs(P.Bim, nb), r4 = mkrs(P.Cre, nb), r5 = mkrs(P.Cim, nb);
#pragma unroll
        for (int p = 0; p < 2; ++p) {
            const u32 off = ((4 * chb + 2 * p + (t >> 8)) * 128 + (t & 127)) * 2;
            pv[6 * p + 0] = ld2(r0, off); pv[6 * p + 1] = ld2(r1, off); pv[6 * p + 2] = ld2(r2, off);
            pv[6 * p + 3] = ld2(r3, off); pv[6 * p + 4] = ld2(r4, off); pv[6 * p + 5] = ld2(r5, off);
        }
    }
    fence();
    if (t >= 256 && t < 256 + 30) *(u32x4*)(smem + L_S + 321 * 32 + (t - 256) * 16) = u32x4{0u, 0u, 0u, 0u};

    if (w < 4) {
        u32x4 f20c, f20s, c32, s32;
        float twc[8], tws[8];
        {
            float c[8], s[8];
#pragma unroll
            for (int j = 0; j < 8; ++j) {
                const int d2 = (j < 4) ? 4 * g + j : 16 + 4 * g + j - 4;
                const bool ok = (d2 < 20) && (i16 <= 10);
                const float a = (float)d2 * (float)i16 * 0.05f;
                c[j] = ok ? cosr(a) : 0.f;
                s[j] = ok ? -sinr(a) : 0.f;
            }
            f20c = pk8(c); f20s = pk8(s);
#pragma unroll
            for (int j = 0; j < 8; ++j) {
                const int d1 = (j < 4) ? 4 * g + j : 16 + 4 * g + j - 4;
                const float pr = (float)d1 * (float)i16;
                c[j] = cosr(pr * 0.03125f);
                s[j] = sinr(pr * 0.03125f);
                twc[j] = cosr(pr * (1.0f / 640.0f));
                tws[j] = sinr(pr * (1.0f / 640.0f));
            }
            c32 = pk8(c); s32 = pk8(s);
        }
        const u32x4 ns32 = s32 ^ 0x80008000u, c32b = c32 ^ 0x80000000u, s32b = s32 ^ 0x80000000u, nc32b = c32b ^ 0x80008000u;
        char* xs = smem + L_X + w * 2560;
#pragma unroll
        for (int i = 0; i < 3; ++i) {
            const int ci = min(l + 64 * i, 159), c = ci >= 80 ? ci - 80 : ci;
            *(u32x4*)(xs + ((ci * 16) ^ (((c >> 4) & 1) << 5))) = xv[i];
        }
        const int q = i16 >> 2, p = i16 & 3;
        const int d2a = 4 * g + q, d2b = (g == 0) ? 16 + q : d2a;
        const int offa = 64 * d2a + 8 * p, swa = ((d2a >> 2) & 1) << 5;
        const int offb = 64 * d2b + 8 * p, swb = ((d2b >> 2) & 1) << 5;
        f32x4 z[2][4];
#pragma unroll
        for (int rr = 0; rr < 2; ++rr) {
            const char* xr = xs + rr * 1280;
            f32x4 tre[2], tim[2];
            const f32x4 zero = {0.f, 0.f, 0.f, 0.f};
#pragma unroll
            for (int tau = 0; tau < 2; ++tau) {
                const u32x2 a0 = trread(xr + offa + ((32 * tau) ^ swa));
                const u32x2 a1 = trread(xr + offb + ((32 * tau) ^ swb));
                const u32x4 a = {a0.x, a0.y, a1.x, a1.y};
                tre[tau] = mfma16(a, f20c, zero);
                tim[tau] = mfma16(a, f20s, zero);
            }
            float pr[8], pi[8];
#pragma unroll
            for (int j = 0; j < 8; ++j) {
                const float a = tre[j >> 2][j & 3], bb = tim[j >> 2][j & 3];
                pr[j] = a * twc[j] + bb * tws[j];
                pi[j] = bb * twc[j] - a * tws[j];
            }
            const u32x4 bre = pk8(pr), bim = pk8(pi);
            z[rr][0] = mfma16(s32, bim, mfma16(c32, bre, zero));
            z[rr][1] = mfma16(ns32, bre, mfma16(c32, bim, zero));
            z[rr][2] = mfma16(s32b, bim, mfma16(c32b, bre, zero));
            z[rr][3] = mfma16(s32b, bre, mfma16(nc32b, bim, zero));
        }
        char* st = smem + L_S + w * 4;
        if (i16 <= 10) {
#pragma unroll
            for (int i = 0; i < 4; ++i) {
                const int k = i16 + 20 * (4 * g + i);
                *(u32*)(st + k * 32) = pk(z[0][0][i], z[1][0][i]);
                *(u32*)(st + k * 32 + 16) = pk(z[0][1][i], z[1][1][i]);
            }
#pragma unroll
            for (int i = 0; i < 4; ++i) {
                const int k = 320 - i16 - 20 * (4 * g + i);
                const bool ok = (i16 >= 1 && i16 <= 9) || (i16 == 0 && g == 0 && i == 0);
                if (ok) {
                    *(u32*)(st + k * 32) = pk(z[0][2][i], z[1][2][i]);
                    *(u32*)(st + k * 32 + 16) = pk(z[0][3][i], z[1][3][i]);
                }
            }
        }
        drain();
    }
    STAMP();
    __syncthreads();
    if (t == 0) __hip_atomic_store((GAS u32*)(P.ws + OFF_SYNC + 1024) + b, tag, __ATOMIC_RELAXED, __HIP_MEMORY_SCOPE_AGENT);
    {
        float f[4];
        const int kap = i16 >> 1, plo = i16 & 1, pl = g >> 1;
#pragma unroll
        for (int j = 0; j < 4; ++j) {
            const int a = 4 * (g & 1) + j;
            const float th = (float)kap * (float)(256 * a + b) * (1.0f / 2048.0f) - ((pl != plo) ? 0.25f : 0.f);
            const float v = cosr(th);
            f[j] = (plo == 1 && pl == 0) ? -v : v;
        }
        const u32x2 fb = {pk(f[0], f[1]), pk(f[2], f[3])};
#pragma unroll
        for (int it = 0; it < 3; ++it) {
            const int T = w + 8 * it;
            if (T < 21) {
                const u32x2 a = *(const u32x2*)(smem + L_S + (16 * T + i16) * 32 + g * 8);
                const f32x4 d = mfma16k16(a, fb, f32x4{0.f, 0.f, 0.f, 0.f});
                if (T < 20 || g == 0) *(u32x2*)(smem + L_X + i16 * 648 + (16 * T + 4 * g) * 2) = u32x2{pk(d[0], d[1]), pk(d[2], d[3])};
            }
        }
    }
    __syncthreads();
#pragma unroll
    for (int i = 0; i < 2; ++i) {
        const int m = min(t + 512 * i, 647);
        st16sc(rws, OFF_X1 + b * X1_ROW + m * 16, sane(*(const u32x4*)(smem + L_X + m * 16)));
    }
    STAMP();

    if (!isB) {
        if (w >= 4) {
#pragma unroll
            for (int it = 0; it < 2; ++it) {
                const int pl = (w - 4) >> 1;
                const int task = min((t & 127) + 128 * it, 223), quad = task % 7, r = task / 7;
                const int k1 = r & 15, oc = r >> 4;
                if (colbase + 4 * quad > 317) {
#pragma unroll
                    for (int e = 0; e < 8; ++e) gv[8 * it + e] = u32x2{gv[8 * it + e].y >> 16, 0u};
                }
                u32x4 o[4];
                tr8x4(&gv[8 * it], o);
#pragma unroll
                for (int c = 0; c < 4; ++c) {
                    const int m = (4 * quad + c) * 2 + pl;
                    *(u32x4*)(smem + L_W + m * 512 + (((k1 * 2 + oc) ^ ((m >> 3) & 7)) * 16)) = o[c];
                }
            }
        }
        u32x4 f1re, f1im, f2re, f2im;
        float twc[4], tws[4];
        {
            float c[8], s[8];
#pragma unroll
            for (int j = 0; j < 8; ++j) {
                const float a = (float)(8 * (g & 1) + j) * (float)i16 * 0.0625f;
                const float co = cosr(a), si = sinr(a);
                c[j] = (g < 2) ? co : si;
                s[j] = (g < 2) ? -si : co;
            }
            f1re = pk8(c); f1im = pk8(s);
#pragma unroll
            for (int j = 0; j < 4; ++j) {
                const float pr = (float)(4 * g + j) * (float)i16;
                const float co = cosr(pr * 0.0625f), si = sinr(pr * 0.0625f);
                c[j] = co; c[4 + j] = si;
                s[j] = -si; s[4 + j] = co;
                twc[j] = cosr(pr * (1.0f / 256.0f));
                tws[j] = sinr(pr * (1.0f / 256.0f));
            }
            f2re = pk8(c); f2im = pk8(s);
        }
        const u32x4 ire = {f2re.x, f2re.y, f2re.z ^ 0x80008000u, f2re.w ^ 0x80008000u};
        const u32x4 iim = {f2im.x ^ 0x80008000u, f2im.y ^ 0x80008000u, f2im.z, f2im.w};
        wait_filled(rws, tag, w, l);
        STAMP();
        {
            const int task = min(t, 447), quad = task % 7, r = task / 7, pl = r >> 5, oc = r & 31;
            const u32 base = OFF_X1 + (8 * oc) * X1_ROW + (ka * 2 + pl) * 648 + (colbase + 4 * quad) * 2;
            u32x2 v[8];
            for (u32 spins = 0; spins < SPIN_MAX; ++spins) {
                fence();
                bool ok = true;
#pragma unroll
                for (int e = 0; e < 8; ++e) v[e] = ld8(rws, base + e * X1_ROW, 1);
#pragma unroll
                for (int e = 0; e < 8; ++e) ok &= fresh2(v[e]);
                if (__all(ok)) break;
                __builtin_amdgcn_s_sleep(1);
            }
            u32x4 o[4];
            tr8x4(v, o);
#pragma unroll
            for (int c = 0; c < 4; ++c) {
                const int m = (4 * quad + c) * 2 + pl;
                *(u32x4*)(smem + L_C + m * 512 + ((oc ^ ((m >> 3) & 7)) * 16)) = o[c];
            }
        }
        STAMP();
        __syncthreads();
        {
            const int q = i16 >> 2, p = i16 & 3;
#pragma unroll 1
            for (int col = w; col < 28; col += 8) {
                const f32x4 zero = {0.f, 0.f, 0.f, 0.f};
                const int mi = col * 2 + (g >> 1), swi = (mi >> 3) & 7;
                const char* ib = smem + L_C + mi * 512 + (p & 1) * 8;
                const u32x2 a0 = trread(ib + (((2 * (8 * (g & 1) + q) + (p >> 1)) ^ swi) * 16));
                const u32x2 a1 = trread(ib + (((2 * (8 * (g & 1) + 4 + q) + (p >> 1)) ^ swi) * 16));
                const u32x4 a = {a0.x, a0.y, a1.x, a1.y};
                const int m0 = col * 2, sw = (m0 >> 3) & 7;
                const int woff = (((i16 * 2 + (g >> 1)) ^ sw) * 16) + (g & 1) * 8;
                const u32x2 wre = *(const u32x2*)(smem + L_W + m0 * 512 + woff);
                const u32x2 wim = *(const u32x2*)(smem + L_W + (m0 + 1) * 512 + woff);
                f32x4 re = mfma16(a, f1re, zero), im = mfma16(a, f1im, zero);
                float x[8];
#pragma unroll
                for (int i = 0; i < 4; ++i) {
                    x[i] = re[i] * twc[i] + im[i] * tws[i];
                    x[4 + i] = im[i] * twc[i] - re[i] * tws[i];
                }
                u32x4 d = pk8(x);
                re = mfma16(f2re, d, zero); im = mfma16(f2im, d, zero);
#pragma unroll
                for (int i = 0; i < 4; ++i) {
                    const float a_ = (i & 1) ? bfhi(wre[i >> 1]) : bflo(wre[i >> 1]);
                    const float b_ = (i & 1) ? bfhi(wim[i >> 1]) : bflo(wim[i >> 1]);
                    x[i] = re[i] * a_ - im[i] * b_;
                    x[4 + i] = re[i] * b_ + im[i] * a_;
                }
                d = pk8(x);
                re = mfma16(d, ire, zero); im = mfma16(d, iim, zero);
#pragma unroll
                for (int i = 0; i < 4; ++i) {
                    x[i] = re[i] * twc[i] - im[i] * tws[i];
                    x[4 + i] = im[i] * twc[i] + re[i] * tws[i];
                }
                d = pk8(x);
                re = mfma16(d, ire, zero); im = mfma16(d, iim, zero);
                const int ooff = (((2 * i16 + (g >> 1)) ^ sw) * 16) + (g & 1) * 8;
                *(u32x2*)(smem + L_C + m0 * 512 + ooff) = u32x2{pk(re[0], re[1]), pk(re[2], re[3])};
                *(u32x2*)(smem + L_C + (m0 + 1) * 512 + ooff) = u32x2{pk(im[0], im[1]), pk(im[2], im[3])};
            }
        }
        STAMP();
        __syncthreads();
        {
            const int q = i16 >> 2, p = i16 & 3;
#pragma unroll
            for (int it = 0; it < 7; ++it) {
                const int cmb = (w * 7 + it) * 4 + g;
                const int quad = cmb % 7, r = cmb / 7, pl = r & 1, G = r >> 1;
                const int m = (4 * quad + q) * 2 + pl;
                const u32x2 v = trread(smem + L_C + m * 512 + (((2 * G + (p >> 1)) ^ ((m >> 3) & 7)) * 16) + (p & 1) * 8);
                *(u32x2*)(smem + L_W + (16 * G + i16) * 112 + pl * 56 + quad * 8) = v;
            }
        }
        __syncthreads();
#pragma unroll
        for (int i = 0; i < 4; ++i) {
            const int m = min(t + 512 * i, 1791);
            st16sc(rws, OFF_Y1 + (ka * 12 + cg) * Y1_BLK + m * 16, sane(*(const u32x4*)(smem + L_W + m * 16)));
        }
        STAMP();
    } else {
        if (t < 128) *(u32x4*)(smem + L_U + (t >> 5) * PB_U + (t & 31) * 16) = u32x4{0u, 0u, 0u, 0u};
        else if (t < 144) {
            const int v = t - 128;
            *(u32x4*)(smem + L_K + (v >> 2) * PB_KS + ((v >> 1) & 1) * PB_K1 + 4096 + (v & 1) * 16) = u32x4{0u, 0u, 0u, 0u};
        }
        const rsrc_t rld = mkrs(P.logdt, 1280);
#pragma unroll
        for (int p = 0; p < 2; ++p) {
            const int chn = w >> 2, half = (w >> 1) & 1, n = t & 127, chl = 2 * p + chn;
            char* sc = smem + L_G + chn * KG_SZ;
            const float are = bf2f(pv[6 * p + 0]), aim = bf2f(pv[6 * p + 1]), bre = bf2f(pv[6 * p + 2]), bim = bf2f(pv[6 * p + 3]);
            const float cre = bf2f(pv[6 * p + 4]), cim = bf2f(pv[6 * p + 5]);
            const float dt = rbf(expf(bf2f(ld2(rld, (4 * chb + chl) * 2))));
            const float lre = dt * are, lim = dt * aim * 0.15915494309189535f;
            const float dbr = dt * bre, dbi = dt * bim;
            const cf c = {cre * dbr - cim * dbi, cre * dbi + cim * dbr};
            char* rowp = sc + n * 4;
            const cf z128 = cexpk(lre, lim, 128.f);
            if (half == 0) {
                const cf z1 = cexpk(lre, lim, 1.f), z8 = cexpk(lre, lim, 8.f);
                cf v = {1.f, 0.f};
                cf r0 = c, r1 = cmul(c, z128);
#pragma unroll
                for (int j = 0; j < 8; ++j) {
                    *(u32*)(rowp + KG_B + (2 * j) * KG_RS) = pk(v.re, v.im);
                    v = cmul(v, z8);
                    *(u32*)(rowp + KG_B + (2 * j + 1) * KG_RS) = pk(v.re, v.im);
                    v = cmul(v, z8);
                    *(u32*)(rowp + j * KG_RS) = pk(r0.re, -r0.im);
                    *(u32*)(rowp + (8 + j) * KG_RS) = pk(r1.re, -r1.im);
                    r0 = cmul(r0, z1);
                    r1 = cmul(r1, z1);
                }
            } else {
                const cf z2 = cexpk(lre, lim, 2.f), z4 = cexpk(lre, lim, 4.f);
                const cf z6 = cmul(z2, z4);
                cf q = cmul(c, cexpk(lre, lim, 256.f));
#pragma unroll
                for (int hh = 2; hh <= 16; ++hh) {
                    if (hh < 4) {
                        const int r = 16 + (hh - 2) * 4;
                        const cf a = cmul(q, z2), bq = cmul(q, z4), cq = cmul(q, z6);
                        *(u32*)(rowp + r * KG_RS) = pk(q.re, -q.im);
                        *(u32*)(rowp + (r + 1) * KG_RS) = pk(a.re, -a.im);
                        *(u32*)(rowp + (r + 2) * KG_RS) = pk(bq.re, -bq.im);
                        *(u32*)(rowp + (r + 3) * KG_RS) = pk(cq.re, -cq.im);
                    } else if (hh < 8) {
                        const int r = 24 + (hh - 4) * 2;
                        const cf bq = cmul(q, z4);
                        *(u32*)(rowp + r * KG_RS) = pk(q.re, -q.im);
                        *(u32*)(rowp + (r + 1) * KG_RS) = pk(bq.re, -bq.im);
                    } else {
                        *(u32*)(rowp + (32 + hh - 8) * KG_RS) = pk(q.re, -q.im);
                    }
                    q = cmul(q, z128);
                }
            }
            __syncthreads();
            const int wq = w & 3;
            if (wq < 3) {
                const char* ap = sc + (16 * wq + i16) * KG_RS + g * 16;
                const char* bp = sc + KG_B + i16 * KG_RS + g * 16;
                f32x4 acc = {0.f, 0.f, 0.f, 0.f};
#pragma unroll
                for (int ks = 0; ks < 8; ++ks) acc = mfma16(*(const u32x4*)(ap + ks * 64), *(const u32x4*)(bp + ks * 64), acc);
#pragma unroll
                for (int i = 0; i < 4; ++i) {
                    const int row = 16 * wq + 4 * g + i;
                    int e;
                    if (row < 16) e = 128 * (row >> 3) + (row & 7);
                    else if (row < 24) e = 256 + 128 * ((row - 16) >> 2) + 2 * ((row - 16) & 3);
                    else if (row < 32) e = 512 + 128 * ((row - 24) >> 1) + 4 * ((row - 24) & 1);
                    else e = 1024 + 128 * (row - 32);
                    if (row < 40 || (row == 40 && i16 == 0))
                        *(u16*)(sc + KG_T + (e + 8 * i16) * 2) = (u16)(pk(acc[i], acc[i]) & 0xffffu);
                }
            }
            __syncthreads();
            {
                const int tt = t & 255, a = 255 - tt, am = a - 1;
                u32 code;
                if (a < 32) code = 0x76543210u;
                else if (a < 64) code = 0x86444200u;
                else if (a < 128) code = 0x88444000u;
                else code = (a & 1) ? 0x88880000u : 0x88800000u;
                const int lastoff = (am < 32) ? 7 : 8;
                const char* tb = sc + KG_T + 16 * a;
                u32 v[8];
#pragma unroll
                for (int e = 0; e < 8; ++e) v[e] = *(const u16*)(tb + 2 * ((code >> (4 * e)) & 15));
                const u32 vm = (a == 0) ? 0u : (u32) * (const u16*)(tb - 16 + 2 * lastoff);
                char* kb = smem + L_K + chl * PB_KS + tt * 16;
                *(u32x4*)(kb) = u32x4{v[7] | (v[6] << 16), v[5] | (v[4] << 16), v[3] | (v[2] << 16), v[1] | (v[0] << 16)};
                *(u32x4*)(kb + PB_K1) = u32x4{v[6] | (v[5] << 16), v[4] | (v[3] << 16), v[2] | (v[1] << 16), v[0] | (vm << 16)};
            }
        }
        STAMP();
        if (w >= 4) {
            u32x4 o[4];
            tr8x4(gv, o);
#pragma unroll
            for (int c = 0; c < 4; ++c) *(u32x4*)(smem + L_U + c * PB_U + 512 + (t - 256) * 16) = o[c];
        }
        __syncthreads();
        STAMP();
        const int ch = w & 3, rh = w >> 2;
        const int mu = g >> 1, hp = g & 1;
        const int x0 = 2047 - 16 * mu - i16 + 8 * hp;
        const char* ap = smem + L_K + ch * PB_KS + ((x0 & 1) ? PB_K1 + (x0 - 1) * 2 : x0 * 2);
        const char* bp = smem + L_U + ch * PB_U + (256 + 16 * (i16 - mu) + 8 * hp) * 2;
        f32x4 acc[8];
#pragma unroll
        for (int c = 0; c < 8; ++c) acc[c] = f32x4{0.f, 0.f, 0.f, 0.f};
#pragma unroll 1
        for (int r = 4 * rh; r < 4 * rh + 4; ++r) {
            u32x4 A[8], B[8];
#pragma unroll
            for (int p = 0; p < 8; ++p) {
                const u32* a = (const u32*)(ap - 64 * r - 512 * p);
                A[p] = u32x4{a[0], a[1], a[2], a[3]};
                B[p] = *(const u32x4*)(bp - 64 * r + 512 * p);
            }
#pragma unroll
            for (int p = 0; p < 8; ++p)
#pragma unroll
                for (int q = 0; q + p < 8; ++q) acc[p + q] = mfma16(A[p], B[q], acc[p + q]);
        }
        STAMP();
        char* rb = smem + L_G;
#pragma unroll
        for (int c = 0; c < 4; ++c) *(f32x4*)(rb + (w * 4 + c) * 1024 + l * 16) = rh ? acc[c] : acc[4 + c];
        __syncthreads();
        const float dsk = bf2f(ld2(mkrs(P.dskip, 1280), (4 * chb + ch) * 2));
        char* st = smem + L_G + 32768;
#pragma unroll
        for (int c = 0; c < 4; ++c) {
            const f32x4 o = *(const f32x4*)(rb + ((w ^ 4) * 4 + c) * 1024 + l * 16);
            const f32x4 mine = rh ? acc[4 + c] : acc[c];
            const int tt = 256 * (4 * rh + c) + 16 * i16 + 4 * g;
            const u32x2 uu = *(const u32x2*)(smem + L_U + ch * PB_U + (256 + tt) * 2);
#pragma unroll
            for (int i = 0; i < 4; ++i) {
                const float uf = (i & 1) ? bfhi(i < 2 ? uu.x : uu.y) : bflo(i < 2 ? uu.x : uu.y);
                const float v = rbf(mine[i] + o[i]) + rbf(uf * dsk);
                *(u16*)(st + (tt >> 4) * 128 + (((4 * g + i) ^ i16) * 8) + ch * 2) = (u16)(pk(v, v) & 0xffffu);
            }
        }
        __syncthreads();
        {
            const rsrc_t ro = mkrs(P.out, 2048 * 2560);
#pragma unroll
            for (int i = 0; i < 4; ++i) {
                const int row = t + 512 * i;
                __builtin_amdgcn_raw_buffer_store_b64(*(const u32x2*)(st + (row >> 4) * 128 + (((row & 15) ^ ((row >> 4) & 15)) * 8)), ro,
                                                      row * 2560 + 1280 + chb * 8, 0, 0);
            }
        }
        STAMP();
        wait_filled(rws, tag, w, l);
    }
    if (b == 0 && t == 0) __hip_atomic_store((GAS u32*)(P.ws + OFF_SYNC), tag, __ATOMIC_RELAXED, __HIP_MEMORY_SCOPE_AGENT);

    u32x4 C[2], S[2], nsS[2], sC[2], F[2];
    float twc[8], tws[8];
    if (w < 4) {
        const u32 sgn = (g >= 2) ? 0x80008000u : 0u;
#pragma unroll
        for (int tau = 0; tau < 2; ++tau) {
            float c[8], s[8];
            const float d1 = (float)(16 * tau + i16);
#pragma unroll
            for (int j = 0; j < 8; ++j) {
                const float a = (float)(8 * g + j) * d1 * 0.03125f;
                c[j] = cosr(a);
                s[j] = sinr(a);
            }
            C[tau] = pk8(c); S[tau] = pk8(s);
            nsS[tau] = S[tau] ^ (sgn ^ 0x80008000u);
            sC[tau] = C[tau] ^ sgn;
#pragma unroll
            for (int i = 0; i < 4; ++i) {
                const float a = d1 * (float)(4 * g + i) * (1.0f / 640.0f);
                twc[4 * tau + i] = cosr(a) * (1.0f / (2048.0f * 640.0f));
                tws[4 * tau + i] = sinr(a) * (1.0f / (2048.0f * 640.0f));
            }
        }
#pragma unroll
        for (int dl = 0; dl < 2; ++dl) {
            float v[8];
#pragma unroll
            for (int j = 0; j < 8; ++j) {
                const int k2 = 4 * g + (j & 3), d2 = 16 * dl + i16;
                const float wgt = (k2 > 10 || d2 >= 20) ? 0.f : ((k2 == 0 || k2 == 10) ? 1.f : 2.f);
                const float a = (float)k2 * (float)d2 * 0.05f;
                v[j] = (j < 4) ? wgt * cosr(a) : -wgt * sinr(a);
            }
            F[dl] = pk8(v);
        }
    }
    u32x2 fa;
    {
        float f[4];
        const int plo = i16 >> 3, a = i16 & 7;
#pragma unroll
        for (int j = 0; j < 4; ++j) {
            const int kap = 2 * g + (j >> 1), pl = j & 1;
            const float th = (float)kap * (float)(256 * a + b) * (1.0f / 2048.0f) - ((pl != plo) ? 0.25f : 0.f);
            const float v = cosr(th);
            f[j] = (plo == 0 && pl == 1) ? -v : v;
        }
        fa = u32x2{pk(f[0], f[1]), pk(f[2], f[3])};
    }
    {
        if (w == 0) {
            for (u32 spins = 0; spins < SPIN_MAX; ++spins) {
                fence();
                const u32x4 f = ld16(rws, OFF_Y1 + min(l + l / 2, 95) * Y1_BLK + b * 112 + 96, 1);
                if (__all(fresh4(f))) break;
                __builtin_amdgcn_s_sleep(2);
            }
        }
        __syncthreads();
        STAMP();
        u32 off[2];
#pragma unroll
        for (int i = 0; i < 2; ++i) {
            const int m = min(t + 512 * i, 671), ab = m / 7;
            off[i] = OFF_Y1 + ab * Y1_BLK + b * 112 + (m - 7 * ab) * 16;
        }
        u32x4 v[2];
        for (u32 spins = 0; spins < SPIN_MAX; ++spins) {
            fence();
            v[0] = ld16(rws, off[0], 1);
            v[1] = ld16(rws, off[1], 1);
            if (__all(fresh4(v[0]) && fresh4(v[1]))) break;
            __builtin_amdgcn_s_sleep(1);
        }
#pragma unroll
        for (int i = 0; i < 2; ++i) *(u32x4*)(smem + L_X + min(t + 512 * i, 671) * 16) = v[i];
    }
    STAMP();
    __syncthreads();
    {
        const int q = i16 >> 2, p = i16 & 3;
        const int kap = 2 * g + (q >> 1), pl = q & 1;
#pragma unroll
        for (int it = 0; it < 3; ++it) {
            const int T = w + 8 * it;
            if (T < 21) {
                const int k = min(16 * T + 4 * p, 320);
                const int cgk = min(k / 28, 11), idx = k - min(28 * cgk, 296);
                const u32x2 bfr = trread(smem + L_X + (kap * 12 + cgk) * 112 + pl * 56 + idx * 2);
                const f32x4 d = mfma16k16(fa, bfr, f32x4{0.f, 0.f, 0.f, 0.f});
                *(u32x2*)(smem + L_S + (16 * T + i16) * 32 + g * 8) = u32x2{pk(d[0], d[1]), pk(d[2], d[3])};
            }
        }
    }
    __syncthreads();
    if (w < 4) {
        const int k2 = i16 <= 10 ? i16 : 10;
        u32 dre[8], dim[8];
#pragma unroll
        for (int j = 0; j < 8; ++j) {
            const int k = k2 + 20 * (8 * g + j);
            const int kk = (g < 2) ? k : 640 - k;
            dre[j] = *(const u32*)(smem + L_S + kk * 32 + w * 4);
            dim[j] = *(const u32*)(smem + L_S + kk * 32 + 16 + w * 4);
        }
#pragma unroll
        for (int rr = 0; rr < 2; ++rr) {
            u32x4 are, aim;
            if (rr == 0) {
                are = u32x4{permlo(dre[0], dre[1]), permlo(dre[2], dre[3]), permlo(dre[4], dre[5]), permlo(dre[6], dre[7])};
                aim = u32x4{permlo(dim[0], dim[1]), permlo(dim[2], dim[3]), permlo(dim[4], dim[5]), permlo(dim[6], dim[7])};
            } else {
                are = u32x4{permhi(dre[0], dre[1]), permhi(dre[2], dre[3]), permhi(dre[4], dre[5]), permhi(dre[6], dre[7])};
                aim = u32x4{permhi(dim[0], dim[1]), permhi(dim[2], dim[3]), permhi(dim[4], dim[5]), permhi(dim[6], dim[7])};
            }
            u32x4 va[2];
            const f32x4 zero = {0.f, 0.f, 0.f, 0.f};
#pragma unroll
            for (int tau = 0; tau < 2; ++tau) {
                const f32x4 vre = mfma16(aim, nsS[tau], mfma16(are, C[tau], zero));
                const f32x4 vim = mfma16(aim, sC[tau], mfma16(are, S[tau], zero));
                float pr[8];
#pragma unroll
                for (int i = 0; i < 4; ++i) {
                    pr[i] = vre[i] * twc[4 * tau + i] - vim[i] * tws[4 * tau + i];
                    pr[4 + i] = vim[i] * twc[4 * tau + i] + vre[i] * tws[4 * tau + i];
                }
                va[tau] = pk8(pr);
            }
            u16* orow = P.out + (size_t)(b + 256 * (2 * w + rr)) * 1280;
#pragma unroll
            for (int dl = 0; dl < 2; ++dl) {
                const f32x4 y0 = mfma16(va[0], F[dl], zero);
                const f32x4 y1 = mfma16(va[1], F[dl], zero);
                u32 a0 = pk(y0[0], y0[1]), a1 = pk(y0[2], y0[3]);
                u32 b0 = pk(y1[0], y1[1]), b1 = pk(y1[2], y1[3]);
                auto s0 = __builtin_amdgcn_permlane16_swap(a0, b0, false, false);
                auto s1 = __builtin_amdgcn_permlane16_swap(a1, b1, false, false);
                const int d2 = 16 * dl + i16;
                if (d2 < 20) *(u32x4*)(orow + 32 * d2 + (g & 1) * 16 + (g >> 1) * 8) = u32x4{s0[0], s1[0], s0[1], s1[1]};
            }
        }
    }
    STAMP();
    STAMP_OUT(P.ws);
}

extern "C" void kernel_launch(void* const* d_in, const int* in_sizes, int n_in,
                              void* d_out, int out_size, void* d_ws, size_t ws_size,
                              hipStream_t stream) {
    Args a;
    a.x = (const u16*)d_in[0];
    a.wr = (const u16*)d_in[1];
    a.wi = (const u16*)d_in[2];
    a.u = (const u16*)d_in[3];
    a.Are = (const u16*)d_in[4];
    a.Aim = (const u16*)d_in[5];
    a.Bre = (const u16*)d_in[6];
    a.Bim = (const u16*)d_in[7];
    a.Cre = (const u16*)d_in[8];
    a.Cim = (const u16*)d_in[9];
    a.logdt = (const u16*)d_in[10];
    a.dskip = (const u16*)d_in[11];
    a.ws = (char*)d_ws;
    a.out = (u16*)d_out;
    (void)hipFuncSetAttribute((const void*)fused_kernel, hipFuncAttributeMaxDynamicSharedMemorySize, LDS_BYTES);
    fused_kernel<<<256, 512, LDS_BYTES, stream>>>(a);
}
```

```cpp
#include <hip/hip_runtime.h>
#include <hip/hip_bf16.h>

typedef unsigned short u16;
typedef unsigned int u32;
typedef __attribute__((ext_vector_type(2))) __bf16 bf16x2;
typedef __attribute__((ext_vector_type(8))) __bf16 bf16x8;
typedef __attribute__((ext_vector_type(2))) float f32x2;
typedef __attribute__((ext_vector_type(4))) float f32x4;
typedef __attribute__((ext_vector_type(4))) u32 u32x4;
typedef __attribute__((ext_vector_type(2))) u32 u32x2;
typedef __attribute__((ext_vector_type(4))) short s16x4;
typedef __amdgpu_buffer_rsrc_t rsrc_t;
#define LAS __attribute__((address_space(3)))
#define GAS __attribute__((address_space(1)))
#define DI __device__ __forceinline__
#define SC1 16

constexpr u32 X1_ROW = 10368;
constexpr u32 OFF_X1 = 0;
constexpr u32 Y1_BLK = 28672;
constexpr u32 OFF_Y1 = 256 * X1_ROW;
constexpr u32 OFF_SYNC = OFF_Y1 + 96 * Y1_BLK;
constexpr u32 WS_USED = OFF_SYNC + 4096;
constexpr u32 SENT = 0xFFFFFFFFu;
constexpr u32 SPIN_MAX = 1u << 16;

constexpr int L_X = 0;
constexpr int L_S = 32256;
constexpr int L_W = 64512;
constexpr int L_C = L_W + 28672;
constexpr int PB_U = 4608;
constexpr int L_U = 0;
constexpr int L_K = L_U + 4 * PB_U;
constexpr int PB_K1 = 4160, PB_KS = 8320;
constexpr int L_G = L_K + 4 * PB_KS;
constexpr int KG_RS = 272, KG_B = 41 * KG_RS, KG_T = 57 * KG_RS, KG_SZ = 19616;
constexpr int L_CNT = L_G + 4 * KG_SZ;
constexpr int LDS_BYTES = L_CNT + 16;

#define STAMP_DECL
#define STAMP()
#define STAMP_OUT(ws)

DI u32 pk(float a, float b) {
    f32x2 v = {a, b};
    return __builtin_bit_cast(u32, __builtin_convertvector(v, bf16x2));
}
DI u32x4 pk8(const float* v) { return u32x4{pk(v[0], v[1]), pk(v[2], v[3]), pk(v[4], v[5]), pk(v[6], v[7])}; }
DI float bflo(u32 v) { return __uint_as_float(v << 16); }
DI float bfhi(u32 v) { return __uint_as_float(v & 0xffff0000u); }
DI float bf2f(u32 v) { return __uint_as_float(v << 16); }
DI float rbf(float a) { return bflo(pk(a, a)); }
DI float cosr(float rev) { return __builtin_amdgcn_cosf(rev); }
DI float sinr(float rev) { return __builtin_amdgcn_sinf(rev); }
DI f32x4 mfma16(u32x4 a, u32x4 b, f32x4 c) {
    return __builtin_amdgcn_mfma_f32_16x16x32_bf16(__builtin_bit_cast(bf16x8, a), __builtin_bit_cast(bf16x8, b), c, 0, 0, 0);
}
DI f32x4 mfma16k16(u32x2 a, u32x2 b, f32x4 c) {
    return __builtin_amdgcn_mfma_f32_16x16x16bf16_1k(__builtin_bit_cast(s16x4, a), __builtin_bit_cast(s16x4, b), c, 0, 0, 0);
}
DI u32x2 trread(const char* p) {
    return __builtin_bit_cast(u32x2, __builtin_amdgcn_ds_read_tr16_b64_v4i16((LAS s16x4*)(p)));
}
DI u32 permlo(u32 a, u32 b) { return __builtin_amdgcn_perm(b, a, 0x05040100u); }
DI u32 permhi(u32 a, u32 b) { return __builtin_amdgcn_perm(b, a, 0x07060302u); }
DI rsrc_t mkrs(const void* p, u32 bytes) { return __builtin_amdgcn_make_buffer_rsrc((void*)p, 0, bytes, 0x00020000); }
DI u32x4 ld16(rsrc_t r, u32 off, int aux) { return aux ? __builtin_amdgcn_raw_buffer_load_b128(r, off, 0, SC1) : __builtin_amdgcn_raw_buffer_load_b128(r, off, 0, 0); }
DI u32x2 ld8(rsrc_t r, u32 off, int aux) { return aux ? __builtin_amdgcn_raw_buffer_load_b64(r, off, 0, SC1) : __builtin_amdgcn_raw_buffer_load_b64(r, off, 0, 0); }
DI u32 ld2(rsrc_t r, u32 off) { return __builtin_amdgcn_raw_buffer_load_b16(r, off, 0, 0); }
DI void st16sc(rsrc_t r, u32 off, u32x4 v) { __builtin_amdgcn_raw_buffer_store_b128(v, r, off, 0, SC1); }
DI u32x4 sane(u32x4 v) { return u32x4{min(v.x, SENT - 1), min(v.y, SENT - 1), min(v.z, SENT - 1), min(v.w, SENT - 1)}; }
DI bool fresh4(u32x4 v) { return max(max(v.x, v.y), max(v.z, v.w)) != SENT; }
DI bool fresh2(u32x2 v) { return max(v.x, v.y) != SENT; }
DI void fence() { asm volatile("" ::: "memory"); }
DI void drain() { asm volatile("s_waitcnt vmcnt(0)" ::: "memory"); }
DI void tr8x4(const u32x2* v, u32x4* o) {
    o[0] = u32x4{permlo(v[0].x, v[1].x), permlo(v[2].x, v[3].x), permlo(v[4].x, v[5].x), permlo(v[6].x, v[7].x)};
    o[1] = u32x4{permhi(v[0].x, v[1].x), permhi(v[2].x, v[3].x), permhi(v[4].x, v[5].x), permhi(v[6].x, v[7].x)};
    o[2] = u32x4{permlo(v[0].y, v[1].y), permlo(v[2].y, v[3].y), permlo(v[4].y, v[5].y), permlo(v[6].y, v[7].y)};
    o[3] = u32x4{permhi(v[0].y, v[1].y), permhi(v[2].y, v[3].y), permhi(v[4].y, v[5].y), permhi(v[6].y, v[7].y)};
}
template <int NF>
DI void wait_flags(rsrc_t rws, u32 off, u32 tag, int w, int l) {
    if (w == 0) {
        const int q = min(l, NF / 4 - 1);
        for (u32 spins = 0; spins < SPIN_MAX; ++spins) {
            fence();
            const u32x4 f = ld16(rws, off + q * 16, 1);
            if (__all(f.x == tag && f.y == tag && f.z == tag && f.w == tag)) break;
            __builtin_amdgcn_s_sleep(2);
        }
    }
    __syncthreads();
}

struct cf { float re, im; };
DI cf cmul(cf a, cf b) { return cf{a.re * b.re - a.im * b.im, a.re * b.im + a.im * b.re}; }
DI cf cexpk(float lre, float lim_rev, float k) {
    const float m = __builtin_amdgcn_exp2f(k * lre * 1.44269504088896341f);
    const float a = __builtin_amdgcn_fractf(k * lim_rev);
    return cf{m * cosr(a), m * sinr(a)};
}

struct Args {
    const u16 *x, *wr, *wi, *u, *Are, *Aim, *Bre, *Bim, *Cre, *Cim, *logdt, *dskip;
    char* ws;
    u16* out;
};

DI void role_a(const Args& P, char* smem, const int t, const int w, const int ka, const int cg, const u32 tag) {
    const int l = t & 63, g = l >> 4, i16 = l & 15;
    const int ai = ka * 12 + cg, nset = ai < 64 ? 3 : 2;
    const int colbase = min(28 * cg, 296);
    const rsrc_t rws = mkrs(P.ws, WS_USED);
    STAMP_DECL STAMP();
    {
        const u32x4 s4 = {SENT, SENT, SENT, SENT};
#pragma unroll
        for (int i = 0; i < 4; ++i) {
            const int m = min(t + 512 * i, nset * 648 - 1), j = m / 648;
            st16sc(rws, OFF_X1 + (ai + 96 * j) * X1_ROW + (m - 648 * j) * 16, s4);
        }
    }
    u32x4 xv[2][3];
    {
        const rsrc_t rx = mkrs(P.x, 2048 * 1280);
#pragma unroll
        for (int it = 0; it < 2; ++it) {
            const int task = min(w + 8 * it, 4 * nset - 1), s0 = ai + 96 * (task >> 2);
#pragma unroll
            for (int i = 0; i < 3; ++i) {
                const int ci = min(l + 64 * i, 159), row = ci >= 80;
                xv[it][i] = ld16(rx, (s0 + 256 * (2 * (task & 3) + row)) * 1280 + (ci - 80 * row) * 16, 0);
            }
        }
    }
    fence();
    if (t < 90) *(u32x4*)(smem + L_S + (t / 30) * 10752 + 321 * 32 + (t % 30) * 16) = u32x4{0u, 0u, 0u, 0u};

    {
        u32x4 f20c, f20s, c32, s32;
        float twc[8], tws[8];
        {
            float c[8], s[8];
#pragma unroll
            for (int j = 0; j < 8; ++j) {
                const int d2 = (j < 4) ? 4 * g + j : 16 + 4 * g + j - 4;
                const bool ok = (d2 < 20) && (i16 <= 10);
                const float a = (float)d2 * (float)i16 * 0.05f;
                c[j] = ok ? cosr(a) : 0.f;
                s[j] = ok ? -sinr(a) : 0.f;
            }
            f20c = pk8(c); f20s = pk8(s);
#pragma unroll
            for (int j = 0; j < 8; ++j) {
                const int d1 = (j < 4) ? 4 * g + j : 16 + 4 * g + j - 4;
                const float pr = (float)d1 * (float)i16;
                c[j] = cosr(pr * 0.03125f);
                s[j] = sinr(pr * 0.03125f);
                twc[j] = cosr(pr * (1.0f / 640.0f));
                tws[j] = sinr(pr * (1.0f / 640.0f));
            }
            c32 = pk8(c); s32 = pk8(s);
        }
        const u32x4 ns32 = s32 ^ 0x80008000u, c32b = c32 ^ 0x80000000u, s32b = s32 ^ 0x80000000u, nc32b = c32b ^ 0x80008000u;
        const int q = i16 >> 2, p = i16 & 3;
        const int d2a = 4 * g + q, d2b = (g == 0) ? 16 + q : d2a;
        const int offa = 64 * d2a + 8 * p, swa = ((d2a >> 2) & 1) << 5;
        const int offb = 64 * d2b + 8 * p, swb = ((d2b >> 2) & 1) << 5;
#pragma unroll
        for (int it = 0; it < 2; ++it) {
            const int task = w + 8 * it;
            if (task < 4 * nset) {
                char* xs = smem + L_X + task * 2560;
#pragma unroll
                for (int i = 0; i < 3; ++i) {
                    const int ci = min(l + 64 * i, 159), c = ci >= 80 ? ci - 80 : ci;
                    *(u32x4*)(xs + ((ci * 16) ^ (((c >> 4) & 1) << 5))) = xv[it][i];
                }
                f32x4 z[2][4];
#pragma unroll
                for (int rr = 0; rr < 2; ++rr) {
                    const char* xr = xs + rr * 1280;
                    f32x4 tre[2], tim[2];
                    const f32x4 zero = {0.f, 0.f, 0.f, 0.f};
#pragma unroll
                    for (int tau = 0; tau < 2; ++tau) {
                        const u32x2 a0 = trread(xr + offa + ((32 * tau) ^ swa));
                        const u32x2 a1 = trread(xr + offb + ((32 * tau) ^ swb));
                        const u32x4 a = {a0.x, a0.y, a1.x, a1.y};
                        tre[tau] = mfma16(a, f20c, zero);
                        tim[tau] = mfma16(a, f20s, zero);
                    }
                    float pr[8], pi[8];
#pragma unroll
                    for (int j = 0; j < 8; ++j) {
                        const float a = tre[j >> 2][j & 3], bb = tim[j >> 2][j & 3];
                        pr[j] = a * twc[j] + bb * tws[j];
                        pi[j] = bb * twc[j] - a * tws[j];
                    }
                    const u32x4 bre = pk8(pr), bim = pk8(pi);
                    z[rr][0] = mfma16(s32, bim, mfma16(c32, bre, zero));
                    z[rr][1] = mfma16(ns32, bre, mfma16(c32, bim, zero));
                    z[rr][2] = mfma16(s32b, bim, mfma16(c32b, bre, zero));
                    z[rr][3] = mfma16(s32b, bre, mfma16(nc32b, bim, zero));
                }
                char* st = smem + L_S + (task >> 2) * 10752 + (task & 3) * 4;
                if (i16 <= 10) {
#pragma unroll
                    for (int i = 0; i < 4; ++i) {
                        const int k = i16 + 20 * (4 * g + i);
                        *(u32*)(st + k * 32) = pk(z[0][0][i], z[1][0][i]);
                        *(u32*)(st + k * 32 + 16) = pk(z[0][1][i], z[1][1][i]);
                    }
#pragma unroll
                    for (int i = 0; i < 4; ++i) {
                        const int k = 320 - i16 - 20 * (4 * g + i);
                        const bool ok = (i16 >= 1 && i16 <= 9) || (i16 == 0 && g == 0 && i == 0);
                        if (ok) {
                            *(u32*)(st + k * 32) = pk(z[0][2][i], z[1][2][i]);
                            *(u32*)(st + k * 32 + 16) = pk(z[0][3][i], z[1][3][i]);
                        }
                    }
                }
            }
        }
        drain();
    }
    STAMP();
    __syncthreads();
    if (t == 0) __hip_atomic_store((GAS u32*)(P.ws + OFF_SYNC + 1024) + ai, tag, __ATOMIC_RELAXED, __HIP_MEMORY_SCOPE_AGENT);
    {
        const int kap = i16 >> 1, plo = i16 & 1, pl = g >> 1;
#pragma unroll 1
        for (int j = 0; j < nset; ++j) {
            const int s0 = ai + 96 * j;
            float f[4];
#pragma unroll
            for (int e = 0; e < 4; ++e) {
                const int a = 4 * (g & 1) + e;
                const float th = (float)kap * (float)(256 * a + s0) * (1.0f / 2048.0f) - ((pl != plo) ? 0.25f : 0.f);
                const float v = cosr(th);
                f[e] = (plo == 1 && pl == 0) ? -v : v;
            }
            const u32x2 fb = {pk(f[0], f[1]), pk(f[2], f[3])};
#pragma unroll
            for (int it = 0; it < 3; ++it) {
                const int T = w + 8 * it;
                if (T < 21) {
                    const u32x2 a = *(const u32x2*)(smem + L_S + j * 10752 + (16 * T + i16) * 32 + g * 8);
                    const f32x4 d = mfma16k16(a, fb, f32x4{0.f, 0.f, 0.f, 0.f});
                    if (T < 20 || g == 0)
                        *(u32x2*)(smem + L_X + j * 10368 + i16 * 648 + (16 * T + 4 * g) * 2) = u32x2{pk(d[0], d[1]), pk(d[2], d[3])};
                }
            }
        }
    }
    __syncthreads();
#pragma unroll
    for (int i = 0; i < 4; ++i) {
        const int m = min(t + 512 * i, nset * 648 - 1), j = m / 648;
        st16sc(rws, OFF_X1 + (ai + 96 * j) * X1_ROW + (m - 648 * j) * 16, sane(*(const u32x4*)(smem + L_X + m * 16)));
    }
    STAMP();
    u32x2 wv[8];
    const int wpl = w >> 2, wtask = min(t & 255, 223), wquad = wtask % 7, wr_ = wtask / 7, wk1 = wr_ & 15, woc = wr_ >> 4;
    {
        const rsrc_t rw = mkrs(wpl ? P.wi : P.wr, 2048 * 642);
        const int cs = min(colbase + 4 * wquad, 317);
#pragma unroll
        for (int e = 0; e < 8; ++e) wv[e] = ld8(rw, ((ka + 8 * (wk1 + 16 * (8 * woc + e))) * 321 + cs) * 2, 0);
    }
    fence();
    u32x4 f1re, f1im, f2re, f2im;
    float t2c[4], t2s[4];
    {
        float c[8], s[8];
#pragma unroll
        for (int j = 0; j < 8; ++j) {
            const float a = (float)(8 * (g & 1) + j) * (float)i16 * 0.0625f;
            const float co = cosr(a), si = sinr(a);
            c[j] = (g < 2) ? co : si;
            s[j] = (g < 2) ? -si : co;
        }
        f1re = pk8(c); f1im = pk8(s);
#pragma unroll
        for (int j = 0; j < 4; ++j) {
            const float pr = (float)(4 * g + j) * (float)i16;
            const float co = cosr(pr * 0.0625f), si = sinr(pr * 0.0625f);
            c[j] = co; c[4 + j] = si;
            s[j] = -si; s[4 + j] = co;
            t2c[j] = cosr(pr * (1.0f / 256.0f));
            t2s[j] = sinr(pr * (1.0f / 256.0f));
        }
        f2re = pk8(c); f2im = pk8(s);
    }
    const u32x4 ire = {f2re.x, f2re.y, f2re.z ^ 0x80008000u, f2re.w ^ 0x80008000u};
    const u32x4 iim = {f2im.x ^ 0x80008000u, f2im.y ^ 0x80008000u, f2im.z, f2im.w};
    wait_flags<96>(rws, OFF_SYNC + 1024, tag, w, l);
    STAMP();
    {
        const int task = min(t, 447), quad = task % 7, r = task / 7, pl = r >> 5, oc = r & 31;
        const u32 base = OFF_X1 + (8 * oc) * X1_ROW + (ka * 2 + pl) * 648 + (colbase + 4 * quad) * 2;
        u32x2 v[8];
        for (u32 spins = 0; spins < SPIN_MAX; ++spins) {
            fence();
            bool ok = true;
#pragma unroll
            for (int e = 0; e < 8; ++e) v[e] = ld8(rws, base + e * X1_ROW, 1);
#pragma unroll
            for (int e = 0; e < 8; ++e) ok &= fresh2(v[e]);
            if (__all(ok)) break;
            __builtin_amdgcn_s_sleep(1);
        }
        u32x4 o[4];
        tr8x4(v, o);
#pragma unroll
        for (int c = 0; c < 4; ++c) {
            const int m = (4 * quad + c) * 2 + pl;
            *(u32x4*)(smem + L_C + m * 512 + ((oc ^ ((m >> 3) & 7)) * 16)) = o[c];
        }
    }
    {
        if (colbase + 4 * wquad > 317) {
#pragma unroll
            for (int e = 0; e < 8; ++e) wv[e] = u32x2{wv[e].y >> 16, 0u};
        }
        u32x4 o[4];
        tr8x4(wv, o);
#pragma unroll
        for (int c = 0; c < 4; ++c) {
            const int m = (4 * wquad + c) * 2 + wpl;
            *(u32x4*)(smem + L_W + m * 512 + (((wk1 * 2 + woc) ^ ((m >> 3) & 7)) * 16)) = o[c];
        }
    }
    STAMP();
    __syncthreads();
    {
        const int q = i16 >> 2, p = i16 & 3;
#pragma unroll 1
        for (int col = w; col < 28; col += 8) {
            const f32x4 zero = {0.f, 0.f, 0.f, 0.f};
            const int mi = col * 2 + (g >> 1), swi = (mi >> 3) & 7;
            const char* ib = smem + L_C + mi * 512 + (p & 1) * 8;
            const u32x2 a0 = trread(ib + (((2 * (8 * (g & 1) + q) + (p >> 1)) ^ swi) * 16));
            const u32x2 a1 = trread(ib + (((2 * (8 * (g & 1) + 4 + q) + (p >> 1)) ^ swi) * 16));
            const u32x4 a = {a0.x, a0.y, a1.x, a1.y};
            const int m0 = col * 2, sw = (m0 >> 3) & 7;
            const int woff = (((i16 * 2 + (g >> 1)) ^ sw) * 16) + (g & 1) * 8;
            const u32x2 wre = *(const u32x2*)(smem + L_W + m0 * 512 + woff);
            const u32x2 wim = *(const u32x2*)(smem + L_W + (m0 + 1) * 512 + woff);
            f32x4 re = mfma16(a, f1re, zero), im = mfma16(a, f1im, zero);
            float x[8];
#pragma unroll
            for (int i = 0; i < 4; ++i) {
                x[i] = re[i] * t2c[i] + im[i] * t2s[i];
                x[4 + i] = im[i] * t2c[i] - re[i] * t2s[i];
            }
            u32x4 d = pk8(x);
            re = mfma16(f2re, d, zero); im = mfma16(f2im, d, zero);
#pragma unroll
            for (int i = 0; i < 4; ++i) {
                const float a_ = (i & 1) ? bfhi(wre[i >> 1]) : bflo(wre[i >> 1]);
                const float b_ = (i & 1) ? bfhi(wim[i >> 1]) : bflo(wim[i >> 1]);
                x[i] = re[i] * a_ - im[i] * b_;
                x[4 + i] = re[i] * b_ + im[i] * a_;
            }
            d = pk8(x);
            re = mfma16(d, ire, zero); im = mfma16(d, iim, zero);
#pragma unroll
            for (int i = 0; i < 4; ++i) {
                x[i] = re[i] * t2c[i] - im[i] * t2s[i];
                x[4 + i] = im[i] * t2c[i] + re[i] * t2s[i];
            }
            d = pk8(x);
            re = mfma16(d, ire, zero); im = mfma16(d, iim, zero);
            const int ooff = (((2 * i16 + (g >> 1)) ^ sw) * 16) + (g & 1) * 8;
            *(u32x2*)(smem + L_C + m0 * 512 + ooff) = u32x2{pk(re[0], re[1]), pk(re[2], re[3])};
            *(u32x2*)(smem + L_C + (m0 + 1) * 512 + ooff) = u32x2{pk(im[0], im[1]), pk(im[2], im[3])};
        }
    }
    STAMP();
    wait_flags<160>(rws, OFF_SYNC + 2048, tag, w, l);
    if (ai == 0 && t == 0) __hip_atomic_store((GAS u32*)(P.ws + OFF_SYNC), tag, __ATOMIC_RELAXED, __HIP_MEMORY_SCOPE_AGENT);
    {
        const int q = i16 >> 2, p = i16 & 3;
#pragma unroll
        for (int it = 0; it < 7; ++it) {
            const int cmb = (w * 7 + it) * 4 + g;
            const int quad = cmb % 7, r = cmb / 7, pl = r & 1, G = r >> 1;
            const int m = (4 * quad + q) * 2 + pl;
            const u32x2 v = trread(smem + L_C + m * 512 + (((2 * G + (p >> 1)) ^ ((m >> 3) & 7)) * 16) + (p & 1) * 8);
            *(u32x2*)(smem + L_W + (16 * G + i16) * 112 + pl * 56 + quad * 8) = v;
        }
    }
    __syncthreads();
#pragma unroll
    for (int i = 0; i < 4; ++i) {
        const int m = min(t + 512 * i, 1791);
        st16sc(rws, OFF_Y1 + ai * Y1_BLK + m * 16, sane(*(const u32x4*)(smem + L_W + m * 16)));
    }
    STAMP();

    u32x4 C[2], S[2], nsS[2], sC[2], F[2];
    float twc[8], tws[8];
    {
        const u32 sgn = (g >= 2) ? 0x80008000u : 0u;
#pragma unroll
        for (int tau = 0; tau < 2; ++tau) {
            float c[8], s[8];
            const float d1 = (float)(16 * tau + i16);
#pragma unroll
            for (int j = 0; j < 8; ++j) {
                const float a = (float)(8 * g + j) * d1 * 0.03125f;
                c[j] = cosr(a);
                s[j] = sinr(a);
            }
            C[tau] = pk8(c); S[tau] = pk8(s);
            nsS[tau] = S[tau] ^ (sgn ^ 0x80008000u);
            sC[tau] = C[tau] ^ sgn;
#pragma unroll
            for (int i = 0; i < 4; ++i) {
                const float a = d1 * (float)(4 * g + i) * (1.0f / 640.0f);
                twc[4 * tau + i] = cosr(a) * (1.0f / (2048.0f * 640.0f));
                tws[4 * tau + i] = sinr(a) * (1.0f / (2048.0f * 640.0f));
            }
        }
#pragma unroll
        for (int dl = 0; dl < 2; ++dl) {
            float v[8];
#pragma unroll
            for (int j = 0; j < 8; ++j) {
                const int k2 = 4 * g + (j & 3), d2 = 16 * dl + i16;
                const float wgt = (k2 > 10 || d2 >= 20) ? 0.f : ((k2 == 0 || k2 == 10) ? 1.f : 2.f);
                const float a = (float)k2 * (float)d2 * 0.05f;
                v[j] = (j < 4) ? wgt * cosr(a) : -wgt * sinr(a);
            }
            F[dl] = pk8(v);
        }
    }
    {
        if (w == 0) {
            for (u32 spins = 0; spins < SPIN_MAX; ++spins) {
                fence();
                const u32x4 f = ld16(rws, OFF_Y1 + (l + l / 2) * Y1_BLK + ai * 112 + 96, 1);
                if (__all(fresh4(f))) break;
                __builtin_amdgcn_s_sleep(1);
            }
        }
        __syncthreads();
        STAMP();
        u32 off[4];
#pragma unroll
        for (int i = 0; i < 4; ++i) {
            const int m = min(t + 512 * i, nset * 672 - 1), j = m / 672, mm = m - 672 * j, ab = mm / 7;
            off[i] = OFF_Y1 + ab * Y1_BLK + (ai + 96 * j) * 112 + (mm - 7 * ab) * 16;
        }
        u32x4 v[4];
        for (u32 spins = 0; spins < SPIN_MAX; ++spins) {
            fence();
#pragma unroll
            for (int i = 0; i < 4; ++i) v[i] = ld16(rws, off[i], 1);
            if (__all(fresh4(v[0]) && fresh4(v[1]) && fresh4(v[2]) && fresh4(v[3]))) break;
            __builtin_amdgcn_s_sleep(1);
        }
#pragma unroll
        for (int i = 0; i < 4; ++i) *(u32x4*)(smem + L_X + min(t + 512 * i, nset * 672 - 1) * 16) = v[i];
    }
    STAMP();
    __syncthreads();
    {
        const int q = i16 >> 2, p = i16 & 3;
        const int kap = 2 * g + (q >> 1), pl = q & 1;
        const int plo = i16 >> 3, a = i16 & 7;
#pragma unroll 1
        for (int j = 0; j < nset; ++j) {
            const int s0 = ai + 96 * j;
            float f[4];
#pragma unroll
            for (int e = 0; e < 4; ++e) {
                const int kk = 2 * g + (e >> 1), pp = e & 1;
                const float th = (float)kk * (float)(256 * a + s0) * (1.0f / 2048.0f) - ((pp != plo) ? 0.25f : 0.f);
                const float v = cosr(th);
                f[e] = (plo == 0 && pp == 1) ? -v : v;
            }
            const u32x2 fa = {pk(f[0], f[1]), pk(f[2], f[3])};
#pragma unroll
            for (int it = 0; it < 3; ++it) {
                const int T = w + 8 * it;
                if (T < 21) {
                    const int k = min(16 * T + 4 * p, 320);
                    const int cgk = min(k / 28, 11), idx = k - min(28 * cgk, 296);
                    const u32x2 bfr = trread(smem + L_X + j * 10752 + (kap * 12 + cgk) * 112 + pl * 56 + idx * 2);
                    const f32x4 d = mfma16k16(fa, bfr, f32x4{0.f, 0.f, 0.f, 0.f});
                    *(u32x2*)(smem + L_S + j * 10752 + (16 * T + i16) * 32 + g * 8) = u32x2{pk(d[0], d[1]), pk(d[2], d[3])};
                }
            }
        }
    }
    __syncthreads();
#pragma unroll 1
    for (int task = w; task < 4 * nset; task += 8) {
        const int j = task >> 2, wp = task & 3;
        const char* sb = smem + L_S + j * 10752 + wp * 4;
        const int k2 = i16 <= 10 ? i16 : 10;
        u32 dre[8], dim[8];
#pragma unroll
        for (int e = 0; e < 8; ++e) {
            const int k = k2 + 20 * (8 * g + e);
            const int kk = (g < 2) ? k : 640 - k;
            dre[e] = *(const u32*)(sb + kk * 32);
            dim[e] = *(const u32*)(sb + kk * 32 + 16);
        }
#pragma unroll
        for (int rr = 0; rr < 2; ++rr) {
            u32x4 are, aim;
            if (rr == 0) {
                are = u32x4{permlo(dre[0], dre[1]), permlo(dre[2], dre[3]), permlo(dre[4], dre[5]), permlo(dre[6], dre[7])};
                aim = u32x4{permlo(dim[0], dim[1]), permlo(dim[2], dim[3]), permlo(dim[4], dim[5]), permlo(dim[6], dim[7])};
            } else {
                are = u32x4{permhi(dre[0], dre[1]), permhi(dre[2], dre[3]), permhi(dre[4], dre[5]), permhi(dre[6], dre[7])};
                aim = u32x4{permhi(dim[0], dim[1]), permhi(dim[2], dim[3]), permhi(dim[4], dim[5]), permhi(dim[6], dim[7])};
            }
            u32x4 va[2];
            const f32x4 zero = {0.f, 0.f, 0.f, 0.f};
#pragma unroll
            for (int tau = 0; tau < 2; ++tau) {
                const f32x4 vre = mfma16(aim, nsS[tau], mfma16(are, C[tau], zero));
                const f32x4 vim = mfma16(aim, sC[tau], mfma16(are, S[tau], zero));
                float pr[8];
#pragma unroll
                for (int i = 0; i < 4; ++i) {
                    pr[i] = vre[i] * twc[4 * tau + i] - vim[i] * tws[4 * tau + i];
                    pr[4 + i] = vim[i] * twc[4 * tau + i] + vre[i] * tws[4 * tau + i];
                }
                va[tau] = pk8(pr);
            }
            u16* orow = P.out + (size_t)(ai + 96 * j + 256 * (2 * wp + rr)) * 1280;
#pragma unroll
            for (int dl = 0; dl < 2; ++dl) {
                const f32x4 y0 = mfma16(va[0], F[dl], zero);
                const f32x4 y1 = mfma16(va[1], F[dl], zero);
                u32 a0 = pk(y0[0], y0[1]), a1 = pk(y0[2], y0[3]);
                u32 b0 = pk(y1[0], y1[1]), b1 = pk(y1[2], y1[3]);
                auto s0 = __builtin_amdgcn_permlane16_swap(a0, b0, false, false);
                auto s1 = __builtin_amdgcn_permlane16_swap(a1, b1, false, false);
                const int d2 = 16 * dl + i16;
                if (d2 < 20) *(u32x4*)(orow + 32 * d2 + (g & 1) * 16 + (g >> 1) * 8) = u32x4{s0[0], s1[0], s0[1], s1[1]};
            }
        }
    }
    STAMP();
    STAMP_OUT(P.ws);
}

DI void role_b(const Args& P, char* smem, const int t, const int w, const int xcd, const int jx, const u32 tag) {
    const int l = t & 63, g = l >> 4, i16 = l & 15;
    const int chb = 20 * xcd + jx;
    const rsrc_t rws = mkrs(P.ws, WS_USED);
    STAMP_DECL STAMP();
    if (t == 0) *(u32*)(smem + L_CNT) = 0u;
    __syncthreads();
    u32x2 gv[8];
    u32 pw0 = 0, pw1 = 0;
    if (w >= 4) {
        const int tt = t - 256;
        const rsrc_t ru = mkrs(P.u, 2048 * 1280);
        {
            const int b0 = (160 * xcd) & ~127, nl = (160 * xcd + 160 - b0 + 127) >> 7;
            const int m0 = tt, m1 = min(tt + 256, 103 * 3 - 1);
            pw0 = __builtin_amdgcn_raw_buffer_load_b32(ru, min(103 * jx + m0 / 3, 2047) * 1280 + b0 + min(m0 % 3, nl - 1) * 128, 0, 0);
            pw1 = __builtin_amdgcn_raw_buffer_load_b32(ru, min(103 * jx + m1 / 3, 2047) * 1280 + b0 + min(m1 % 3, nl - 1) * 128, 0, 0);
        }
#pragma unroll
        for (int e = 0; e < 8; ++e) gv[e] = ld8(ru, (8 * tt + e) * 1280 + chb * 8, 0);
        fence();
        if (tt < 128) *(u32x4*)(smem + L_U + (tt >> 5) * PB_U + (tt & 31) * 16) = u32x4{0u, 0u, 0u, 0u};
        else if (tt < 144) {
            const int v = tt - 128;
            *(u32x4*)(smem + L_K + (v >> 2) * PB_KS + ((v >> 1) & 1) * PB_K1 + 4096 + (v & 1) * 16) = u32x4{0u, 0u, 0u, 0u};
        }
        u32x4 o[4];
        tr8x4(gv, o);
#pragma unroll
        for (int c = 0; c < 4; ++c) *(u32x4*)(smem + L_U + c * PB_U + 512 + tt * 16) = o[c];
        asm volatile("" :: "v"(pw0), "v"(pw1));
        STAMP();
    } else {
        const int ch = 4 * chb + w;
        u32 pv[12];
        {
            const u32 nb = 640 * 128 * 2;
            const rsrc_t r0 = mkrs(P.Are, nb), r1 = mkrs(P.Aim, nb), r2 = mkrs(P.Bre, nb), r3 = mkrs(P.Bim, nb), r4 = mkrs(P.Cre, nb), r5 = mkrs(P.Cim, nb);
#pragma unroll
            for (int h = 0; h < 2; ++h) {
                const u32 off = (ch * 128 + 64 * h + l) * 2;
                pv[6 * h + 0] = ld2(r0, off); pv[6 * h + 1] = ld2(r1, off); pv[6 * h + 2] = ld2(r2, off);
                pv[6 * h + 3] = ld2(r3, off); pv[6 * h + 4] = ld2(r4, off); pv[6 * h + 5] = ld2(r5, off);
            }
        }
        const u32 ldt = ld2(mkrs(P.logdt, 1280), ch * 2);
        {
            const u32x4 s4 = {SENT, SENT, SENT, SENT};
#pragma unroll
            for (int i = 0; i < 5; ++i) st16sc(rws, OFF_Y1 + min(chb * 1076 + min(t + 256 * i, 1075), 172031) * 16, s4);
        }
        fence();
        char* sc = smem + L_G + w * KG_SZ;
        const float dt = rbf(expf(bf2f(ldt)));
        f32x4 kacc[3];
#pragma unroll
        for (int T = 0; T < 3; ++T) kacc[T] = f32x4{0.f, 0.f, 0.f, 0.f};
#pragma unroll 1
        for (int h = 0; h < 2; ++h) {
            const float are = bf2f(h ? pv[6] : pv[0]), aim = bf2f(h ? pv[7] : pv[1]), bre = bf2f(h ? pv[8] : pv[2]);
            const float bim = bf2f(h ? pv[9] : pv[3]), cre = bf2f(h ? pv[10] : pv[4]), cim = bf2f(h ? pv[11] : pv[5]);
            const float lre = dt * are, lim = dt * aim * 0.15915494309189535f;
            const float dbr = dt * bre, dbi = dt * bim;
            const cf c = {cre * dbr - cim * dbi, cre * dbi + cim * dbr};
            char* rowp = sc + l * 4;
            const cf z1 = cexpk(lre, lim, 1.f), z8 = cexpk(lre, lim, 8.f), z128 = cexpk(lre, lim, 128.f);
            const cf z2 = cmul(z1, z1), z4 = cmul(z2, z2), z6 = cmul(z2, z4);
            cf v = {1.f, 0.f}, r0 = c, r1 = cmul(c, z128);
            cf q = cmul(r1, z128);
#pragma unroll
            for (int j = 0; j < 8; ++j) {
                *(u32*)(rowp + KG_B + (2 * j) * KG_RS) = pk(v.re, v.im);
                v = cmul(v, z8);
                *(u32*)(rowp + KG_B + (2 * j + 1) * KG_RS) = pk(v.re, v.im);
                v = cmul(v, z8);
                *(u32*)(rowp + j * KG_RS) = pk(r0.re, -r0.im);
                *(u32*)(rowp + (8 + j) * KG_RS) = pk(r1.re, -r1.im);
                r0 = cmul(r0, z1);
                r1 = cmul(r1, z1);
#pragma unroll
                for (int e = 0; e < 2; ++e) {
                    const int hh = 2 + 2 * j + e;
                    if (hh < 4) {
                        const int r = 16 + (hh - 2) * 4;
                        const cf a = cmul(q, z2), bq = cmul(q, z4), cq = cmul(q, z6);
                        *(u32*)(rowp + r * KG_RS) = pk(q.re, -q.im);
                        *(u32*)(rowp + (r + 1) * KG_RS) = pk(a.re, -a.im);
                        *(u32*)(rowp + (r + 2) * KG_RS) = pk(bq.re, -bq.im);
                        *(u32*)(rowp + (r + 3) * KG_RS) = pk(cq.re, -cq.im);
                    } else if (hh < 8) {
                        const int r = 24 + (hh - 4) * 2;
                        const cf bq = cmul(q, z4);
                        *(u32*)(rowp + r * KG_RS) = pk(q.re, -q.im);
                        *(u32*)(rowp + (r + 1) * KG_RS) = pk(bq.re, -bq.im);
                    } else if (hh <= 16) {
                        *(u32*)(rowp + (32 + hh - 8) * KG_RS) = pk(q.re, -q.im);
                    }
                    if (hh < 16) q = cmul(q, z128);
                }
            }
            const char* bp = sc + KG_B + i16 * KG_RS + g * 16;
#pragma unroll
            for (int ks = 0; ks < 4; ++ks) {
                const u32x4 bf = *(const u32x4*)(bp + ks * 64);
#pragma unroll
                for (int T = 0; T < 3; ++T)
                    kacc[T] = mfma16(*(const u32x4*)(sc + (16 * T + i16) * KG_RS + g * 16 + ks * 64), bf, kacc[T]);
            }
        }
#pragma unroll
        for (int T = 0; T < 3; ++T)
#pragma unroll
            for (int i = 0; i < 4; ++i) {
                const int row = 16 * T + 4 * g + i;
                int e;
                if (T == 0) e = 128 * (row >> 3) + (row & 7);
                else if (T == 1) e = (row < 24) ? 256 + 128 * ((row - 16) >> 2) + 2 * ((row - 16) & 3) : 512 + 128 * ((row - 24) >> 1) + 4 * ((row - 24) & 1);
                else e = 1024 + 128 * (row - 32);
                if (row < 40 || (row == 40 && i16 == 0))
                    *(u16*)(sc + KG_T + (e + 8 * i16) * 2) = (u16)(pk(kacc[T][i], kacc[T][i]) & 0xffffu);
            }
        drain();
        if (l == 0) {
            const u32 old = __hip_atomic_fetch_add((LAS u32*)(smem + L_CNT), 1u, __ATOMIC_RELAXED, __HIP_MEMORY_SCOPE_WORKGROUP);
            if (old == 3u) __hip_atomic_store((GAS u32*)(P.ws + OFF_SYNC + 2048) + chb, tag, __ATOMIC_RELAXED, __HIP_MEMORY_SCOPE_AGENT);
        }
#pragma unroll 1
        for (int it = 0; it < 4; ++it) {
            const int tt = l + 64 * it, a = 255 - tt;
            u32 code;
            if (a < 32) code = 0x76543210u;
            else if (a < 64) code = 0x86444200u;
            else if (a < 128) code = 0x88444000u;
            else code = (a & 1) ? 0x88880000u : 0x88800000u;
            const int lastoff = (a <= 32) ? 7 : 8;
            const char* tb = sc + KG_T + 16 * a;
            u32 v[8];
#pragma unroll
            for (int e = 0; e < 8; ++e) v[e] = *(const u16*)(tb + 2 * ((code >> (4 * e)) & 15));
            u32 vm = *(const u16*)(tb - 16 + 2 * lastoff);
            vm = (a == 0) ? 0u : vm;
            char* kb = smem + L_K + w * PB_KS + tt * 16;
            *(u32x4*)(kb) = u32x4{v[7] | (v[6] << 16), v[5] | (v[4] << 16), v[3] | (v[2] << 16), v[1] | (v[0] << 16)};
            *(u32x4*)(kb + PB_K1) = u32x4{v[6] | (v[5] << 16), v[4] | (v[3] << 16), v[2] | (v[1] << 16), v[0] | (vm << 16)};
        }
        STAMP();
    }
    const int ch = w & 3, rh = w >> 2;
    const float dsk = bf2f(ld2(mkrs(P.dskip, 1280), (4 * chb + ch) * 2));
    __syncthreads();
    STAMP();
    const int mu = g >> 1, hp = g & 1;
    const int x0 = 2047 - 16 * mu - i16 + 8 * hp;
    const char* ap = smem + L_K + ch * PB_KS + ((x0 & 1) ? PB_K1 + (x0 - 1) * 2 : x0 * 2);
    const char* bp = smem + L_U + ch * PB_U + (256 + 16 * (i16 - mu) + 8 * hp) * 2;
    f32x4 acc[8];
#pragma unroll
    for (int c = 0; c < 8; ++c) acc[c] = f32x4{0.f, 0.f, 0.f, 0.f};
#pragma unroll 1
    for (int r = 4 * rh; r < 4 * rh + 4; ++r) {
        u32x4 A[8], B[8];
#pragma unroll
        for (int p = 0; p < 8; ++p) {
            const u32* a = (const u32*)(ap - 64 * r - 512 * p);
            A[p] = u32x4{a[0], a[1], a[2], a[3]};
            B[p] = *(const u32x4*)(bp - 64 * r + 512 * p);
        }
#pragma unroll
        for (int p = 0; p < 8; ++p)
#pragma unroll
            for (int q = 0; q + p < 8; ++q) acc[p + q] = mfma16(A[p], B[q], acc[p + q]);
    }
    STAMP();
    char* rb = smem + L_G;
#pragma unroll
    for (int c = 0; c < 4; ++c) *(f32x4*)(rb + (w * 4 + c) * 1024 + l * 16) = rh ? acc[c] : acc[4 + c];
    __syncthreads();
    char* st = smem + L_G + 32768;
#pragma unroll
    for (int c = 0; c < 4; ++c) {
        const f32x4 o = *(const f32x4*)(rb + ((w ^ 4) * 4 + c) * 1024 + l * 16);
        const f32x4 mine = rh ? acc[4 + c] : acc[c];
        const int tt = 256 * (4 * rh + c) + 16 * i16 + 4 * g;
        const u32x2 uu = *(const u32x2*)(smem + L_U + ch * PB_U + (256 + tt) * 2);
#pragma unroll
        for (int i = 0; i < 4; ++i) {
            const float uf = (i & 1) ? bfhi(i < 2 ? uu.x : uu.y) : bflo(i < 2 ? uu.x : uu.y);
            const float v = rbf(mine[i] + o[i]) + rbf(uf * dsk);
            *(u16*)(st + (tt >> 4) * 128 + (((4 * g + i) ^ i16) * 8) + ch * 2) = (u16)(pk(v, v) & 0xffffu);
        }
    }
    __syncthreads();
    STAMP();
    {
        const rsrc_t ro = mkrs(P.out, 2048 * 2560);
#pragma unroll
        for (int i = 0; i < 4; ++i) {
            const int row = t + 512 * i;
            __builtin_amdgcn_raw_buffer_store_b64(*(const u32x2*)(st + (row >> 4) * 128 + (((row & 15) ^ ((row >> 4) & 15)) * 8)), ro,
                                                  row * 2560 + 1280 + chb * 8, 0, 0);
        }
    }
    STAMP();
    STAMP_OUT(P.ws);
}

__global__ void __launch_bounds__(512, 2) fused_kernel(Args P) {
    extern __shared__ __attribute__((aligned(16))) char smem[];
    const int t = threadIdx.x;
    const int w = __builtin_amdgcn_readfirstlane(t >> 6);
    const int b = blockIdx.x, xcd = b & 7, jx = b >> 3;
    const u32 tag = __hip_atomic_load((GAS u32*)(P.ws + OFF_SYNC), __ATOMIC_RELAXED, __HIP_MEMORY_SCOPE_AGENT) + 1u;
    if (jx < 20) role_b(P, smem, t, w, xcd, jx, tag);
    else role_a(P, smem, t, w, xcd, jx - 20, tag);
}

extern "C" void kernel_launch(void* const* d_in, const int* in_sizes, int n_in,
                              void* d_out, int out_size, void* d_ws, size_t ws_size,
                              hipStream_t stream) {
    Args a;
    a.x = (const u16*)d_in[0];
    a.wr = (const u16*)d_in[1];
    a.wi = (const u16*)d_in[2];
    a.u = (const u16*)d_in[3];
    a.Are = (const u16*)d_in[4];
    a.Aim = (const u16*)d_in[5];
    a.Bre = (const u16*)d_in[6];
    a.Bim = (const u16*)d_in[7];
    a.Cre = (const u16*)d_in[8];
    a.Cim = (const u16*)d_in[9];
    a.logdt = (const u16*)d_in[10];
    a.dskip = (const u16*)d_in[11];
    a.ws = (char*)d_ws;
    a.out = (u16*)d_out;
    (void)hipFuncSetAttribute((const void*)fused_kernel, hipFuncAttributeMaxDynamicSharedMemorySize, LDS_BYTES);
    fused_kernel<<<256, 512, LDS_BYTES, stream>>>(a);
}
```

```cpp
#include <hip/hip_runtime.h>
#include <hip/hip_bf16.h>

typedef unsigned short u16;
typedef unsigned int u32;
typedef __attribute__((ext_vector_type(2))) __bf16 bf16x2;
typedef __attribute__((ext_vector_type(8))) __bf16 bf16x8;
typedef __attribute__((ext_vector_type(2))) float f32x2;
typedef __attribute__((ext_vector_type(4))) float f32x4;
typedef __attribute__((ext_vector_type(4))) u32 u32x4;
typedef __attribute__((ext_vector_type(2))) u32 u32x2;
typedef __attribute__((ext_vector_type(4))) short s16x4;
typedef __amdgpu_buffer_rsrc_t rsrc_t;
#define LAS __attribute__((address_space(3)))
#define GAS __attribute__((address_space(1)))
#define DI __device__ __forceinline__
#define SC1 16

constexpr u32 X1_ROW = 10368;
constexpr u32 OFF_X1 = 0;
constexpr u32 Y1_BLK = 28672;
constexpr u32 OFF_Y1 = 256 * X1_ROW;
constexpr u32 OFF_SYNC = OFF_Y1 + 96 * Y1_BLK;
constexpr u32 WS_USED = OFF_SYNC + 4096;
constexpr u32 SENT = 0xFFFFFFFFu;
constexpr u32 SPIN_MAX = 1u << 16;
constexpr int CODE_BYTES = 25216;

constexpr int L_X = 0;
constexpr int L_S = 32256;
constexpr int L_W = 64512;
constexpr int L_C = L_W + 28672;
constexpr int PB_U = 4608;
constexpr int L_U = 0;
constexpr int L_K = L_U + 4 * PB_U;
constexpr int PB_K1 = 4160, PB_KS = 8320;
constexpr int L_G = L_K + 4 * PB_KS;
constexpr int KG_RS = 272, KG_B = 41 * KG_RS, KG_T = 57 * KG_RS, KG_SZ = 19616;
constexpr int L_CNT = L_G + 4 * KG_SZ;
constexpr int LDS_BYTES = L_CNT + 16;

#define STAMP_DECL
#define STAMP()
#define STAMP_OUT(ws)

DI u32 pk(float a, float b) {
    f32x2 v = {a, b};
    return __builtin_bit_cast(u32, __builtin_convertvector(v, bf16x2));
}
DI u32x4 pk8(const float* v) { return u32x4{pk(v[0], v[1]), pk(v[2], v[3]), pk(v[4], v[5]), pk(v[6], v[7])}; }
DI float bflo(u32 v) { return __uint_as_float(v << 16); }
DI float bfhi(u32 v) { return __uint_as_float(v & 0xffff0000u); }
DI float bf2f(u32 v) { return __uint_as_float(v << 16); }
DI float rbf(float a) { return bflo(pk(a, a)); }
DI float cosr(float rev) { return __builtin_amdgcn_cosf(rev); }
DI float sinr(float rev) { return __builtin_amdgcn_sinf(rev); }
DI f32x4 mfma16(u32x4 a, u32x4 b, f32x4 c) {
    return __builtin_amdgcn_mfma_f32_16x16x32_bf16(__builtin_bit_cast(bf16x8, a), __builtin_bit_cast(bf16x8, b), c, 0, 0, 0);
}
DI f32x4 mfma16k16(u32x2 a, u32x2 b, f32x4 c) {
    return __builtin_amdgcn_mfma_f32_16x16x16bf16_1k(__builtin_bit_cast(s16x4, a), __builtin_bit_cast(s16x4, b), c, 0, 0, 0);
}
DI u32x2 trread(const char* p) {
    return __builtin_bit_cast(u32x2, __builtin_amdgcn_ds_read_tr16_b64_v4i16((LAS s16x4*)(p)));
}
DI u32 permlo(u32 a, u32 b) { return __builtin_amdgcn_perm(b, a, 0x05040100u); }
DI u32 permhi(u32 a, u32 b) { return __builtin_amdgcn_perm(b, a, 0x07060302u); }
DI rsrc_t mkrs(const void* p, u32 bytes) { return __builtin_amdgcn_make_buffer_rsrc((void*)p, 0, bytes, 0x00020000); }
DI u32x4 ld16(rsrc_t r, u32 off, int aux) { return aux ? __builtin_amdgcn_raw_buffer_load_b128(r, off, 0, SC1) : __builtin_amdgcn_raw_buffer_load_b128(r, off, 0, 0); }
DI u32x2 ld8(rsrc_t r, u32 off, int aux) { return aux ? __builtin_amdgcn_raw_buffer_load_b64(r, off, 0, SC1) : __builtin_amdgcn_raw_buffer_load_b64(r, off, 0, 0); }
DI u32 ld2(rsrc_t r, u32 off) { return __builtin_amdgcn_raw_buffer_load_b16(r, off, 0, 0); }
DI void st16sc(rsrc_t r, u32 off, u32x4 v) { __builtin_amdgcn_raw_buffer_store_b128(v, r, off, 0, SC1); }
DI u32x4 sane(u32x4 v) { return u32x4{min(v.x, SENT - 1), min(v.y, SENT - 1), min(v.z, SENT - 1), min(v.w, SENT - 1)}; }
DI bool fresh4(u32x4 v) { return max(max(v.x, v.y), max(v.z, v.w)) != SENT; }
DI bool fresh2(u32x2 v) { return max(v.x, v.y) != SENT; }
DI void fence() { asm volatile("" ::: "memory"); }
DI void drain() { asm volatile("s_waitcnt vmcnt(0)" ::: "memory"); }
DI void tr8x4(const u32x2* v, u32x4* o) {
    o[0] = u32x4{permlo(v[0].x, v[1].x), permlo(v[2].x, v[3].x), permlo(v[4].x, v[5].x), permlo(v[6].x, v[7].x)};
    o[1] = u32x4{permhi(v[0].x, v[1].x), permhi(v[2].x, v[3].x), permhi(v[4].x, v[5].x), permhi(v[6].x, v[7].x)};
    o[2] = u32x4{permlo(v[0].y, v[1].y), permlo(v[2].y, v[3].y), permlo(v[4].y, v[5].y), permlo(v[6].y, v[7].y)};
    o[3] = u32x4{permhi(v[0].y, v[1].y), permhi(v[2].y, v[3].y), permhi(v[4].y, v[5].y), permhi(v[6].y, v[7].y)};
}
template <int NF>
DI void wait_flags(rsrc_t rws, u32 off, u32 tag, int w, int l) {
    if (w == 0) {
        const int q = min(l, NF / 4 - 1);
        for (u32 spins = 0; spins < SPIN_MAX; ++spins) {
            fence();
            const u32x4 f = ld16(rws, off + q * 16, 1);
            if (__all(f.x == tag && f.y == tag && f.z == tag && f.w == tag)) break;
            __builtin_amdgcn_s_sleep(2);
        }
    }
    __syncthreads();
}

struct cf { float re, im; };
DI cf cmul(cf a, cf b) { return cf{a.re * b.re - a.im * b.im, a.re * b.im + a.im * b.re}; }
DI cf cexpk(float lre, float lim_rev, float k) {
    const float m = __builtin_amdgcn_exp2f(k * lre * 1.44269504088896341f);
    const float a = __builtin_amdgcn_fractf(k * lim_rev);
    return cf{m * cosr(a), m * sinr(a)};
}

struct Args {
    const u16 *x, *wr, *wi, *u, *Are, *Aim, *Bre, *Bim, *Cre, *Cim, *logdt, *dskip;
    char* ws;
    u16* out;
};

DI void role_a(const Args& P, char* smem, const int t, const int w, const int ka, const int cg, const u32 tag) {
    const int l = t & 63, g = l >> 4, i16 = l & 15;
    const int ai = ka * 12 + cg, nset = ai < 64 ? 3 : 2;
    const int colbase = min(28 * cg, 296);
    const rsrc_t rws = mkrs(P.ws, WS_USED);
    STAMP_DECL STAMP();
    {
        const u32x4 s4 = {SENT, SENT, SENT, SENT};
#pragma unroll
        for (int i = 0; i < 4; ++i) {
            const int m = min(t + 512 * i, nset * 648 - 1), j = m / 648;
            st16sc(rws, OFF_X1 + (ai + 96 * j) * X1_ROW + (m - 648 * j) * 16, s4);
        }
    }
    u32x4 xv[2][3];
    {
        const rsrc_t rx = mkrs(P.x, 2048 * 1280);
#pragma unroll
        for (int it = 0; it < 2; ++it) {
            const int task = min(w + 8 * it, 4 * nset - 1), s0 = ai + 96 * (task >> 2);
#pragma unroll
            for (int i = 0; i < 3; ++i) {
                const int ci = min(l + 64 * i, 159), row = ci >= 80;
                xv[it][i] = ld16(rx, (s0 + 256 * (2 * (task & 3) + row)) * 1280 + (ci - 80 * row) * 16, 0);
            }
        }
    }
    fence();
    if (t < 90) *(u32x4*)(smem + L_S + (t / 30) * 10752 + 321 * 32 + (t % 30) * 16) = u32x4{0u, 0u, 0u, 0u};

    {
        u32x4 f20c, f20s, c32, s32;
        float twc[8], tws[8];
        {
            float c[8], s[8];
#pragma unroll
            for (int j = 0; j < 8; ++j) {
                const int d2 = (j < 4) ? 4 * g + j : 16 + 4 * g + j - 4;
                const bool ok = (d2 < 20) && (i16 <= 10);
                const float a = (float)d2 * (float)i16 * 0.05f;
                c[j] = ok ? cosr(a) : 0.f;
                s[j] = ok ? -sinr(a) : 0.f;
            }
            f20c = pk8(c); f20s = pk8(s);
#pragma unroll
            for (int j = 0; j < 8; ++j) {
                const int d1 = (j < 4) ? 4 * g + j : 16 + 4 * g + j - 4;
                const float pr = (float)d1 * (float)i16;
                c[j] = cosr(pr * 0.03125f);
                s[j] = sinr(pr * 0.03125f);
                twc[j] = cosr(pr * (1.0f / 640.0f));
                tws[j] = sinr(pr * (1.0f / 640.0f));
            }
            c32 = pk8(c); s32 = pk8(s);
        }
        const u32x4 ns32 = s32 ^ 0x80008000u, c32b = c32 ^ 0x80000000u, s32b = s32 ^ 0x80000000u, nc32b = c32b ^ 0x80008000u;
        const int q = i16 >> 2, p = i16 & 3;
        const int d2a = 4 * g + q, d2b = (g == 0) ? 16 + q : d2a;
        const int offa = 64 * d2a + 8 * p, swa = ((d2a >> 2) & 1) << 5;
        const int offb = 64 * d2b + 8 * p, swb = ((d2b >> 2) & 1) << 5;
#pragma unroll
        for (int it = 0; it < 2; ++it) {
            const int task = w + 8 * it;
            if (task < 4 * nset) {
                char* xs = smem + L_X + task * 2560;
#pragma unroll
                for (int i = 0; i < 3; ++i) {
                    const int ci = min(l + 64 * i, 159), c = ci >= 80 ? ci - 80 : ci;
                    *(u32x4*)(xs + ((ci * 16) ^ (((c >> 4) & 1) << 5))) = xv[it][i];
                }
                f32x4 z[2][4];
#pragma unroll
                for (int rr = 0; rr < 2; ++rr) {
                    const char* xr = xs + rr * 1280;
                    f32x4 tre[2], tim[2];
                    const f32x4 zero = {0.f, 0.f, 0.f, 0.f};
#pragma unroll
                    for (int tau = 0; tau < 2; ++tau) {
                        const u32x2 a0 = trread(xr + offa + ((32 * tau) ^ swa));
                        const u32x2 a1 = trread(xr + offb + ((32 * tau) ^ swb));
                        const u32x4 a = {a0.x, a0.y, a1.x, a1.y};
                        tre[tau] = mfma16(a, f20c, zero);
                        tim[tau] = mfma16(a, f20s, zero);
                    }
                    float pr[8], pi[8];
#pragma unroll
                    for (int j = 0; j < 8; ++j) {
                        const float a = tre[j >> 2][j & 3], bb = tim[j >> 2][j & 3];
                        pr[j] = a * twc[j] + bb * tws[j];
                        pi[j] = bb * twc[j] - a * tws[j];
                    }
                    const u32x4 bre = pk8(pr), bim = pk8(pi);
                    z[rr][0] = mfma16(s32, bim, mfma16(c32, bre, zero));
                    z[rr][1] = mfma16(ns32, bre, mfma16(c32, bim, zero));
                    z[rr][2] = mfma16(s32b, bim, mfma16(c32b, bre, zero));
                    z[rr][3] = mfma16(s32b, bre, mfma16(nc32b, bim, zero));
                }
                char* st = smem + L_S + (task >> 2) * 10752 + (task & 3) * 4;
                if (i16 <= 10) {
#pragma unroll
                    for (int i = 0; i < 4; ++i) {
                        const int k = i16 + 20 * (4 * g + i);
                        *(u32*)(st + k * 32) = pk(z[0][0][i], z[1][0][i]);
                        *(u32*)(st + k * 32 + 16) = pk(z[0][1][i], z[1][1][i]);
                    }
#pragma unroll
                    for (int i = 0; i < 4; ++i) {
                        const int k = 320 - i16 - 20 * (4 * g + i);
                        const bool ok = (i16 >= 1 && i16 <= 9) || (i16 == 0 && g == 0 && i == 0);
                        if (ok) {
                            *(u32*)(st + k * 32) = pk(z[0][2][i], z[1][2][i]);
                            *(u32*)(st + k * 32 + 16) = pk(z[0][3][i], z[1][3][i]);
                        }
                    }
                }
            }
        }
        drain();
    }
    STAMP();
    __syncthreads();
    if (t == 0) __hip_atomic_store((GAS u32*)(P.ws + OFF_SYNC + 1024) + ai, tag, __ATOMIC_RELAXED, __HIP_MEMORY_SCOPE_AGENT);
    {
        const int kap = i16 >> 1, plo = i16 & 1, pl = g >> 1;
#pragma unroll 1
        for (int j = 0; j < nset; ++j) {
            const int s0 = ai + 96 * j;
            float f[4];
#pragma unroll
            for (int e = 0; e < 4; ++e) {
                const int a = 4 * (g & 1) + e;
                const float th = (float)kap * (float)(256 * a + s0) * (1.0f / 2048.0f) - ((pl != plo) ? 0.25f : 0.f);
                const float v = cosr(th);
                f[e] = (plo == 1 && pl == 0) ? -v : v;
            }
            const u32x2 fb = {pk(f[0], f[1]), pk(f[2], f[3])};
#pragma unroll
            for (int it = 0; it < 3; ++it) {
                const int T = w + 8 * it;
                if (T < 21) {
                    const u32x2 a = *(const u32x2*)(smem + L_S + j * 10752 + (16 * T + i16) * 32 + g * 8);
                    const f32x4 d = mfma16k16(a, fb, f32x4{0.f, 0.f, 0.f, 0.f});
                    if (T < 20 || g == 0)
                        *(u32x2*)(smem + L_X + j * 10368 + i16 * 648 + (16 * T + 4 * g) * 2) = u32x2{pk(d[0], d[1]), pk(d[2], d[3])};
                }
            }
        }
    }
    __syncthreads();
#pragma unroll
    for (int i = 0; i < 4; ++i) {
        const int m = min(t + 512 * i, nset * 648 - 1), j = m / 648;
        st16sc(rws, OFF_X1 + (ai + 96 * j) * X1_ROW + (m - 648 * j) * 16, sane(*(const u32x4*)(smem + L_X + m * 16)));
    }
    STAMP();
    u32x2 wv[8];
    const int wpl = w >> 2, wtask = min(t & 255, 223), wquad = wtask % 7, wr_ = wtask / 7, wk1 = wr_ & 15, woc = wr_ >> 4;
    {
        const rsrc_t rw = mkrs(wpl ? P.wi : P.wr, 2048 * 642);
        const int cs = min(colbase + 4 * wquad, 317);
#pragma unroll
        for (int e = 0; e < 8; ++e) wv[e] = ld8(rw, ((ka + 8 * (wk1 + 16 * (8 * woc + e))) * 321 + cs) * 2, 0);
    }
    fence();
    u32x4 f1re, f1im, f2re, f2im;
    float t2c[4], t2s[4];
    {
        float c[8], s[8];
#pragma unroll
        for (int j = 0; j < 8; ++j) {
            const float a = (float)(8 * (g & 1) + j) * (float)i16 * 0.0625f;
            const float co = cosr(a), si = sinr(a);
            c[j] = (g < 2) ? co : si;
            s[j] = (g < 2) ? -si : co;
        }
        f1re = pk8(c); f1im = pk8(s);
#pragma unroll
        for (int j = 0; j < 4; ++j) {
            const float pr = (float)(4 * g + j) * (float)i16;
            const float co = cosr(pr * 0.0625f), si = sinr(pr * 0.0625f);
            c[j] = co; c[4 + j] = si;
            s[j] = -si; s[4 + j] = co;
            t2c[j] = cosr(pr * (1.0f / 256.0f));
            t2s[j] = sinr(pr * (1.0f / 256.0f));
        }
        f2re = pk8(c); f2im = pk8(s);
    }
    const u32x4 ire = {f2re.x, f2re.y, f2re.z ^ 0x80008000u, f2re.w ^ 0x80008000u};
    const u32x4 iim = {f2im.x ^ 0x80008000u, f2im.y ^ 0x80008000u, f2im.z, f2im.w};
    wait_flags<96>(rws, OFF_SYNC + 1024, tag, w, l);
    STAMP();
    {
        const int task = min(t, 447), quad = task % 7, r = task / 7, pl = r >> 5, oc = r & 31;
        const u32 base = OFF_X1 + (8 * oc) * X1_ROW + (ka * 2 + pl) * 648 + (colbase + 4 * quad) * 2;
        u32x2 v[8];
        for (u32 spins = 0; spins < SPIN_MAX; ++spins) {
            fence();
            bool ok = true;
#pragma unroll
            for (int e = 0; e < 8; ++e) v[e] = ld8(rws, base + e * X1_ROW, 1);
#pragma unroll
            for (int e = 0; e < 8; ++e) ok &= fresh2(v[e]);
            if (__all(ok)) break;
            __builtin_amdgcn_s_sleep(1);
        }
        u32x4 o[4];
        tr8x4(v, o);
#pragma unroll
        for (int c = 0; c < 4; ++c) {
            const int m = (4 * quad + c) * 2 + pl;
            *(u32x4*)(smem + L_C + m * 512 + ((oc ^ ((m >> 3) & 7)) * 16)) = o[c];
        }
    }
    {
        if (colbase + 4 * wquad > 317) {
#pragma unroll
            for (int e = 0; e < 8; ++e) wv[e] = u32x2{wv[e].y >> 16, 0u};
        }
        u32x4 o[4];
        tr8x4(wv, o);
#pragma unroll
        for (int c = 0; c < 4; ++c) {
            const int m = (4 * wquad + c) * 2 + wpl;
            *(u32x4*)(smem + L_W + m * 512 + (((wk1 * 2 + woc) ^ ((m >> 3) & 7)) * 16)) = o[c];
        }
    }
    STAMP();
    __syncthreads();
    {
        const int q = i16 >> 2, p = i16 & 3;
#pragma unroll 1
        for (int col = w; col < 28; col += 8) {
            const f32x4 zero = {0.f, 0.f, 0.f, 0.f};
            const int mi = col * 2 + (g >> 1), swi = (mi >> 3) & 7;
            const char* ib = smem + L_C + mi * 512 + (p & 1) * 8;
            const u32x2 a0 = trread(ib + (((2 * (8 * (g & 1) + q) + (p >> 1)) ^ swi) * 16));
            const u32x2 a1 = trread(ib + (((2 * (8 * (g & 1) + 4 + q) + (p >> 1)) ^ swi) * 16));
            const u32x4 a = {a0.x, a0.y, a1.x, a1.y};
            const int m0 = col * 2, sw = (m0 >> 3) & 7;
            const int woff = (((i16 * 2 + (g >> 1)) ^ sw) * 16) + (g & 1) * 8;
            const u32x2 wre = *(const u32x2*)(smem + L_W + m0 * 512 + woff);
            const u32x2 wim = *(const u32x2*)(smem + L_W + (m0 + 1) * 512 + woff);
            f32x4 re = mfma16(a, f1re, zero), im = mfma16(a, f1im, zero);
            float x[8];
#pragma unroll
            for (int i = 0; i < 4; ++i) {
                x[i] = re[i] * t2c[i] + im[i] * t2s[i];
                x[4 + i] = im[i] * t2c[i] - re[i] * t2s[i];
            }
            u32x4 d = pk8(x);
            re = mfma16(f2re, d, zero); im = mfma16(f2im, d, zero);
#pragma unroll
            for (int i = 0; i < 4; ++i) {
                const float a_ = (i & 1) ? bfhi(wre[i >> 1]) : bflo(wre[i >> 1]);
                const float b_ = (i & 1) ? bfhi(wim[i >> 1]) : bflo(wim[i >> 1]);
                x[i] = re[i] * a_ - im[i] * b_;
                x[4 + i] = re[i] * b_ + im[i] * a_;
            }
            d = pk8(x);
            re = mfma16(d, ire, zero); im = mfma16(d, iim, zero);
#pragma unroll
            for (int i = 0; i < 4; ++i) {
                x[i] = re[i] * t2c[i] - im[i] * t2s[i];
                x[4 + i] = im[i] * t2c[i] + re[i] * t2s[i];
            }
            d = pk8(x);
            re = mfma16(d, ire, zero); im = mfma16(d, iim, zero);
            const int ooff = (((2 * i16 + (g >> 1)) ^ sw) * 16) + (g & 1) * 8;
            *(u32x2*)(smem + L_C + m0 * 512 + ooff) = u32x2{pk(re[0], re[1]), pk(re[2], re[3])};
            *(u32x2*)(smem + L_C + (m0 + 1) * 512 + ooff) = u32x2{pk(im[0], im[1]), pk(im[2], im[3])};
        }
    }
    STAMP();
    wait_flags<160>(rws, OFF_SYNC + 2048, tag, w, l);
    if (ai == 0 && t == 0) __hip_atomic_store((GAS u32*)(P.ws + OFF_SYNC), tag, __ATOMIC_RELAXED, __HIP_MEMORY_SCOPE_AGENT);
    {
        const int q = i16 >> 2, p = i16 & 3;
#pragma unroll
        for (int it = 0; it < 7; ++it) {
            const int cmb = (w * 7 + it) * 4 + g;
            const int quad = cmb % 7, r = cmb / 7, pl = r & 1, G = r >> 1;
            const int m = (4 * quad + q) * 2 + pl;
            const u32x2 v = trread(smem + L_C + m * 512 + (((2 * G + (p >> 1)) ^ ((m >> 3) & 7)) * 16) + (p & 1) * 8);
            *(u32x2*)(smem + L_W + (16 * G + i16) * 112 + pl * 56 + quad * 8) = v;
        }
    }
    __syncthreads();
#pragma unroll
    for (int i = 0; i < 4; ++i) {
        const int m = min(t + 512 * i, 1791);
        st16sc(rws, OFF_Y1 + ai * Y1_BLK + m * 16, sane(*(const u32x4*)(smem + L_W + m * 16)));
    }
    STAMP();

    u32x4 C[2], S[2], nsS[2], sC[2], F[2];
    float twc[8], tws[8];
    {
        const u32 sgn = (g >= 2) ? 0x80008000u : 0u;
#pragma unroll
        for (int tau = 0; tau < 2; ++tau) {
            float c[8], s[8];
            const float d1 = (float)(16 * tau + i16);
#pragma unroll
            for (int j = 0; j < 8; ++j) {
                const float a = (float)(8 * g + j) * d1 * 0.03125f;
                c[j] = cosr(a);
                s[j] = sinr(a);
            }
            C[tau] = pk8(c); S[tau] = pk8(s);
            nsS[tau] = S[tau] ^ (sgn ^ 0x80008000u);
            sC[tau] = C[tau] ^ sgn;
#pragma unroll
            for (int i = 0; i < 4; ++i) {
                const float a = d1 * (float)(4 * g + i) * (1.0f / 640.0f);
                twc[4 * tau + i] = cosr(a) * (1.0f / (2048.0f * 640.0f));
                tws[4 * tau + i] = sinr(a) * (1.0f / (2048.0f * 640.0f));
            }
        }
#pragma unroll
        for (int dl = 0; dl < 2; ++dl) {
            float v[8];
#pragma unroll
            for (int j = 0; j < 8; ++j) {
                const int k2 = 4 * g + (j & 3), d2 = 16 * dl + i16;
                const float wgt = (k2 > 10 || d2 >= 20) ? 0.f : ((k2 == 0 || k2 == 10) ? 1.f : 2.f);
                const float a = (float)k2 * (float)d2 * 0.05f;
                v[j] = (j < 4) ? wgt * cosr(a) : -wgt * sinr(a);
            }
            F[dl] = pk8(v);
        }
    }
    {
        if (w == 0) {
            for (u32 spins = 0; spins < SPIN_MAX; ++spins) {
                fence();
                const u32x4 f = ld16(rws, OFF_Y1 + (l + l / 2) * Y1_BLK + ai * 112 + 96, 1);
                if (__all(fresh4(f))) break;
                __builtin_amdgcn_s_sleep(1);
            }
        }
        __syncthreads();
        STAMP();
        u32 off[4];
#pragma unroll
        for (int i = 0; i < 4; ++i) {
            const int m = min(t + 512 * i, nset * 672 - 1), j = m / 672, mm = m - 672 * j, ab = mm / 7;
            off[i] = OFF_Y1 + ab * Y1_BLK + (ai + 96 * j) * 112 + (mm - 7 * ab) * 16;
        }
        u32x4 v[4];
        for (u32 spins = 0; spins < SPIN_MAX; ++spins) {
            fence();
#pragma unroll
            for (int i = 0; i < 4; ++i) v[i] = ld16(rws, off[i], 1);
            if (__all(fresh4(v[0]) && fresh4(v[1]) && fresh4(v[2]) && fresh4(v[3]))) break;
            __builtin_amdgcn_s_sleep(1);
        }
#pragma unroll
        for (int i = 0; i < 4; ++i) *(u32x4*)(smem + L_X + min(t + 512 * i, nset * 672 - 1) * 16) = v[i];
    }
    STAMP();
    __syncthreads();
    {
        const int q = i16 >> 2, p = i16 & 3;
        const int kap = 2 * g + (q >> 1), pl = q & 1;
        const int plo = i16 >> 3, a = i16 & 7;
#pragma unroll 1
        for (int j = 0; j < nset; ++j) {
            const int s0 = ai + 96 * j;
            float f[4];
#pragma unroll
            for (int e = 0; e < 4; ++e) {
                const int kk = 2 * g + (e >> 1), pp = e & 1;
                const float th = (float)kk * (float)(256 * a + s0) * (1.0f / 2048.0f) - ((pp != plo) ? 0.25f : 0.f);
                const float v = cosr(th);
                f[e] = (plo == 0 && pp == 1) ? -v : v;
            }
            const u32x2 fa = {pk(f[0], f[1]), pk(f[2], f[3])};
#pragma unroll
            for (int it = 0; it < 3; ++it) {
                const int T = w + 8 * it;
                if (T < 21) {
                    const int k = min(16 * T + 4 * p, 320);
                    const int cgk = min(k / 28, 11), idx = k - min(28 * cgk, 296);
                    const u32x2 bfr = trread(smem + L_X + j * 10752 + (kap * 12 + cgk) * 112 + pl * 56 + idx * 2);
                    const f32x4 d = mfma16k16(fa, bfr, f32x4{0.f, 0.f, 0.f, 0.f});
                    *(u32x2*)(smem + L_S + j * 10752 + (16 * T + i16) * 32 + g * 8) = u32x2{pk(d[0], d[1]), pk(d[2], d[3])};
                }
            }
        }
    }
    __syncthreads();
#pragma unroll 1
    for (int task = w; task < 4 * nset; task += 8) {
        const int j = task >> 2, wp = task & 3;
        const char* sb = smem + L_S + j * 10752 + wp * 4;
        const int k2 = i16 <= 10 ? i16 : 10;
        u32 dre[8], dim[8];
#pragma unroll
        for (int e = 0; e < 8; ++e) {
            const int k = k2 + 20 * (8 * g + e);
            const int kk = (g < 2) ? k : 640 - k;
            dre[e] = *(const u32*)(sb + kk * 32);
            dim[e] = *(const u32*)(sb + kk * 32 + 16);
        }
#pragma unroll
        for (int rr = 0; rr < 2; ++rr) {
            u32x4 are, aim;
            if (rr == 0) {
                are = u32x4{permlo(dre[0], dre[1]), permlo(dre[2], dre[3]), permlo(dre[4], dre[5]), permlo(dre[6], dre[7])};
                aim = u32x4{permlo(dim[0], dim[1]), permlo(dim[2], dim[3]), permlo(dim[4], dim[5]), permlo(dim[6], dim[7])};
            } else {
                are = u32x4{permhi(dre[0], dre[1]), permhi(dre[2], dre[3]), permhi(dre[4], dre[5]), permhi(dre[6], dre[7])};
                aim = u32x4{permhi(dim[0], dim[1]), permhi(dim[2], dim[3]), permhi(dim[4], dim[5]), permhi(dim[6], dim[7])};
            }
            u32x4 va[2];
            const f32x4 zero = {0.f, 0.f, 0.f, 0.f};
#pragma unroll
            for (int tau = 0; tau < 2; ++tau) {
                const f32x4 vre = mfma16(aim, nsS[tau], mfma16(are, C[tau], zero));
                const f32x4 vim = mfma16(aim, sC[tau], mfma16(are, S[tau], zero));
                float pr[8];
#pragma unroll
                for (int i = 0; i < 4; ++i) {
                    pr[i] = vre[i] * twc[4 * tau + i] - vim[i] * tws[4 * tau + i];
                    pr[4 + i] = vim[i] * twc[4 * tau + i] + vre[i] * tws[4 * tau + i];
                }
                va[tau] = pk8(pr);
            }
            u16* orow = P.out + (size_t)(ai + 96 * j + 256 * (2 * wp + rr)) * 1280;
#pragma unroll
            for (int dl = 0; dl < 2; ++dl) {
                const f32x4 y0 = mfma16(va[0], F[dl], zero);
                const f32x4 y1 = mfma16(va[1], F[dl], zero);
                u32 a0 = pk(y0[0], y0[1]), a1 = pk(y0[2], y0[3]);
                u32 b0 = pk(y1[0], y1[1]), b1 = pk(y1[2], y1[3]);
                auto s0 = __builtin_amdgcn_permlane16_swap(a0, b0, false, false);
                auto s1 = __builtin_amdgcn_permlane16_swap(a1, b1, false, false);
                const int d2 = 16 * dl + i16;
                if (d2 < 20) *(u32x4*)(orow + 32 * d2 + (g & 1) * 16 + (g >> 1) * 8) = u32x4{s0[0], s1[0], s0[1], s1[1]};
            }
        }
    }
    STAMP();
    STAMP_OUT(P.ws);
}

DI void role_b(const Args& P, char* smem, const int t, const int w, const int xcd, const int jx, const u32 tag) {
    const int l = t & 63, g = l >> 4, i16 = l & 15;
    const int chb = 20 * xcd + jx;
    const rsrc_t rws = mkrs(P.ws, WS_USED);
    STAMP_DECL STAMP();
    if (t == 0) *(u32*)(smem + L_CNT) = 0u;
    __syncthreads();
    u32x2 gv[8];
    u32 pw0 = 0, pw1 = 0;
    if (w >= 4) {
        const int tt = t - 256;
        const rsrc_t ru = mkrs(P.u, 2048 * 1280);
        {
            const int b0 = (160 * xcd) & ~127, nl = (160 * xcd + 160 - b0 + 127) >> 7;
            const int m0 = tt, m1 = min(tt + 256, 103 * 3 - 1);
            pw0 = __builtin_amdgcn_raw_buffer_load_b32(ru, min(103 * jx + m0 / 3, 2047) * 1280 + b0 + min(m0 % 3, nl - 1) * 128, 0, 0);
            pw1 = __builtin_amdgcn_raw_buffer_load_b32(ru, min(103 * jx + m1 / 3, 2047) * 1280 + b0 + min(m1 % 3, nl - 1) * 128, 0, 0);
        }
#pragma unroll
        for (int e = 0; e < 8; ++e) gv[e] = ld8(ru, (8 * tt + e) * 1280 + chb * 8, 0);
        fence();
        if (tt < 128) *(u32x4*)(smem + L_U + (tt >> 5) * PB_U + (tt & 31) * 16) = u32x4{0u, 0u, 0u, 0u};
        else if (tt < 144) {
            const int v = tt - 128;
            *(u32x4*)(smem + L_K + (v >> 2) * PB_KS + ((v >> 1) & 1) * PB_K1 + 4096 + (v & 1) * 16) = u32x4{0u, 0u, 0u, 0u};
        }
        u32x4 o[4];
        tr8x4(gv, o);
#pragma unroll
        for (int c = 0; c < 4; ++c) *(u32x4*)(smem + L_U + c * PB_U + 512 + tt * 16) = o[c];
        asm volatile("" :: "v"(pw0), "v"(pw1));
        STAMP();
    } else {
        const int ch = 4 * chb + w;
        u32 pv[12];
        {
            const u32 nb = 640 * 128 * 2;
            const rsrc_t r0 = mkrs(P.Are, nb), r1 = mkrs(P.Aim, nb), r2 = mkrs(P.Bre, nb), r3 = mkrs(P.Bim, nb), r4 = mkrs(P.Cre, nb), r5 = mkrs(P.Cim, nb);
#pragma unroll
            for (int h = 0; h < 2; ++h) {
                const u32 off = (ch * 128 + 64 * h + l) * 2;
                pv[6 * h + 0] = ld2(r0, off); pv[6 * h + 1] = ld2(r1, off); pv[6 * h + 2] = ld2(r2, off);
                pv[6 * h + 3] = ld2(r3, off); pv[6 * h + 4] = ld2(r4, off); pv[6 * h + 5] = ld2(r5, off);
            }
        }
        const u32 ldt = ld2(mkrs(P.logdt, 1280), ch * 2);
        {
            const u32x4 s4 = {SENT, SENT, SENT, SENT};
#pragma unroll
            for (int i = 0; i < 5; ++i) st16sc(rws, OFF_Y1 + min(chb * 1076 + min(t + 256 * i, 1075), 172031) * 16, s4);
        }
        fence();
        char* sc = smem + L_G + w * KG_SZ;
        const float dt = rbf(expf(bf2f(ldt)));
        f32x4 kacc[3];
#pragma unroll
        for (int T = 0; T < 3; ++T) kacc[T] = f32x4{0.f, 0.f, 0.f, 0.f};
#pragma unroll 1
        for (int h = 0; h < 2; ++h) {
            const float are = bf2f(h ? pv[6] : pv[0]), aim = bf2f(h ? pv[7] : pv[1]), bre = bf2f(h ? pv[8] : pv[2]);
            const float bim = bf2f(h ? pv[9] : pv[3]), cre = bf2f(h ? pv[10] : pv[4]), cim = bf2f(h ? pv[11] : pv[5]);
            const float lre = dt * are, lim = dt * aim * 0.15915494309189535f;
            const float dbr = dt * bre, dbi = dt * bim;
            const cf c = {cre * dbr - cim * dbi, cre * dbi + cim * dbr};
            char* rowp = sc + l * 4;
            const cf z1 = cexpk(lre, lim, 1.f), z8 = cexpk(lre, lim, 8.f), z128 = cexpk(lre, lim, 128.f);
            const cf z2 = cmul(z1, z1), z4 = cmul(z2, z2), z6 = cmul(z2, z4);
            cf v = {1.f, 0.f}, r0 = c, r1 = cmul(c, z128);
            cf q = cmul(r1, z128);
#pragma unroll
            for (int j = 0; j < 8; ++j) {
                *(u32*)(rowp + KG_B + (2 * j) * KG_RS) = pk(v.re, v.im);
                v = cmul(v, z8);
                *(u32*)(rowp + KG_B + (2 * j + 1) * KG_RS) = pk(v.re, v.im);
                v = cmul(v, z8);
                *(u32*)(rowp + j * KG_RS) = pk(r0.re, -r0.im);
                *(u32*)(rowp + (8 + j) * KG_RS) = pk(r1.re, -r1.im);
                r0 = cmul(r0, z1);
                r1 = cmul(r1, z1);
#pragma unroll
                for (int e = 0; e < 2; ++e) {
                    const int hh = 2 + 2 * j + e;
                    if (hh < 4) {
                        const int r = 16 + (hh - 2) * 4;
                        const cf a = cmul(q, z2), bq = cmul(q, z4), cq = cmul(q, z6);
                        *(u32*)(rowp + r * KG_RS) = pk(q.re, -q.im);
                        *(u32*)(rowp + (r + 1) * KG_RS) = pk(a.re, -a.im);
                        *(u32*)(rowp + (r + 2) * KG_RS) = pk(bq.re, -bq.im);
                        *(u32*)(rowp + (r + 3) * KG_RS) = pk(cq.re, -cq.im);
                    } else if (hh < 8) {
                        const int r = 24 + (hh - 4) * 2;
                        const cf bq = cmul(q, z4);
                        *(u32*)(rowp + r * KG_RS) = pk(q.re, -q.im);
                        *(u32*)(rowp + (r + 1) * KG_RS) = pk(bq.re, -bq.im);
                    } else if (hh <= 16) {
                        *(u32*)(rowp + (32 + hh - 8) * KG_RS) = pk(q.re, -q.im);
                    }
                    if (hh < 16) q = cmul(q, z128);
                }
            }
            const char* bp = sc + KG_B + i16 * KG_RS + g * 16;
#pragma unroll
            for (int ks = 0; ks < 4; ++ks) {
                const u32x4 bf = *(const u32x4*)(bp + ks * 64);
#pragma unroll
                for (int T = 0; T < 3; ++T)
                    kacc[T] = mfma16(*(const u32x4*)(sc + (16 * T + i16) * KG_RS + g * 16 + ks * 64), bf, kacc[T]);
            }
        }
#pragma unroll
        for (int T = 0; T < 3; ++T)
#pragma unroll
            for (int i = 0; i < 4; ++i) {
                const int row = 16 * T + 4 * g + i;
                int e;
                if (T == 0) e = 128 * (row >> 3) + (row & 7);
                else if (T == 1) e = (row < 24) ? 256 + 128 * ((row - 16) >> 2) + 2 * ((row - 16) & 3) : 512 + 128 * ((row - 24) >> 1) + 4 * ((row - 24) & 1);
                else e = 1024 + 128 * (row - 32);
                if (row < 40 || (row == 40 && i16 == 0))
                    *(u16*)(sc + KG_T + (e + 8 * i16) * 2) = (u16)(pk(kacc[T][i], kacc[T][i]) & 0xffffu);
            }
        drain();
        if (l == 0) {
            const u32 old = __hip_atomic_fetch_add((LAS u32*)(smem + L_CNT), 1u, __ATOMIC_RELAXED, __HIP_MEMORY_SCOPE_WORKGROUP);
            if (old == 3u) __hip_atomic_store((GAS u32*)(P.ws + OFF_SYNC + 2048) + chb, tag, __ATOMIC_RELAXED, __HIP_MEMORY_SCOPE_AGENT);
        }
#pragma unroll 1
        for (int it = 0; it < 4; ++it) {
            const int tt = l + 64 * it, a = 255 - tt;
            u32 code;
            if (a < 32) code = 0x76543210u;
            else if (a < 64) code = 0x86444200u;
            else if (a < 128) code = 0x88444000u;
            else code = (a & 1) ? 0x88880000u : 0x88800000u;
            const int lastoff = (a <= 32) ? 7 : 8;
            const char* tb = sc + KG_T + 16 * a;
            u32 v[8];
#pragma unroll
            for (int e = 0; e < 8; ++e) v[e] = *(const u16*)(tb + 2 * ((code >> (4 * e)) & 15));
            u32 vm = *(const u16*)(tb - 16 + 2 * lastoff);
            vm = (a == 0) ? 0u : vm;
            char* kb = smem + L_K + w * PB_KS + tt * 16;
            *(u32x4*)(kb) = u32x4{v[7] | (v[6] << 16), v[5] | (v[4] << 16), v[3] | (v[2] << 16), v[1] | (v[0] << 16)};
            *(u32x4*)(kb + PB_K1) = u32x4{v[6] | (v[5] << 16), v[4] | (v[3] << 16), v[2] | (v[1] << 16), v[0] | (vm << 16)};
        }
        STAMP();
    }
    const int ch = w & 3, rh = w >> 2;
    const float dsk = bf2f(ld2(mkrs(P.dskip, 1280), (4 * chb + ch) * 2));
    __syncthreads();
    STAMP();
    const int mu = g >> 1, hp = g & 1;
    const int x0 = 2047 - 16 * mu - i16 + 8 * hp;
    const char* ap = smem + L_K + ch * PB_KS + ((x0 & 1) ? PB_K1 + (x0 - 1) * 2 : x0 * 2);
    const char* bp = smem + L_U + ch * PB_U + (256 + 16 * (i16 - mu) + 8 * hp) * 2;
    f32x4 acc[8];
#pragma unroll
    for (int c = 0; c < 8; ++c) acc[c] = f32x4{0.f, 0.f, 0.f, 0.f};
#pragma unroll 1
    for (int r = 4 * rh; r < 4 * rh + 4; ++r) {
        u32x4 A[8], B[8];
#pragma unroll
        for (int p = 0; p < 8; ++p) {
            const u32* a = (const u32*)(ap - 64 * r - 512 * p);
            A[p] = u32x4{a[0], a[1], a[2], a[3]};
            B[p] = *(const u32x4*)(bp - 64 * r + 512 * p);
        }
#pragma unroll
        for (int p = 0; p < 8; ++p)
#pragma unroll
            for (int q = 0; q + p < 8; ++q) acc[p + q] = mfma16(A[p], B[q], acc[p + q]);
    }
    STAMP();
    char* rb = smem + L_G;
#pragma unroll
    for (int c = 0; c < 4; ++c) *(f32x4*)(rb + (w * 4 + c) * 1024 + l * 16) = rh ? acc[c] : acc[4 + c];
    __syncthreads();
    char* st = smem + L_G + 32768;
#pragma unroll
    for (int c = 0; c < 4; ++c) {
        const f32x4 o = *(const f32x4*)(rb + ((w ^ 4) * 4 + c) * 1024 + l * 16);
        const f32x4 mine = rh ? acc[4 + c] : acc[c];
        const int tt = 256 * (4 * rh + c) + 16 * i16 + 4 * g;
        const u32x2 uu = *(const u32x2*)(smem + L_U + ch * PB_U + (256 + tt) * 2);
#pragma unroll
        for (int i = 0; i < 4; ++i) {
            const float uf = (i & 1) ? bfhi(i < 2 ? uu.x : uu.y) : bflo(i < 2 ? uu.x : uu.y);
            const float v = rbf(mine[i] + o[i]) + rbf(uf * dsk);
            *(u16*)(st + (tt >> 4) * 128 + (((4 * g + i) ^ i16) * 8) + ch * 2) = (u16)(pk(v, v) & 0xffffu);
        }
    }
    __syncthreads();
    STAMP();
    {
        const rsrc_t ro = mkrs(P.out, 2048 * 2560);
#pragma unroll
        for (int i = 0; i < 4; ++i) {
            const int row = t + 512 * i;
            __builtin_amdgcn_raw_buffer_store_b64(*(const u32x2*)(st + (row >> 4) * 128 + (((row & 15) ^ ((row >> 4) & 15)) * 8)), ro,
                                                  row * 2560 + 1280 + chb * 8, 0, 0);
        }
    }
    STAMP();
    STAMP_OUT(P.ws);
}

__global__ void __launch_bounds__(512, 2) fused_kernel(Args P) {
    extern __shared__ __attribute__((aligned(16))) char smem[];
    const int t = threadIdx.x;
    const int w = __builtin_amdgcn_readfirstlane(t >> 6);
    const int b = blockIdx.x, xcd = b & 7, jx = b >> 3;
    u32 pf = 0;
    if (w == 7) {
        unsigned long long pc;
        asm volatile("s_getpc_b64 %0" : "=s"(pc));
        const rsrc_t rc = mkrs((const void*)(pc & ~127ull), CODE_BYTES);
        const int l = t & 63;
#pragma unroll
        for (int i = 0; i < (CODE_BYTES / 128 + 63) / 64; ++i) pf ^= __builtin_amdgcn_raw_buffer_load_b32(rc, (l + 64 * i) * 128, 0, 0);
    }
    const u32 tag = __hip_atomic_load((GAS u32*)(P.ws + OFF_SYNC), __ATOMIC_RELAXED, __HIP_MEMORY_SCOPE_AGENT) + 1u;
    if (jx < 20) role_b(P, smem, t, w, xcd, jx, tag);
    else role_a(P, smem, t, w, xcd, jx - 20, tag);
    asm volatile("" :: "v"(pf));
}

extern "C" void kernel_launch(void* const* d_in, const int* in_sizes, int n_in,
                              void* d_out, int out_size, void* d_ws, size_t ws_size,
                              hipStream_t stream) {
    Args a;
    a.x = (const u16*)d_in[0];
    a.wr = (const u16*)d_in[1];
    a.wi = (const u16*)d_in[2];
    a.u = (const u16*)d_in[3];
    a.Are = (const u16*)d_in[4];
    a.Aim = (const u16*)d_in[5];
    a.Bre = (const u16*)d_in[6];
    a.Bim = (const u16*)d_in[7];
    a.Cre = (const u16*)d_in[8];
    a.Cim = (const u16*)d_in[9];
    a.logdt = (const u16*)d_in[10];
    a.dskip = (const u16*)d_in[11];
    a.ws = (char*)d_ws;
    a.out = (u16*)d_out;
    (void)hipFuncSetAttribute((const void*)fused_kernel, hipFuncAttributeMaxDynamicSharedMemorySize, LDS_BYTES);
    fused_kernel<<<256, 512, LDS_BYTES, stream>>>(a);
}
```

```cpp
#include <hip/hip_runtime.h>
#include <hip/hip_bf16.h>

typedef unsigned short u16;
typedef unsigned int u32;
typedef __attribute__((ext_vector_type(2))) __bf16 bf16x2;
typedef __attribute__((ext_vector_type(8))) __bf16 bf16x8;
typedef __attribute__((ext_vector_type(2))) float f32x2;
typedef __attribute__((ext_vector_type(4))) float f32x4;
typedef __attribute__((ext_vector_type(4))) u32 u32x4;
typedef __attribute__((ext_vector_type(2))) u32 u32x2;
typedef __attribute__((ext_vector_type(4))) short s16x4;
typedef __amdgpu_buffer_rsrc_t rsrc_t;
#define LAS __attribute__((address_space(3)))
#define GAS __attribute__((address_space(1)))
#define DI __device__ __forceinline__
#define SC1 16

constexpr u32 X1_ROW = 10368;
constexpr u32 OFF_X1 = 0;
constexpr u32 Y1_BLK = 28672;
constexpr u32 OFF_Y1 = 256 * X1_ROW;
constexpr u32 OFF_SYNC = OFF_Y1 + 96 * Y1_BLK;
constexpr u32 WS_USED = OFF_SYNC + 4096;
constexpr u32 SENT = 0xFFFFFFFFu;
constexpr u32 SPIN_MAX = 1u << 16;

constexpr int L_X = 0;
constexpr int L_S = 32256;
constexpr int L_W = 64512;
constexpr int L_C = L_W + 28672;
constexpr int PB_U = 4608;
constexpr int L_U = 0;
constexpr int L_K = L_U + 4 * PB_U;
constexpr int PB_K1 = 4160, PB_KS = 8320;
constexpr int L_G = L_K + 4 * PB_KS;
constexpr int KG_RS = 272, KG_B = 41 * KG_RS, KG_T = 57 * KG_RS, KG_SZ = 19616;
constexpr int L_CNT = L_G + 4 * KG_SZ;
constexpr int LDS_BYTES = L_CNT + 16;

#define STAMP_DECL
#define STAMP()
#define STAMP_OUT(ws)

DI u32 pk(float a, float b) {
    f32x2 v = {a, b};
    return __builtin_bit_cast(u32, __builtin_convertvector(v, bf16x2));
}
DI u32x4 pk8(const float* v) { return u32x4{pk(v[0], v[1]), pk(v[2], v[3]), pk(v[4], v[5]), pk(v[6], v[7])}; }
DI float bflo(u32 v) { return __uint_as_float(v << 16); }
DI float bfhi(u32 v) { return __uint_as_float(v & 0xffff0000u); }
DI float bf2f(u32 v) { return __uint_as_float(v << 16); }
DI float rbf(float a) { return bflo(pk(a, a)); }
DI float cosr(float rev) { return __builtin_amdgcn_cosf(rev); }
DI float sinr(float rev) { return __builtin_amdgcn_sinf(rev); }
DI f32x4 mfma16(u32x4 a, u32x4 b, f32x4 c) {
    return __builtin_amdgcn_mfma_f32_16x16x32_bf16(__builtin_bit_cast(bf16x8, a), __builtin_bit_cast(bf16x8, b), c, 0, 0, 0);
}
DI f32x4 mfma16k16(u32x2 a, u32x2 b, f32x4 c) {
    return __builtin_amdgcn_mfma_f32_16x16x16bf16_1k(__builtin_bit_cast(s16x4, a), __builtin_bit_cast(s16x4, b), c, 0, 0, 0);
}
DI u32x2 trread(const char* p) {
    return __builtin_bit_cast(u32x2, __builtin_amdgcn_ds_read_tr16_b64_v4i16((LAS s16x4*)(p)));
}
DI u32 permlo(u32 a, u32 b) { return __builtin_amdgcn_perm(b, a, 0x05040100u); }
DI u32 permhi(u32 a, u32 b) { return __builtin_amdgcn_perm(b, a, 0x07060302u); }
DI rsrc_t mkrs(const void* p, u32 bytes) { return __builtin_amdgcn_make_buffer_rsrc((void*)p, 0, bytes, 0x00020000); }
DI u32x4 ld16(rsrc_t r, u32 off, int aux) { return aux ? __builtin_amdgcn_raw_buffer_load_b128(r, off, 0, SC1) : __builtin_amdgcn_raw_buffer_load_b128(r, off, 0, 0); }
DI u32x2 ld8(rsrc_t r, u32 off, int aux) { return aux ? __builtin_amdgcn_raw_buffer_load_b64(r, off, 0, SC1) : __builtin_amdgcn_raw_buffer_load_b64(r, off, 0, 0); }
DI u32 ld2(rsrc_t r, u32 off) { return __builtin_amdgcn_raw_buffer_load_b16(r, off, 0, 0); }
DI void st16sc(rsrc_t r, u32 off, u32x4 v) { __builtin_amdgcn_raw_buffer_store_b128(v, r, off, 0, SC1); }
DI u32x4 sane(u32x4 v) { return u32x4{min(v.x, SENT - 1), min(v.y, SENT - 1), min(v.z, SENT - 1), min(v.w, SENT - 1)}; }
DI bool fresh4(u32x4 v) { return max(max(v.x, v.y), max(v.z, v.w)) != SENT; }
DI bool fresh2(u32x2 v) { return max(v.x, v.y) != SENT; }
DI void fence() { asm volatile("" ::: "memory"); }
DI void drain() { asm volatile("s_waitcnt vmcnt(0)" ::: "memory"); }
DI void tr8x4(const u32x2* v, u32x4* o) {
    o[0] = u32x4{permlo(v[0].x, v[1].x), permlo(v[2].x, v[3].x), permlo(v[4].x, v[5].x), permlo(v[6].x, v[7].x)};
    o[1] = u32x4{permhi(v[0].x, v[1].x), permhi(v[2].x, v[3].x), permhi(v[4].x, v[5].x), permhi(v[6].x, v[7].x)};
    o[2] = u32x4{permlo(v[0].y, v[1].y), permlo(v[2].y, v[3].y), permlo(v[4].y, v[5].y), permlo(v[6].y, v[7].y)};
    o[3] = u32x4{permhi(v[0].y, v[1].y), permhi(v[2].y, v[3].y), permhi(v[4].y, v[5].y), permhi(v[6].y, v[7].y)};
}
template <int NF>
DI void wait_flags(rsrc_t rws, u32 off, u32 tag, int w, int l) {
    if (w == 0) {
        const int q = min(l, NF / 4 - 1);
        for (u32 spins = 0; spins < SPIN_MAX; ++spins) {
            fence();
            const u32x4 f = ld16(rws, off + q * 16, 1);
            if (__all(f.x == tag && f.y == tag && f.z == tag && f.w == tag)) break;
            __builtin_amdgcn_s_sleep(2);
        }
    }
    __syncthreads();
}

struct cf { float re, im; };
DI cf cmul(cf a, cf b) { return cf{a.re * b.re - a.im * b.im, a.re * b.im + a.im * b.re}; }
DI cf cexpk(float lre, float lim_rev, float k) {
    const float m = __builtin_amdgcn_exp2f(k * lre * 1.44269504088896341f);
    const float a = __builtin_amdgcn_fractf(k * lim_rev);
    return cf{m * cosr(a), m * sinr(a)};
}

struct Args {
    const u16 *x, *wr, *wi, *u, *Are, *Aim, *Bre, *Bim, *Cre, *Cim, *logdt, *dskip;
    char* ws;
    u16* out;
};

DI void role_a(const Args& P, char* smem, const int t, const int w, const int ka, const int cg, const u32 tag) {
    const int l = t & 63, g = l >> 4, i16 = l & 15;
    const int ai = ka * 12 + cg, nset = ai < 64 ? 3 : 2;
    const int colbase = min(28 * cg, 296);
    const rsrc_t rws = mkrs(P.ws, WS_USED);
    STAMP_DECL STAMP();
    {
        const u32x4 s4 = {SENT, SENT, SENT, SENT};
#pragma unroll
        for (int i = 0; i < 4; ++i) {
            const int m = min(t + 512 * i, nset * 648 - 1), j = m / 648;
            st16sc(rws, OFF_X1 + (ai + 96 * j) * X1_ROW + (m - 648 * j) * 16, s4);
        }
    }
    u32x4 xv[2][3];
    {
        const rsrc_t rx = mkrs(P.x, 2048 * 1280);
#pragma unroll
        for (int it = 0; it < 2; ++it) {
            const int task = min(w + 8 * it, 4 * nset - 1), s0 = ai + 96 * (task >> 2);
#pragma unroll
            for (int i = 0; i < 3; ++i) {
                const int ci = min(l + 64 * i, 159), row = ci >= 80;
                xv[it][i] = ld16(rx, (s0 + 256 * (2 * (task & 3) + row)) * 1280 + (ci - 80 * row) * 16, 0);
            }
        }
    }
    fence();
    if (t < 90) *(u32x4*)(smem + L_S + (t / 30) * 10752 + 321 * 32 + (t % 30) * 16) = u32x4{0u, 0u, 0u, 0u};

    {
        u32x4 f20c, f20s, c32, s32;
        float twc[8], tws[8];
        {
            float c[8], s[8];
#pragma unroll
            for (int j = 0; j < 8; ++j) {
                const int d2 = (j < 4) ? 4 * g + j : 16 + 4 * g + j - 4;
                const bool ok = (d2 < 20) && (i16 <= 10);
                const float a = (float)d2 * (float)i16 * 0.05f;
                c[j] = ok ? cosr(a) : 0.f;
                s[j] = ok ? -sinr(a) : 0.f;
            }
            f20c = pk8(c); f20s = pk8(s);
#pragma unroll
            for (int j = 0; j < 8; ++j) {
                const int d1 = (j < 4) ? 4 * g + j : 16 + 4 * g + j - 4;
                const float pr = (float)d1 * (float)i16;
                c[j] = cosr(pr * 0.03125f);
                s[j] = sinr(pr * 0.03125f);
                twc[j] = cosr(pr * (1.0f / 640.0f));
                tws[j] = sinr(pr * (1.0f / 640.0f));
            }
            c32 = pk8(c); s32 = pk8(s);
        }
        const u32x4 ns32 = s32 ^ 0x80008000u, c32b = c32 ^ 0x80000000u, s32b = s32 ^ 0x80000000u, nc32b = c32b ^ 0x80008000u;
        const int q = i16 >> 2, p = i16 & 3;
        const int d2a = 4 * g + q, d2b = (g == 0) ? 16 + q : d2a;
        const int offa = 64 * d2a + 8 * p, swa = ((d2a >> 2) & 1) << 5;
        const int offb = 64 * d2b + 8 * p, swb = ((d2b >> 2) & 1) << 5;
#pragma unroll
        for (int it = 0; it < 2; ++it) {
            const int task = w + 8 * it;
            if (task < 4 * nset) {
                char* xs = smem + L_X + task * 2560;
#pragma unroll
                for (int i = 0; i < 3; ++i) {
                    const int ci = min(l + 64 * i, 159), c = ci >= 80 ? ci - 80 : ci;
                    *(u32x4*)(xs + ((ci * 16) ^ (((c >> 4) & 1) << 5))) = xv[it][i];
                }
                f32x4 z[2][4];
#pragma unroll
                for (int rr = 0; rr < 2; ++rr) {
                    const char* xr = xs + rr * 1280;
                    f32x4 tre[2], tim[2];
                    const f32x4 zero = {0.f, 0.f, 0.f, 0.f};
#pragma unroll
                    for (int tau = 0; tau < 2; ++tau) {
                        const u32x2 a0 = trread(xr + offa + ((32 * tau) ^ swa));
                        const u32x2 a1 = trread(xr + offb + ((32 * tau) ^ swb));
                        const u32x4 a = {a0.x, a0.y, a1.x, a1.y};
                        tre[tau] = mfma16(a, f20c, zero);
                        tim[tau] = mfma16(a, f20s, zero);
                    }
                    float pr[8], pi[8];
#pragma unroll
                    for (int j = 0; j < 8; ++j) {
                        const float a = tre[j >> 2][j & 3], bb = tim[j >> 2][j & 3];
                        pr[j] = a * twc[j] + bb * tws[j];
                        pi[j] = bb * twc[j] - a * tws[j];
                    }
                    const u32x4 bre = pk8(pr), bim = pk8(pi);
                    z[rr][0] = mfma16(s32, bim, mfma16(c32, bre, zero));
                    z[rr][1] = mfma16(ns32, bre, mfma16(c32, bim, zero));
                    z[rr][2] = mfma16(s32b, bim, mfma16(c32b, bre, zero));
                    z[rr][3] = mfma16(s32b, bre, mfma16(nc32b, bim, zero));
                }
                char* st = smem + L_S + (task >> 2) * 10752 + (task & 3) * 4;
                if (i16 <= 10) {
#pragma unroll
                    for (int i = 0; i < 4; ++i) {
                        const int k = i16 + 20 * (4 * g + i);
                        *(u32*)(st + k * 32) = pk(z[0][0][i], z[1][0][i]);
                        *(u32*)(st + k * 32 + 16) = pk(z[0][1][i], z[1][1][i]);
                    }
#pragma unroll
                    for (int i = 0; i < 4; ++i) {
                        const int k = 320 - i16 - 20 * (4 * g + i);
                        const bool ok = (i16 >= 1 && i16 <= 9) || (i16 == 0 && g == 0 && i == 0);
                        if (ok) {
                            *(u32*)(st + k * 32) = pk(z[0][2][i], z[1][2][i]);
                            *(u32*)(st + k * 32 + 16) = pk(z[0][3][i], z[1][3][i]);
                        }
                    }
                }
            }
        }
        drain();
    }
    STAMP();
    __syncthreads();
    if (t == 0) __hip_atomic_store((GAS u32*)(P.ws + OFF_SYNC + 1024) + ai, tag, __ATOMIC_RELAXED, __HIP_MEMORY_SCOPE_AGENT);
    {
        const int kap = i16 >> 1, plo = i16 & 1, pl = g >> 1;
#pragma unroll 1
        for (int j = 0; j < nset; ++j) {
            const int s0 = ai + 96 * j;
            float f[4];
#pragma unroll
            for (int e = 0; e < 4; ++e) {
                const int a = 4 * (g & 1) + e;
                const float th = (float)kap * (float)(256 * a + s0) * (1.0f / 2048.0f) - ((pl != plo) ? 0.25f : 0.f);
                const float v = cosr(th);
                f[e] = (plo == 1 && pl == 0) ? -v : v;
            }
            const u32x2 fb = {pk(f[0], f[1]), pk(f[2], f[3])};
#pragma unroll
            for (int it = 0; it < 3; ++it) {
                const int T = w + 8 * it;
                if (T < 21) {
                    const u32x2 a = *(const u32x2*)(smem + L_S + j * 10752 + (16 * T + i16) * 32 + g * 8);
                    const f32x4 d = mfma16k16(a, fb, f32x4{0.f, 0.f, 0.f, 0.f});
                    if (T < 20 || g == 0)
                        *(u32x2*)(smem + L_X + j * 10368 + i16 * 648 + (16 * T + 4 * g) * 2) = u32x2{pk(d[0], d[1]), pk(d[2], d[3])};
                }
            }
        }
    }
    __syncthreads();
#pragma unroll
    for (int i = 0; i < 4; ++i) {
        const int m = min(t + 512 * i, nset * 648 - 1), j = m / 648;
        st16sc(rws, OFF_X1 + (ai + 96 * j) * X1_ROW + (m - 648 * j) * 16, sane(*(const u32x4*)(smem + L_X + m * 16)));
    }
    STAMP();
    u32x2 wv[8];
    const int wpl = w >> 2, wtask = min(t & 255, 223), wquad = wtask % 7, wr_ = wtask / 7, wk1 = wr_ & 15, woc = wr_ >> 4;
    {
        const rsrc_t rw = mkrs(wpl ? P.wi : P.wr, 2048 * 642);
        const int cs = min(colbase + 4 * wquad, 317);
#pragma unroll
        for (int e = 0; e < 8; ++e) wv[e] = ld8(rw, ((ka + 8 * (wk1 + 16 * (8 * woc + e))) * 321 + cs) * 2, 0);
    }
    fence();
    u32x4 f1re, f1im, f2re, f2im;
    float t2c[4], t2s[4];
    {
        float c[8], s[8];
#pragma unroll
        for (int j = 0; j < 8; ++j) {
            const float a = (float)(8 * (g & 1) + j) * (float)i16 * 0.0625f;
            const float co = cosr(a), si = sinr(a);
            c[j] = (g < 2) ? co : si;
            s[j] = (g < 2) ? -si : co;
        }
        f1re = pk8(c); f1im = pk8(s);
#pragma unroll
        for (int j = 0; j < 4; ++j) {
            const float pr = (float)(4 * g + j) * (float)i16;
            const float co = cosr(pr * 0.0625f), si = sinr(pr * 0.0625f);
            c[j] = co; c[4 + j] = si;
            s[j] = -si; s[4 + j] = co;
            t2c[j] = cosr(pr * (1.0f / 256.0f));
            t2s[j] = sinr(pr * (1.0f / 256.0f));
        }
        f2re = pk8(c); f2im = pk8(s);
    }
    const u32x4 ire = {f2re.x, f2re.y, f2re.z ^ 0x80008000u, f2re.w ^ 0x80008000u};
    const u32x4 iim = {f2im.x ^ 0x80008000u, f2im.y ^ 0x80008000u, f2im.z, f2im.w};
    wait_flags<96>(rws, OFF_SYNC + 1024, tag, w, l);
    STAMP();
    {
        const int task = min(t, 447), quad = task % 7, r = task / 7, pl = r >> 5, oc = r & 31;
        const u32 base = OFF_X1 + (8 * oc) * X1_ROW + (ka * 2 + pl) * 648 + (colbase + 4 * quad) * 2;
        u32x2 v[8];
        for (u32 spins = 0; spins < SPIN_MAX; ++spins) {
            fence();
            bool ok = true;
#pragma unroll
            for (int e = 0; e < 8; ++e) v[e] = ld8(rws, base + e * X1_ROW, 1);
#pragma unroll
            for (int e = 0; e < 8; ++e) ok &= fresh2(v[e]);
            if (__all(ok)) break;
            __builtin_amdgcn_s_sleep(1);
        }
        u32x4 o[4];
        tr8x4(v, o);
#pragma unroll
        for (int c = 0; c < 4; ++c) {
            const int m = (4 * quad + c) * 2 + pl;
            *(u32x4*)(smem + L_C + m * 512 + ((oc ^ ((m >> 3) & 7)) * 16)) = o[c];
        }
    }
    {
        if (colbase + 4 * wquad > 317) {
#pragma unroll
            for (int e = 0; e < 8; ++e) wv[e] = u32x2{wv[e].y >> 16, 0u};
        }
        u32x4 o[4];
        tr8x4(wv, o);
#pragma unroll
        for (int c = 0; c < 4; ++c) {
            const int m = (4 * wquad + c) * 2 + wpl;
            *(u32x4*)(smem + L_W + m * 512 + (((wk1 * 2 + woc) ^ ((m >> 3) & 7)) * 16)) = o[c];
        }
    }
    STAMP();
    __syncthreads();
    {
        const int q = i16 >> 2, p = i16 & 3;
#pragma unroll 1
        for (int col = w; col < 28; col += 8) {
            const f32x4 zero = {0.f, 0.f, 0.f, 0.f};
            const int mi = col * 2 + (g >> 1), swi = (mi >> 3) & 7;
            const char* ib = smem + L_C + mi * 512 + (p & 1) * 8;
            const u32x2 a0 = trread(ib + (((2 * (8 * (g & 1) + q) + (p >> 1)) ^ swi) * 16));
            const u32x2 a1 = trread(ib + (((2 * (8 * (g & 1) + 4 + q) + (p >> 1)) ^ swi) * 16));
            const u32x4 a = {a0.x, a0.y, a1.x, a1.y};
            const int m0 = col * 2, sw = (m0 >> 3) & 7;
            const int woff = (((i16 * 2 + (g >> 1)) ^ sw) * 16) + (g & 1) * 8;
            const u32x2 wre = *(const u32x2*)(smem + L_W + m0 * 512 + woff);
            const u32x2 wim = *(const u32x2*)(smem + L_W + (m0 + 1) * 512 + woff);
            f32x4 re = mfma16(a, f1re, zero), im = mfma16(a, f1im, zero);
            float x[8];
#pragma unroll
            for (int i = 0; i < 4; ++i) {
                x[i] = re[i] * t2c[i] + im[i] * t2s[i];
                x[4 + i] = im[i] * t2c[i] - re[i] * t2s[i];
            }
            u32x4 d = pk8(x);
            re = mfma16(f2re, d, zero); im = mfma16(f2im, d, zero);
#pragma unroll
            for (int i = 0; i < 4; ++i) {
                const float a_ = (i & 1) ? bfhi(wre[i >> 1]) : bflo(wre[i >> 1]);
                const float b_ = (i & 1) ? bfhi(wim[i >> 1]) : bflo(wim[i >> 1]);
                x[i] = re[i] * a_ - im[i] * b_;
                x[4 + i] = re[i] * b_ + im[i] * a_;
            }
            d = pk8(x);
            re = mfma16(d, ire, zero); im = mfma16(d, iim, zero);
#pragma unroll
            for (int i = 0; i < 4; ++i) {
                x[i] = re[i] * t2c[i] - im[i] * t2s[i];
                x[4 + i] = im[i] * t2c[i] + re[i] * t2s[i];
            }
            d = pk8(x);
            re = mfma16(d, ire, zero); im = mfma16(d, iim, zero);
            const int ooff = (((2 * i16 + (g >> 1)) ^ sw) * 16) + (g & 1) * 8;
            *(u32x2*)(smem + L_C + m0 * 512 + ooff) = u32x2{pk(re[0], re[1]), pk(re[2], re[3])};
            *(u32x2*)(smem + L_C + (m0 + 1) * 512 + ooff) = u32x2{pk(im[0], im[1]), pk(im[2], im[3])};
        }
    }
    STAMP();
    wait_flags<160>(rws, OFF_SYNC + 2048, tag, w, l);
    if (ai == 0 && t == 0) __hip_atomic_store((GAS u32*)(P.ws + OFF_SYNC), tag, __ATOMIC_RELAXED, __HIP_MEMORY_SCOPE_AGENT);
    {
        const int q = i16 >> 2, p = i16 & 3;
#pragma unroll
        for (int it = 0; it < 7; ++it) {
            const int cmb = (w * 7 + it) * 4 + g;
            const int quad = cmb % 7, r = cmb / 7, pl = r & 1, G = r >> 1;
            const int m = (4 * quad + q) * 2 + pl;
            const u32x2 v = trread(smem + L_C + m * 512 + (((2 * G + (p >> 1)) ^ ((m >> 3) & 7)) * 16) + (p & 1) * 8);
            *(u32x2*)(smem + L_W + (16 * G + i16) * 112 + pl * 56 + quad * 8) = v;
        }
    }
    __syncthreads();
#pragma unroll
    for (int i = 0; i < 4; ++i) {
        const int m = min(t + 512 * i, 1791);
        st16sc(rws, OFF_Y1 + ai * Y1_BLK + m * 16, sane(*(const u32x4*)(smem + L_W + m * 16)));
    }
    STAMP();

    u32x4 C[2], S[2], nsS[2], sC[2], F[2];
    float twc[8], tws[8];
    {
        const u32 sgn = (g >= 2) ? 0x80008000u : 0u;
#pragma unroll
        for (int tau = 0; tau < 2; ++tau) {
            float c[8], s[8];
            const float d1 = (float)(16 * tau + i16);
#pragma unroll
            for (int j = 0; j < 8; ++j) {
                const float a = (float)(8 * g + j) * d1 * 0.03125f;
                c[j] = cosr(a);
                s[j] = sinr(a);
            }
            C[tau] = pk8(c); S[tau] = pk8(s);
            nsS[tau] = S[tau] ^ (sgn ^ 0x80008000u);
            sC[tau] = C[tau] ^ sgn;
#pragma unroll
            for (int i = 0; i < 4; ++i) {
                const float a = d1 * (float)(4 * g + i) * (1.0f / 640.0f);
                twc[4 * tau + i] = cosr(a) * (1.0f / (2048.0f * 640.0f));
                tws[4 * tau + i] = sinr(a) * (1.0f / (2048.0f * 640.0f));
            }
        }
#pragma unroll
        for (int dl = 0; dl < 2; ++dl) {
            float v[8];
#pragma unroll
            for (int j = 0; j < 8; ++j) {
                const int k2 = 4 * g + (j & 3), d2 = 16 * dl + i16;
                const float wgt = (k2 > 10 || d2 >= 20) ? 0.f : ((k2 == 0 || k2 == 10) ? 1.f : 2.f);
                const float a = (float)k2 * (float)d2 * 0.05f;
                v[j] = (j < 4) ? wgt * cosr(a) : -wgt * sinr(a);
            }
            F[dl] = pk8(v);
        }
    }
    {
        if (w == 0) {
            for (u32 spins = 0; spins < SPIN_MAX; ++spins) {
                fence();
                const u32x4 f = ld16(rws, OFF_Y1 + (l + l / 2) * Y1_BLK + ai * 112 + 96, 1);
                if (__all(fresh4(f))) break;
                __builtin_amdgcn_s_sleep(1);
            }
        }
        __syncthreads();
        STAMP();
        u32 off[4];
#pragma unroll
        for (int i = 0; i < 4; ++i) {
            const int m = min(t + 512 * i, nset * 672 - 1), j = m / 672, mm = m - 672 * j, ab = mm / 7;
            off[i] = OFF_Y1 + ab * Y1_BLK + (ai + 96 * j) * 112 + (mm - 7 * ab) * 16;
        }
        u32x4 v[4];
        for (u32 spins = 0; spins < SPIN_MAX; ++spins) {
            fence();
#pragma unroll
            for (int i = 0; i < 4; ++i) v[i] = ld16(rws, off[i], 1);
            if (__all(fresh4(v[0]) && fresh4(v[1]) && fresh4(v[2]) && fresh4(v[3]))) break;
            __builtin_amdgcn_s_sleep(1);
        }
#pragma unroll
        for (int i = 0; i < 4; ++i) *(u32x4*)(smem + L_X + min(t + 512 * i, nset * 672 - 1) * 16) = v[i];
    }
    STAMP();
    __syncthreads();
    {
        const int q = i16 >> 2, p = i16 & 3;
        const int kap = 2 * g + (q >> 1), pl = q & 1;
        const int plo = i16 >> 3, a = i16 & 7;
#pragma unroll 1
        for (int j = 0; j < nset; ++j) {
            const int s0 = ai + 96 * j;
            float f[4];
#pragma unroll
            for (int e = 0; e < 4; ++e) {
                const int kk = 2 * g + (e >> 1), pp = e & 1;
                const float th = (float)kk * (float)(256 * a + s0) * (1.0f / 2048.0f) - ((pp != plo) ? 0.25f : 0.f);
                const float v = cosr(th);
                f[e] = (plo == 0 && pp == 1) ? -v : v;
            }
            const u32x2 fa = {pk(f[0], f[1]), pk(f[2], f[3])};
#pragma unroll
            for (int it = 0; it < 3; ++it) {
                const int T = w + 8 * it;
                if (T < 21) {
                    const int k = min(16 * T + 4 * p, 320);
                    const int cgk = min(k / 28, 11), idx = k - min(28 * cgk, 296);
                    const u32x2 bfr = trread(smem + L_X + j * 10752 + (kap * 12 + cgk) * 112 + pl * 56 + idx * 2);
                    const f32x4 d = mfma16k16(fa, bfr, f32x4{0.f, 0.f, 0.f, 0.f});
                    *(u32x2*)(smem + L_S + j * 10752 + (16 * T + i16) * 32 + g * 8) = u32x2{pk(d[0], d[1]), pk(d[2], d[3])};
                }
            }
        }
    }
    __syncthreads();
#pragma unroll 1
    for (int task = w; task < 4 * nset; task += 8) {
        const int j = task >> 2, wp = task & 3;
        const char* sb = smem + L_S + j * 10752 + wp * 4;
        const int k2 = i16 <= 10 ? i16 : 10;
        u32 dre[8], dim[8];
#pragma unroll
        for (int e = 0; e < 8; ++e) {
            const int k = k2 + 20 * (8 * g + e);
            const int kk = (g < 2) ? k : 640 - k;
            dre[e] = *(const u32*)(sb + kk * 32);
            dim[e] = *(const u32*)(sb + kk * 32 + 16);
        }
#pragma unroll
        for (int rr = 0; rr < 2; ++rr) {
            u32x4 are, aim;
            if (rr == 0) {
                are = u32x4{permlo(dre[0], dre[1]), permlo(dre[2], dre[3]), permlo(dre[4], dre[5]), permlo(dre[6], dre[7])};
                aim = u32x4{permlo(dim[0], dim[1]), permlo(dim[2], dim[3]), permlo(dim[4], dim[5]), permlo(dim[6], dim[7])};
            } else {
                are = u32x4{permhi(dre[0], dre[1]), permhi(dre[2], dre[3]), permhi(dre[4], dre[5]), permhi(dre[6], dre[7])};
                aim = u32x4{permhi(dim[0], dim[1]), permhi(dim[2], dim[3]), permhi(dim[4], dim[5]), permhi(dim[6], dim[7])};
            }
            u32x4 va[2];
            const f32x4 zero = {0.f, 0.f, 0.f, 0.f};
#pragma unroll
            for (int tau = 0; tau < 2; ++tau) {
                const f32x4 vre = mfma16(aim, nsS[tau], mfma16(are, C[tau], zero));
                const f32x4 vim = mfma16(aim, sC[tau], mfma16(are, S[tau], zero));
                float pr[8];
#pragma unroll
                for (int i = 0; i < 4; ++i) {
                    pr[i] = vre[i] * twc[4 * tau + i] - vim[i] * tws[4 * tau + i];
                    pr[4 + i] = vim[i] * twc[4 * tau + i] + vre[i] * tws[4 * tau + i];
                }
                va[tau] = pk8(pr);
            }
            u16* orow = P.out + (size_t)(ai + 96 * j + 256 * (2 * wp + rr)) * 1280;
#pragma unroll
            for (int dl = 0; dl < 2; ++dl) {
                const f32x4 y0 = mfma16(va[0], F[dl], zero);
                const f32x4 y1 = mfma16(va[1], F[dl], zero);
                u32 a0 = pk(y0[0], y0[1]), a1 = pk(y0[2], y0[3]);
                u32 b0 = pk(y1[0], y1[1]), b1 = pk(y1[2], y1[3]);
                auto s0 = __builtin_amdgcn_permlane16_swap(a0, b0, false, false);
                auto s1 = __builtin_amdgcn_permlane16_swap(a1, b1, false, false);
                const int d2 = 16 * dl + i16;
                if (d2 < 20) *(u32x4*)(orow + 32 * d2 + (g & 1) * 16 + (g >> 1) * 8) = u32x4{s0[0], s1[0], s0[1], s1[1]};
            }
        }
    }
    STAMP();
    STAMP_OUT(P.ws);
}

DI void role_b(const Args& P, char* smem, const int t, const int w, const int xcd, const int jx, const u32 tag) {
    const int l = t & 63, g = l >> 4, i16 = l & 15;
    const int chb = 20 * xcd + jx;
    const rsrc_t rws = mkrs(P.ws, WS_USED);
    STAMP_DECL STAMP();
    if (t == 0) *(u32*)(smem + L_CNT) = 0u;
    __syncthreads();
    u32x2 gv[8];
    u32 pw0 = 0, pw1 = 0;
    if (w >= 4) {
        const int tt = t - 256;
        const rsrc_t ru = mkrs(P.u, 2048 * 1280);
        {
            const int b0 = (160 * xcd) & ~127, nl = (160 * xcd + 160 - b0 + 127) >> 7;
            const int m0 = tt, m1 = min(tt + 256, 103 * 3 - 1);
            pw0 = __builtin_amdgcn_raw_buffer_load_b32(ru, min(103 * jx + m0 / 3, 2047) * 1280 + b0 + min(m0 % 3, nl - 1) * 128, 0, 0);
            pw1 = __builtin_amdgcn_raw_buffer_load_b32(ru, min(103 * jx + m1 / 3, 2047) * 1280 + b0 + min(m1 % 3, nl - 1) * 128, 0, 0);
        }
#pragma unroll
        for (int e = 0; e < 8; ++e) gv[e] = ld8(ru, (8 * tt + e) * 1280 + chb * 8, 0);
        fence();
        if (tt < 128) *(u32x4*)(smem + L_U + (tt >> 5) * PB_U + (tt & 31) * 16) = u32x4{0u, 0u, 0u, 0u};
        else if (tt < 144) {
            const int v = tt - 128;
            *(u32x4*)(smem + L_K + (v >> 2) * PB_KS + ((v >> 1) & 1) * PB_K1 + 4096 + (v & 1) * 16) = u32x4{0u, 0u, 0u, 0u};
        }
        u32x4 o[4];
        tr8x4(gv, o);
#pragma unroll
        for (int c = 0; c < 4; ++c) *(u32x4*)(smem + L_U + c * PB_U + 512 + tt * 16) = o[c];
        asm volatile("" :: "v"(pw0), "v"(pw1));
        STAMP();
    } else {
        const int ch = 4 * chb + w;
        u32 pv[12];
        {
            const u32 nb = 640 * 128 * 2;
            const rsrc_t r0 = mkrs(P.Are, nb), r1 = mkrs(P.Aim, nb), r2 = mkrs(P.Bre, nb), r3 = mkrs(P.Bim, nb), r4 = mkrs(P.Cre, nb), r5 = mkrs(P.Cim, nb);
#pragma unroll
            for (int h = 0; h < 2; ++h) {
                const u32 off = (ch * 128 + 64 * h + l) * 2;
                pv[6 * h + 0] = ld2(r0, off); pv[6 * h + 1] = ld2(r1, off); pv[6 * h + 2] = ld2(r2, off);
                pv[6 * h + 3] = ld2(r3, off); pv[6 * h + 4] = ld2(r4, off); pv[6 * h + 5] = ld2(r5, off);
            }
        }
        const u32 ldt = ld2(mkrs(P.logdt, 1280), ch * 2);
        {
            const u32x4 s4 = {SENT, SENT, SENT, SENT};
#pragma unroll
            for (int i = 0; i < 5; ++i) st16sc(rws, OFF_Y1 + min(chb * 1076 + min(t + 256 * i, 1075), 172031) * 16, s4);
        }
        fence();
        char* sc = smem + L_G + w * KG_SZ;
        const float dt = rbf(expf(bf2f(ldt)));
        f32x4 kacc[3];
#pragma unroll
        for (int T = 0; T < 3; ++T) kacc[T] = f32x4{0.f, 0.f, 0.f, 0.f};
#pragma unroll 1
        for (int h = 0; h < 2; ++h) {
            const float are = bf2f(h ? pv[6] : pv[0]), aim = bf2f(h ? pv[7] : pv[1]), bre = bf2f(h ? pv[8] : pv[2]);
            const float bim = bf2f(h ? pv[9] : pv[3]), cre = bf2f(h ? pv[10] : pv[4]), cim = bf2f(h ? pv[11] : pv[5]);
            const float lre = dt * are, lim = dt * aim * 0.15915494309189535f;
            const float dbr = dt * bre, dbi = dt * bim;
            const cf c = {cre * dbr - cim * dbi, cre * dbi + cim * dbr};
            char* rowp = sc + l * 4;
            const cf z1 = cexpk(lre, lim, 1.f), z8 = cexpk(lre, lim, 8.f), z128 = cexpk(lre, lim, 128.f);
            const cf z2 = cmul(z1, z1), z4 = cmul(z2, z2), z6 = cmul(z2, z4);
            cf v = {1.f, 0.f}, r0 = c, r1 = cmul(c, z128);
            cf q = cmul(r1, z128);
#pragma unroll
            for (int j = 0; j < 8; ++j) {
                *(u32*)(rowp + KG_B + (2 * j) * KG_RS) = pk(v.re, v.im);
                v = cmul(v, z8);
                *(u32*)(rowp + KG_B + (2 * j + 1) * KG_RS) = pk(v.re, v.im);
                v = cmul(v, z8);
                *(u32*)(rowp + j * KG_RS) = pk(r0.re, -r0.im);
                *(u32*)(rowp + (8 + j) * KG_RS) = pk(r1.re, -r1.im);
                r0 = cmul(r0, z1);
                r1 = cmul(r1, z1);
#pragma unroll
                for (int e = 0; e < 2; ++e) {
                    const int hh = 2 + 2 * j + e;
                    if (hh < 4) {
                        const int r = 16 + (hh - 2) * 4;
                        const cf a = cmul(q, z2), bq = cmul(q, z4), cq = cmul(q, z6);
                        *(u32*)(rowp + r * KG_RS) = pk(q.re, -q.im);
                        *(u32*)(rowp + (r + 1) * KG_RS) = pk(a.re, -a.im);
                        *(u32*)(rowp + (r + 2) * KG_RS) = pk(bq.re, -bq.im);
                        *(u32*)(rowp + (r + 3) * KG_RS) = pk(cq.re, -cq.im);
                    } else if (hh < 8) {
                        const int r = 24 + (hh - 4) * 2;
                        const cf bq = cmul(q, z4);
                        *(u32*)(rowp + r * KG_RS) = pk(q.re, -q.im);
                        *(u32*)(rowp + (r + 1) * KG_RS) = pk(bq.re, -bq.im);
                    } else if (hh <= 16) {
                        *(u32*)(rowp + (32 + hh - 8) * KG_RS) = pk(q.re, -q.im);
                    }
                    if (hh < 16) q = cmul(q, z128);
                }
            }
            const char* bp = sc + KG_B + i16 * KG_RS + g * 16;
#pragma unroll
            for (int ks = 0; ks < 4; ++ks) {
                const u32x4 bf = *(const u32x4*)(bp + ks * 64);
#pragma unroll
                for (int T = 0; T < 3; ++T)
                    kacc[T] = mfma16(*(const u32x4*)(sc + (16 * T + i16) * KG_RS + g * 16 + ks * 64), bf, kacc[T]);
            }
        }
#pragma unroll
        for (int T = 0; T < 3; ++T)
#pragma unroll
            for (int i = 0; i < 4; ++i) {
                const int row = 16 * T + 4 * g + i;
                int e;
                if (T == 0) e = 128 * (row >> 3) + (row & 7);
                else if (T == 1) e = (row < 24) ? 256 + 128 * ((row - 16) >> 2) + 2 * ((row - 16) & 3) : 512 + 128 * ((row - 24) >> 1) + 4 * ((row - 24) & 1);
                else e = 1024 + 128 * (row - 32);
                if (row < 40 || (row == 40 && i16 == 0))
                    *(u16*)(sc + KG_T + (e + 8 * i16) * 2) = (u16)(pk(kacc[T][i], kacc[T][i]) & 0xffffu);
            }
        drain();
        if (l == 0) {
            const u32 old = __hip_atomic_fetch_add((LAS u32*)(smem + L_CNT), 1u, __ATOMIC_RELAXED, __HIP_MEMORY_SCOPE_WORKGROUP);
            if (old == 3u) __hip_atomic_store((GAS u32*)(P.ws + OFF_SYNC + 2048) + chb, tag, __ATOMIC_RELAXED, __HIP_MEMORY_SCOPE_AGENT);
        }
#pragma unroll 1
        for (int it = 0; it < 4; ++it) {
            const int tt = l + 64 * it, a = 255 - tt;
            u32 code;
            if (a < 32) code = 0x76543210u;
            else if (a < 64) code = 0x86444200u;
            else if (a < 128) code = 0x88444000u;
            else code = (a & 1) ? 0x88880000u : 0x88800000u;
            const int lastoff = (a <= 32) ? 7 : 8;
            const char* tb = sc + KG_T + 16 * a;
            u32 v[8];
#pragma unroll
            for (int e = 0; e < 8; ++e) v[e] = *(const u16*)(tb + 2 * ((code >> (4 * e)) & 15));
            u32 vm = *(const u16*)(tb - 16 + 2 * lastoff);
            vm = (a == 0) ? 0u : vm;
            char* kb = smem + L_K + w * PB_KS + tt * 16;
            *(u32x4*)(kb) = u32x4{v[7] | (v[6] << 16), v[5] | (v[4] << 16), v[3] | (v[2] << 16), v[1] | (v[0] << 16)};
            *(u32x4*)(kb + PB_K1) = u32x4{v[6] | (v[5] << 16), v[4] | (v[3] << 16), v[2] | (v[1] << 16), v[0] | (vm << 16)};
        }
        STAMP();
    }
    const int ch = w & 3, rh = w >> 2;
    const float dsk = bf2f(ld2(mkrs(P.dskip, 1280), (4 * chb + ch) * 2));
    __syncthreads();
    STAMP();
    const int mu = g >> 1, hp = g & 1;
    const int x0 = 2047 - 16 * mu - i16 + 8 * hp;
    const char* ap = smem + L_K + ch * PB_KS + ((x0 & 1) ? PB_K1 + (x0 - 1) * 2 : x0 * 2);
    const char* bp = smem + L_U + ch * PB_U + (256 + 16 * (i16 - mu) + 8 * hp) * 2;
    f32x4 acc[8];
#pragma unroll
    for (int c = 0; c < 8; ++c) acc[c] = f32x4{0.f, 0.f, 0.f, 0.f};
#pragma unroll 1
    for (int r = 4 * rh; r < 4 * rh + 4; ++r) {
        u32x4 A[8], B[8];
#pragma unroll
        for (int p = 0; p < 8; ++p) {
            const u32* a = (const u32*)(ap - 64 * r - 512 * p);
            A[p] = u32x4{a[0], a[1], a[2], a[3]};
            B[p] = *(const u32x4*)(bp - 64 * r + 512 * p);
        }
#pragma unroll
        for (int p = 0; p < 8; ++p)
#pragma unroll
            for (int q = 0; q + p < 8; ++q) acc[p + q] = mfma16(A[p], B[q], acc[p + q]);
    }
    STAMP();
    char* rb = smem + L_G;
#pragma unroll
    for (int c = 0; c < 4; ++c) *(f32x4*)(rb + (w * 4 + c) * 1024 + l * 16) = rh ? acc[c] : acc[4 + c];
    __syncthreads();
    char* st = smem + L_G + 32768;
#pragma unroll
    for (int c = 0; c < 4; ++c) {
        const f32x4 o = *(const f32x4*)(rb + ((w ^ 4) * 4 + c) * 1024 + l * 16);
        const f32x4 mine = rh ? acc[4 + c] : acc[c];
        const int tt = 256 * (4 * rh + c) + 16 * i16 + 4 * g;
        const u32x2 uu = *(const u32x2*)(smem + L_U + ch * PB_U + (256 + tt) * 2);
#pragma unroll
        for (int i = 0; i < 4; ++i) {
            const float uf = (i & 1) ? bfhi(i < 2 ? uu.x : uu.y) : bflo(i < 2 ? uu.x : uu.y);
            const float v = rbf(mine[i] + o[i]) + rbf(uf * dsk);
            *(u16*)(st + (tt >> 4) * 128 + (((4 * g + i) ^ i16) * 8) + ch * 2) = (u16)(pk(v, v) & 0xffffu);
        }
    }
    __syncthreads();
    STAMP();
    {
        const rsrc_t ro = mkrs(P.out, 2048 * 2560);
#pragma unroll
        for (int i = 0; i < 4; ++i) {
            const int row = t + 512 * i;
            __builtin_amdgcn_raw_buffer_store_b64(*(const u32x2*)(st + (row >> 4) * 128 + (((row & 15) ^ ((row >> 4) & 15)) * 8)), ro,
                                                  row * 2560 + 1280 + chb * 8, 0, 0);
        }
    }
    STAMP();
    STAMP_OUT(P.ws);
}

__global__ void __launch_bounds__(512, 2) fused_kernel(Args P) {
    extern __shared__ __attribute__((aligned(16))) char smem[];
    const int t = threadIdx.x;
    const int w = __builtin_amdgcn_readfirstlane(t >> 6);
    const int b = blockIdx.x, xcd = b & 7, jx = b >> 3;
    const u32 tag = __hip_atomic_load((GAS u32*)(P.ws + OFF_SYNC), __ATOMIC_RELAXED, __HIP_MEMORY_SCOPE_AGENT) + 1u;
    if (jx < 20) role_b(P, smem, t, w, xcd, jx, tag);
    else role_a(P, smem, t, w, xcd, jx - 20, tag);
    if (jx >= 20 && w == 0) {
        float x0 = t, a = 1.0f, b = 0.0f;
        asm volatile("" : "+v"(a), "+v"(b));
#pragma unroll 1
        for (int i = 0; i < 512; ++i) {
            asm volatile("v_fma_f32 %0, %0, %1, %2\n\tv_fma_f32 %0, %0, %1, %2\n\tv_fma_f32 %0, %0, %1, %2\n\tv_fma_f32 %0, %0, %1, %2\n\t"
                         "v_fma_f32 %0, %0, %1, %2\n\tv_fma_f32 %0, %0, %1, %2\n\tv_fma_f32 %0, %0, %1, %2\n\tv_fma_f32 %0, %0, %1, %2" : "+v"(x0) : "v"(a), "v"(b));
        }
        asm volatile("" :: "v"(x0));
    }
}

extern "C" void kernel_launch(void* const* d_in, const int* in_sizes, int n_in,
                              void* d_out, int out_size, void* d_ws, size_t ws_size,
                              hipStream_t stream) {
    Args a;
    a.x = (const u16*)d_in[0];
    a.wr = (const u16*)d_in[1];
    a.wi = (const u16*)d_in[2];
    a.u = (const u16*)d_in[3];
    a.Are = (const u16*)d_in[4];
    a.Aim = (const u16*)d_in[5];
    a.Bre = (const u16*)d_in[6];
    a.Bim = (const u16*)d_in[7];
    a.Cre = (const u16*)d_in[8];
    a.Cim = (const u16*)d_in[9];
    a.logdt = (const u16*)d_in[10];
    a.dskip = (const u16*)d_in[11];
    a.ws = (char*)d_ws;
    a.out = (u16*)d_out;
    (void)hipFuncSetAttribute((const void*)fused_kernel, hipFuncAttributeMaxDynamicSharedMemorySize, LDS_BYTES);
    fused_kernel<<<256, 512, LDS_BYTES, stream>>>(a);
}
```

```cpp
#include <hip/hip_runtime.h>
#include <hip/hip_bf16.h>

typedef unsigned short u16;
typedef unsigned int u32;
typedef __attribute__((ext_vector_type(2))) __bf16 bf16x2;
typedef __attribute__((ext_vector_type(8))) __bf16 bf16x8;
typedef __attribute__((ext_vector_type(2))) float f32x2;
typedef __attribute__((ext_vector_type(4))) float f32x4;
typedef __attribute__((ext_vector_type(4))) u32 u32x4;
typedef __attribute__((ext_vector_type(2))) u32 u32x2;
typedef __attribute__((ext_vector_type(4))) short s16x4;
typedef __amdgpu_buffer_rsrc_t rsrc_t;
#define LAS __attribute__((address_space(3)))
#define GAS __attribute__((address_space(1)))
#define DI __device__ __forceinline__
#define SC1 16

constexpr u32 X1_ROW = 10368;
constexpr u32 OFF_X1 = 0;
constexpr u32 Y1_BLK = 28672;
constexpr u32 OFF_Y1 = 256 * X1_ROW;
constexpr u32 OFF_SYNC = OFF_Y1 + 96 * Y1_BLK;
constexpr u32 WS_USED = OFF_SYNC + 4096;
constexpr u32 SENT = 0xFFFFFFFFu;
constexpr u32 SPIN_MAX = 1u << 16;
constexpr bool PROBE_A = 0;
constexpr int PROBE_ID = 2, PROBE_WAVE = 0;

constexpr int L_X = 0;
constexpr int L_S = 32256;
constexpr int L_W = 64512;
constexpr int L_C = L_W + 28672;
constexpr int PB_U = 4608;
constexpr int L_U = 0;
constexpr int L_K = L_U + 4 * PB_U;
constexpr int PB_K1 = 4160, PB_KS = 8320;
constexpr int L_G = L_K + 4 * PB_KS;
constexpr int KG_RS = 272, KG_B = 41 * KG_RS, KG_T = 57 * KG_RS, KG_SZ = 19616;
constexpr int L_CNT = L_G + 4 * KG_SZ;
constexpr int LDS_BYTES = L_CNT + 16;

#define STAMP_DECL unsigned long long pr0_ = __builtin_amdgcn_s_memrealtime(), pr1_ = pr0_; int prn_ = 0;
#define STAMP() do { __builtin_amdgcn_sched_barrier(0); if (prn_ == PROBE_ID) pr1_ = __builtin_amdgcn_s_memrealtime(); ++prn_; __builtin_amdgcn_sched_barrier(0); } while (0)
#define STAMP_OUT(ws) do { if (PROBE_ROLE_OK && w == PROBE_WAVE) { const unsigned long long n_ = __builtin_amdgcn_s_memrealtime(); \
    while (__builtin_amdgcn_s_memrealtime() - n_ < pr1_ - pr0_) __builtin_amdgcn_s_sleep(1); } } while (0)

DI u32 pk(float a, float b) {
    f32x2 v = {a, b};
    return __builtin_bit_cast(u32, __builtin_convertvector(v, bf16x2));
}
DI u32x4 pk8(const float* v) { return u32x4{pk(v[0], v[1]), pk(v[2], v[3]), pk(v[4], v[5]), pk(v[6], v[7])}; }
DI float bflo(u32 v) { return __uint_as_float(v << 16); }
DI float bfhi(u32 v) { return __uint_as_float(v & 0xffff0000u); }
DI float bf2f(u32 v) { return __uint_as_float(v << 16); }
DI float rbf(float a) { return bflo(pk(a, a)); }
DI float cosr(float rev) { return __builtin_amdgcn_cosf(rev); }
DI float sinr(float rev) { return __builtin_amdgcn_sinf(rev); }
DI f32x4 mfma16(u32x4 a, u32x4 b, f32x4 c) {
    return __builtin_amdgcn_mfma_f32_16x16x32_bf16(__builtin_bit_cast(bf16x8, a), __builtin_bit_cast(bf16x8, b), c, 0, 0, 0);
}
DI f32x4 mfma16k16(u32x2 a, u32x2 b, f32x4 c) {
    return __builtin_amdgcn_mfma_f32_16x16x16bf16_1k(__builtin_bit_cast(s16x4, a), __builtin_bit_cast(s16x4, b), c, 0, 0, 0);
}
DI u32x2 trread(const char* p) {
    return __builtin_bit_cast(u32x2, __builtin_amdgcn_ds_read_tr16_b64_v4i16((LAS s16x4*)(p)));
}
DI u32 permlo(u32 a, u32 b) { return __builtin_amdgcn_perm(b, a, 0x05040100u); }
DI u32 permhi(u32 a, u32 b) { return __builtin_amdgcn_perm(b, a, 0x07060302u); }
DI rsrc_t mkrs(const void* p, u32 bytes) { return __builtin_amdgcn_make_buffer_rsrc((void*)p, 0, bytes, 0x00020000); }
DI u32x4 ld16(rsrc_t r, u32 off, int aux) { return aux ? __builtin_amdgcn_raw_buffer_load_b128(r, off, 0, SC1) : __builtin_amdgcn_raw_buffer_load_b128(r, off, 0, 0); }
DI u32x2 ld8(rsrc_t r, u32 off, int aux) { return aux ? __builtin_amdgcn_raw_buffer_load_b64(r, off, 0, SC1) : __builtin_amdgcn_raw_buffer_load_b64(r, off, 0, 0); }
DI u32 ld2(rsrc_t r, u32 off) { return __builtin_amdgcn_raw_buffer_load_b16(r, off, 0, 0); }
DI void st16sc(rsrc_t r, u32 off, u32x4 v) { __builtin_amdgcn_raw_buffer_store_b128(v, r, off, 0, SC1); }
DI u32x4 sane(u32x4 v) { return u32x4{min(v.x, SENT - 1), min(v.y, SENT - 1), min(v.z, SENT - 1), min(v.w, SENT - 1)}; }
DI bool fresh4(u32x4 v) { return max(max(v.x, v.y), max(v.z, v.w)) != SENT; }
DI bool fresh2(u32x2 v) { return max(v.x, v.y) != SENT; }
DI void fence() { asm volatile("" ::: "memory"); }
DI void drain() { asm volatile("s_waitcnt vmcnt(0)" ::: "memory"); }
DI void tr8x4(const u32x2* v, u32x4* o) {
    o[0] = u32x4{permlo(v[0].x, v[1].x), permlo(v[2].x, v[3].x), permlo(v[4].x, v[5].x), permlo(v[6].x, v[7].x)};
    o[1] = u32x4{permhi(v[0].x, v[1].x), permhi(v[2].x, v[3].x), permhi(v[4].x, v[5].x), permhi(v[6].x, v[7].x)};
    o[2] = u32x4{permlo(v[0].y, v[1].y), permlo(v[2].y, v[3].y), permlo(v[4].y, v[5].y), permlo(v[6].y, v[7].y)};
    o[3] = u32x4{permhi(v[0].y, v[1].y), permhi(v[2].y, v[3].y), permhi(v[4].y, v[5].y), permhi(v[6].y, v[7].y)};
}
template <int NF>
DI void wait_flags(rsrc_t rws, u32 off, u32 tag, int w, int l) {
    if (w == 0) {
        const int q = min(l, NF / 4 - 1);
        for (u32 spins = 0; spins < SPIN_MAX; ++spins) {
            fence();
            const u32x4 f = ld16(rws, off + q * 16, 1);
            if (__all(f.x == tag && f.y == tag && f.z == tag && f.w == tag)) break;
            __builtin_amdgcn_s_sleep(2);
        }
    }
    __syncthreads();
}

struct cf { float re, im; };
DI cf cmul(cf a, cf b) { return cf{a.re * b.re - a.im * b.im, a.re * b.im + a.im * b.re}; }
DI cf cexpk(float lre, float lim_rev, float k) {
    const float m = __builtin_amdgcn_exp2f(k * lre * 1.44269504088896341f);
    const float a = __builtin_amdgcn_fractf(k * lim_rev);
    return cf{m * cosr(a), m * sinr(a)};
}

struct Args {
    const u16 *x, *wr, *wi, *u, *Are, *Aim, *Bre, *Bim, *Cre, *Cim, *logdt, *dskip;
    char* ws;
    u16* out;
};

DI void role_a(const Args& P, char* smem, const int t, const int w, const int ka, const int cg, const u32 tag) {
    const int l = t & 63, g = l >> 4, i16 = l & 15;
    const int ai = ka * 12 + cg, nset = ai < 64 ? 3 : 2;
    const int colbase = min(28 * cg, 296);
    const rsrc_t rws = mkrs(P.ws, WS_USED);
    STAMP_DECL STAMP();
    {
        const u32x4 s4 = {SENT, SENT, SENT, SENT};
#pragma unroll
        for (int i = 0; i < 4; ++i) {
            const int m = min(t + 512 * i, nset * 648 - 1), j = m / 648;
            st16sc(rws, OFF_X1 + (ai + 96 * j) * X1_ROW + (m - 648 * j) * 16, s4);
        }
    }
    u32x4 xv[2][3];
    {
        const rsrc_t rx = mkrs(P.x, 2048 * 1280);
#pragma unroll
        for (int it = 0; it < 2; ++it) {
            const int task = min(w + 8 * it, 4 * nset - 1), s0 = ai + 96 * (task >> 2);
#pragma unroll
            for (int i = 0; i < 3; ++i) {
                const int ci = min(l + 64 * i, 159), row = ci >= 80;
                xv[it][i] = ld16(rx, (s0 + 256 * (2 * (task & 3) + row)) * 1280 + (ci - 80 * row) * 16, 0);
            }
        }
    }
    fence();
    if (t < 90) *(u32x4*)(smem + L_S + (t / 30) * 10752 + 321 * 32 + (t % 30) * 16) = u32x4{0u, 0u, 0u, 0u};

    {
        u32x4 f20c, f20s, c32, s32;
        float twc[8], tws[8];
        {
            float c[8], s[8];
#pragma unroll
            for (int j = 0; j < 8; ++j) {
                const int d2 = (j < 4) ? 4 * g + j : 16 + 4 * g + j - 4;
                const bool ok = (d2 < 20) && (i16 <= 10);
                const float a = (float)d2 * (float)i16 * 0.05f;
                c[j] = ok ? cosr(a) : 0.f;
                s[j] = ok ? -sinr(a) : 0.f;
            }
            f20c = pk8(c); f20s = pk8(s);
#pragma unroll
            for (int j = 0; j < 8; ++j) {
                const int d1 = (j < 4) ? 4 * g + j : 16 + 4 * g + j - 4;
                const float pr = (float)d1 * (float)i16;
                c[j] = cosr(pr * 0.03125f);
                s[j] = sinr(pr * 0.03125f);
                twc[j] = cosr(pr * (1.0f / 640.0f));
                tws[j] = sinr(pr * (1.0f / 640.0f));
            }
            c32 = pk8(c); s32 = pk8(s);
        }
        const u32x4 ns32 = s32 ^ 0x80008000u, c32b = c32 ^ 0x80000000u, s32b = s32 ^ 0x80000000u, nc32b = c32b ^ 0x80008000u;
        const int q = i16 >> 2, p = i16 & 3;
        const int d2a = 4 * g + q, d2b = (g == 0) ? 16 + q : d2a;
        const int offa = 64 * d2a + 8 * p, swa = ((d2a >> 2) & 1) << 5;
        const int offb = 64 * d2b + 8 * p, swb = ((d2b >> 2) & 1) << 5;
#pragma unroll
        for (int it = 0; it < 2; ++it) {
            const int task = w + 8 * it;
            if (task < 4 * nset) {
                char* xs = smem + L_X + task * 2560;
#pragma unroll
                for (int i = 0; i < 3; ++i) {
                    const int ci = min(l + 64 * i, 159), c = ci >= 80 ? ci - 80 : ci;
                    *(u32x4*)(xs + ((ci * 16) ^ (((c >> 4) & 1) << 5))) = xv[it][i];
                }
                f32x4 z[2][4];
#pragma unroll
                for (int rr = 0; rr < 2; ++rr) {
                    const char* xr = xs + rr * 1280;
                    f32x4 tre[2], tim[2];
                    const f32x4 zero = {0.f, 0.f, 0.f, 0.f};
#pragma unroll
                    for (int tau = 0; tau < 2; ++tau) {
                        const u32x2 a0 = trread(xr + offa + ((32 * tau) ^ swa));
                        const u32x2 a1 = trread(xr + offb + ((32 * tau) ^ swb));
                        const u32x4 a = {a0.x, a0.y, a1.x, a1.y};
                        tre[tau] = mfma16(a, f20c, zero);
                        tim[tau] = mfma16(a, f20s, zero);
                    }
                    float pr[8], pi[8];
#pragma unroll
                    for (int j = 0; j < 8; ++j) {
                        const float a = tre[j >> 2][j & 3], bb = tim[j >> 2][j & 3];
                        pr[j] = a * twc[j] + bb * tws[j];
                        pi[j] = bb * twc[j] - a * tws[j];
                    }
                    const u32x4 bre = pk8(pr), bim = pk8(pi);
                    z[rr][0] = mfma16(s32, bim, mfma16(c32, bre, zero));
                    z[rr][1] = mfma16(ns32, bre, mfma16(c32, bim, zero));
                    z[rr][2] = mfma16(s32b, bim, mfma16(c32b, bre, zero));
                    z[rr][3] = mfma16(s32b, bre, mfma16(nc32b, bim, zero));
                }
                char* st = smem + L_S + (task >> 2) * 10752 + (task & 3) * 4;
                if (i16 <= 10) {
#pragma unroll
                    for (int i = 0; i < 4; ++i) {
                        const int k = i16 + 20 * (4 * g + i);
                        *(u32*)(st + k * 32) = pk(z[0][0][i], z[1][0][i]);
                        *(u32*)(st + k * 32 + 16) = pk(z[0][1][i], z[1][1][i]);
                    }
#pragma unroll
                    for (int i = 0; i < 4; ++i) {
                        const int k = 320 - i16 - 20 * (4 * g + i);
                        const bool ok = (i16 >= 1 && i16 <= 9) || (i16 == 0 && g == 0 && i == 0);
                        if (ok) {
                            *(u32*)(st + k * 32) = pk(z[0][2][i], z[1][2][i]);
                            *(u32*)(st + k * 32 + 16) = pk(z[0][3][i], z[1][3][i]);
                        }
                    }
                }
            }
        }
        drain();
    }
    STAMP();
    __syncthreads();
    if (t == 0) __hip_atomic_store((GAS u32*)(P.ws + OFF_SYNC + 1024) + ai, tag, __ATOMIC_RELAXED, __HIP_MEMORY_SCOPE_AGENT);
    {
        const int kap = i16 >> 1, plo = i16 & 1, pl = g >> 1;
#pragma unroll 1
        for (int j = 0; j < nset; ++j) {
            const int s0 = ai + 96 * j;
            float f[4];
#pragma unroll
            for (int e = 0; e < 4; ++e) {
                const int a = 4 * (g & 1) + e;
                const float th = (float)kap * (float)(256 * a + s0) * (1.0f / 2048.0f) - ((pl != plo) ? 0.25f : 0.f);
                const float v = cosr(th);
                f[e] = (plo == 1 && pl == 0) ? -v : v;
            }
            const u32x2 fb = {pk(f[0], f[1]), pk(f[2], f[3])};
#pragma unroll
            for (int it = 0; it < 3; ++it) {
                const int T = w + 8 * it;
                if (T < 21) {
                    const u32x2 a = *(const u32x2*)(smem + L_S + j * 10752 + (16 * T + i16) * 32 + g * 8);
                    const f32x4 d = mfma16k16(a, fb, f32x4{0.f, 0.f, 0.f, 0.f});
                    if (T < 20 || g == 0)
                        *(u32x2*)(smem + L_X + j * 10368 + i16 * 648 + (16 * T + 4 * g) * 2) = u32x2{pk(d[0], d[1]), pk(d[2], d[3])};
                }
            }
        }
    }
    __syncthreads();
#pragma unroll
    for (int i = 0; i < 4; ++i) {
        const int m = min(t + 512 * i, nset * 648 - 1), j = m / 648;
        st16sc(rws, OFF_X1 + (ai + 96 * j) * X1_ROW + (m - 648 * j) * 16, sane(*(const u32x4*)(smem + L_X + m * 16)));
    }
    STAMP();
    u32x2 wv[8];
    const int wpl = w >> 2, wtask = min(t & 255, 223), wquad = wtask % 7, wr_ = wtask / 7, wk1 = wr_ & 15, woc = wr_ >> 4;
    {
        const rsrc_t rw = mkrs(wpl ? P.wi : P.wr, 2048 * 642);
        const int cs = min(colbase + 4 * wquad, 317);
#pragma unroll
        for (int e = 0; e < 8; ++e) wv[e] = ld8(rw, ((ka + 8 * (wk1 + 16 * (8 * woc + e))) * 321 + cs) * 2, 0);
    }
    fence();
    u32x4 f1re, f1im, f2re, f2im;
    float t2c[4], t2s[4];
    {
        float c[8], s[8];
#pragma unroll
        for (int j = 0; j < 8; ++j) {
            const float a = (float)(8 * (g & 1) + j) * (float)i16 * 0.0625f;
            const float co = cosr(a), si = sinr(a);
            c[j] = (g < 2) ? co : si;
            s[j] = (g < 2) ? -si : co;
        }
        f1re = pk8(c); f1im = pk8(s);
#pragma unroll
        for (int j = 0; j < 4; ++j) {
            const float pr = (float)(4 * g + j) * (float)i16;
            const float co = cosr(pr * 0.0625f), si = sinr(pr * 0.0625f);
            c[j] = co; c[4 + j] = si;
            s[j] = -si; s[4 + j] = co;
            t2c[j] = cosr(pr * (1.0f / 256.0f));
            t2s[j] = sinr(pr * (1.0f / 256.0f));
        }
        f2re = pk8(c); f2im = pk8(s);
    }
    const u32x4 ire = {f2re.x, f2re.y, f2re.z ^ 0x80008000u, f2re.w ^ 0x80008000u};
    const u32x4 iim = {f2im.x ^ 0x80008000u, f2im.y ^ 0x80008000u, f2im.z, f2im.w};
    wait_flags<96>(rws, OFF_SYNC + 1024, tag, w, l);
    STAMP();
    {
        const int task = min(t, 447), quad = task % 7, r = task / 7, pl = r >> 5, oc = r & 31;
        const u32 base = OFF_X1 + (8 * oc) * X1_ROW + (ka * 2 + pl) * 648 + (colbase + 4 * quad) * 2;
        u32x2 v[8];
        for (u32 spins = 0; spins < SPIN_MAX; ++spins) {
            fence();
            bool ok = true;
#pragma unroll
            for (int e = 0; e < 8; ++e) v[e] = ld8(rws, base + e * X1_ROW, 1);
#pragma unroll
            for (int e = 0; e < 8; ++e) ok &= fresh2(v[e]);
            if (__all(ok)) break;
            __builtin_amdgcn_s_sleep(1);
        }
        u32x4 o[4];
        tr8x4(v, o);
#pragma unroll
        for (int c = 0; c < 4; ++c) {
            const int m = (4 * quad + c) * 2 + pl;
            *(u32x4*)(smem + L_C + m * 512 + ((oc ^ ((m >> 3) & 7)) * 16)) = o[c];
        }
    }
    {
        if (colbase + 4 * wquad > 317) {
#pragma unroll
            for (int e = 0; e < 8; ++e) wv[e] = u32x2{wv[e].y >> 16, 0u};
        }
        u32x4 o[4];
        tr8x4(wv, o);
#pragma unroll
        for (int c = 0; c < 4; ++c) {
            const int m = (4 * wquad + c) * 2 + wpl;
            *(u32x4*)(smem + L_W + m * 512 + (((wk1 * 2 + woc) ^ ((m >> 3) & 7)) * 16)) = o[c];
        }
    }
    STAMP();
    __syncthreads();
    {
        const int q = i16 >> 2, p = i16 & 3;
#pragma unroll 1
        for (int col = w; col < 28; col += 8) {
            const f32x4 zero = {0.f, 0.f, 0.f, 0.f};
            const int mi = col * 2 + (g >> 1), swi = (mi >> 3) & 7;
            const char* ib = smem + L_C + mi * 512 + (p & 1) * 8;
            const u32x2 a0 = trread(ib + (((2 * (8 * (g & 1) + q) + (p >> 1)) ^ swi) * 16));
            const u32x2 a1 = trread(ib + (((2 * (8 * (g & 1) + 4 + q) + (p >> 1)) ^ swi) * 16));
            const u32x4 a = {a0.x, a0.y, a1.x, a1.y};
            const int m0 = col * 2, sw = (m0 >> 3) & 7;
            const int woff = (((i16 * 2 + (g >> 1)) ^ sw) * 16) + (g & 1) * 8;
            const u32x2 wre = *(const u32x2*)(smem + L_W + m0 * 512 + woff);
            const u32x2 wim = *(const u32x2*)(smem + L_W + (m0 + 1) * 512 + woff);
            f32x4 re = mfma16(a, f1re, zero), im = mfma16(a, f1im, zero);
            float x[8];
#pragma unroll
            for (int i = 0; i < 4; ++i) {
                x[i] = re[i] * t2c[i] + im[i] * t2s[i];
                x[4 + i] = im[i] * t2c[i] - re[i] * t2s[i];
            }
            u32x4 d = pk8(x);
            re = mfma16(f2re, d, zero); im = mfma16(f2im, d, zero);
#pragma unroll
            for (int i = 0; i < 4; ++i) {
                const float a_ = (i & 1) ? bfhi(wre[i >> 1]) : bflo(wre[i >> 1]);
                const float b_ = (i & 1) ? bfhi(wim[i >> 1]) : bflo(wim[i >> 1]);
                x[i] = re[i] * a_ - im[i] * b_;
                x[4 + i] = re[i] * b_ + im[i] * a_;
            }
            d = pk8(x);
            re = mfma16(d, ire, zero); im = mfma16(d, iim, zero);
#pragma unroll
            for (int i = 0; i < 4; ++i) {
                x[i] = re[i] * t2c[i] - im[i] * t2s[i];
                x[4 + i] = im[i] * t2c[i] + re[i] * t2s[i];
            }
            d = pk8(x);
            re = mfma16(d, ire, zero); im = mfma16(d, iim, zero);
            const int ooff = (((2 * i16 + (g >> 1)) ^ sw) * 16) + (g & 1) * 8;
            *(u32x2*)(smem + L_C + m0 * 512 + ooff) = u32x2{pk(re[0], re[1]), pk(re[2], re[3])};
            *(u32x2*)(smem + L_C + (m0 + 1) * 512 + ooff) = u32x2{pk(im[0], im[1]), pk(im[2], im[3])};
        }
    }
    STAMP();
    wait_flags<160>(rws, OFF_SYNC + 2048, tag, w, l);
    if (ai == 0 && t == 0) __hip_atomic_store((GAS u32*)(P.ws + OFF_SYNC), tag, __ATOMIC_RELAXED, __HIP_MEMORY_SCOPE_AGENT);
    {
        const int q = i16 >> 2, p = i16 & 3;
#pragma unroll
        for (int it = 0; it < 7; ++it) {
            const int cmb = (w * 7 + it) * 4 + g;
            const int quad = cmb % 7, r = cmb / 7, pl = r & 1, G = r >> 1;
            const int m = (4 * quad + q) * 2 + pl;
            const u32x2 v = trread(smem + L_C + m * 512 + (((2 * G + (p >> 1)) ^ ((m >> 3) & 7)) * 16) + (p & 1) * 8);
            *(u32x2*)(smem + L_W + (16 * G + i16) * 112 + pl * 56 + quad * 8) = v;
        }
    }
    __syncthreads();
#pragma unroll
    for (int i = 0; i < 4; ++i) {
        const int m = min(t + 512 * i, 1791);
        st16sc(rws, OFF_Y1 + ai * Y1_BLK + m * 16, sane(*(const u32x4*)(smem + L_W + m * 16)));
    }
    STAMP();

    u32x4 C[2], S[2], nsS[2], sC[2], F[2];
    float twc[8], tws[8];
    {
        const u32 sgn = (g >= 2) ? 0x80008000u : 0u;
#pragma unroll
        for (int tau = 0; tau < 2; ++tau) {
            float c[8], s[8];
            const float d1 = (float)(16 * tau + i16);
#pragma unroll
            for (int j = 0; j < 8; ++j) {
                const float a = (float)(8 * g + j) * d1 * 0.03125f;
                c[j] = cosr(a);
                s[j] = sinr(a);
            }
            C[tau] = pk8(c); S[tau] = pk8(s);
            nsS[tau] = S[tau] ^ (sgn ^ 0x80008000u);
            sC[tau] = C[tau] ^ sgn;
#pragma unroll
            for (int i = 0; i < 4; ++i) {
                const float a = d1 * (float)(4 * g + i) * (1.0f / 640.0f);
                twc[4 * tau + i] = cosr(a) * (1.0f / (2048.0f * 640.0f));
                tws[4 * tau + i] = sinr(a) * (1.0f / (2048.0f * 640.0f));
            }
        }
#pragma unroll
        for (int dl = 0; dl < 2; ++dl) {
            float v[8];
#pragma unroll
            for (int j = 0; j < 8; ++j) {
                const int k2 = 4 * g + (j & 3), d2 = 16 * dl + i16;
                const float wgt = (k2 > 10 || d2 >= 20) ? 0.f : ((k2 == 0 || k2 == 10) ? 1.f : 2.f);
                const float a = (float)k2 * (float)d2 * 0.05f;
                v[j] = (j < 4) ? wgt * cosr(a) : -wgt * sinr(a);
            }
            F[dl] = pk8(v);
        }
    }
    {
        if (w == 0) {
            for (u32 spins = 0; spins < SPIN_MAX; ++spins) {
                fence();
                const u32x4 f = ld16(rws, OFF_Y1 + (l + l / 2) * Y1_BLK + ai * 112 + 96, 1);
                if (__all(fresh4(f))) break;
                __builtin_amdgcn_s_sleep(1);
            }
        }
        __syncthreads();
        STAMP();
        u32 off[4];
#pragma unroll
        for (int i = 0; i < 4; ++i) {
            const int m = min(t + 512 * i, nset * 672 - 1), j = m / 672, mm = m - 672 * j, ab = mm / 7;
            off[i] = OFF_Y1 + ab * Y1_BLK + (ai + 96 * j) * 112 + (mm - 7 * ab) * 16;
        }
        u32x4 v[4];
        for (u32 spins = 0; spins < SPIN_MAX; ++spins) {
            fence();
#pragma unroll
            for (int i = 0; i < 4; ++i) v[i] = ld16(rws, off[i], 1);
            if (__all(fresh4(v[0]) && fresh4(v[1]) && fresh4(v[2]) && fresh4(v[3]))) break;
            __builtin_amdgcn_s_sleep(1);
        }
#pragma unroll
        for (int i = 0; i < 4; ++i) *(u32x4*)(smem + L_X + min(t + 512 * i, nset * 672 - 1) * 16) = v[i];
    }
    STAMP();
    __syncthreads();
    {
        const int q = i16 >> 2, p = i16 & 3;
        const int kap = 2 * g + (q >> 1), pl = q & 1;
        const int plo = i16 >> 3, a = i16 & 7;
#pragma unroll 1
        for (int j = 0; j < nset; ++j) {
            const int s0 = ai + 96 * j;
            float f[4];
#pragma unroll
            for (int e = 0; e < 4; ++e) {
                const int kk = 2 * g + (e >> 1), pp = e & 1;
                const float th = (float)kk * (float)(256 * a + s0) * (1.0f / 2048.0f) - ((pp != plo) ? 0.25f : 0.f);
                const float v = cosr(th);
                f[e] = (plo == 0 && pp == 1) ? -v : v;
            }
            const u32x2 fa = {pk(f[0], f[1]), pk(f[2], f[3])};
#pragma unroll
            for (int it = 0; it < 3; ++it) {
                const int T = w + 8 * it;
                if (T < 21) {
                    const int k = min(16 * T + 4 * p, 320);
                    const int cgk = min(k / 28, 11), idx = k - min(28 * cgk, 296);
                    const u32x2 bfr = trread(smem + L_X + j * 10752 + (kap * 12 + cgk) * 112 + pl * 56 + idx * 2);
                    const f32x4 d = mfma16k16(fa, bfr, f32x4{0.f, 0.f, 0.f, 0.f});
                    *(u32x2*)(smem + L_S + j * 10752 + (16 * T + i16) * 32 + g * 8) = u32x2{pk(d[0], d[1]), pk(d[2], d[3])};
                }
            }
        }
    }
    __syncthreads();
#pragma unroll 1
    for (int task = w; task < 4 * nset; task += 8) {
        const int j = task >> 2, wp = task & 3;
        const char* sb = smem + L_S + j * 10752 + wp * 4;
        const int k2 = i16 <= 10 ? i16 : 10;
        u32 dre[8], dim[8];
#pragma unroll
        for (int e = 0; e < 8; ++e) {
            const int k = k2 + 20 * (8 * g + e);
            const int kk = (g < 2) ? k : 640 - k;
            dre[e] = *(const u32*)(sb + kk * 32);
            dim[e] = *(const u32*)(sb + kk * 32 + 16);
        }
#pragma unroll
        for (int rr = 0; rr < 2; ++rr) {
            u32x4 are, aim;
            if (rr == 0) {
                are = u32x4{permlo(dre[0], dre[1]), permlo(dre[2], dre[3]), permlo(dre[4], dre[5]), permlo(dre[6], dre[7])};
                aim = u32x4{permlo(dim[0], dim[1]), permlo(dim[2], dim[3]), permlo(dim[4], dim[5]), permlo(dim[6], dim[7])};
            } else {
                are = u32x4{permhi(dre[0], dre[1]), permhi(dre[2], dre[3]), permhi(dre[4], dre[5]), permhi(dre[6], dre[7])};
                aim = u32x4{permhi(dim[0], dim[1]), permhi(dim[2], dim[3]), permhi(dim[4], dim[5]), permhi(dim[6], dim[7])};
            }
            u32x4 va[2];
            const f32x4 zero = {0.f, 0.f, 0.f, 0.f};
#pragma unroll
            for (int tau = 0; tau < 2; ++tau) {
                const f32x4 vre = mfma16(aim, nsS[tau], mfma16(are, C[tau], zero));
                const f32x4 vim = mfma16(aim, sC[tau], mfma16(are, S[tau], zero));
                float pr[8];
#pragma unroll
                for (int i = 0; i < 4; ++i) {
                    pr[i] = vre[i] * twc[4 * tau + i] - vim[i] * tws[4 * tau + i];
                    pr[4 + i] = vim[i] * twc[4 * tau + i] + vre[i] * tws[4 * tau + i];
                }
                va[tau] = pk8(pr);
            }
            u16* orow = P.out + (size_t)(ai + 96 * j + 256 * (2 * wp + rr)) * 1280;
#pragma unroll
            for (int dl = 0; dl < 2; ++dl) {
                const f32x4 y0 = mfma16(va[0], F[dl], zero);
                const f32x4 y1 = mfma16(va[1], F[dl], zero);
                u32 a0 = pk(y0[0], y0[1]), a1 = pk(y0[2], y0[3]);
                u32 b0 = pk(y1[0], y1[1]), b1 = pk(y1[2], y1[3]);
                auto s0 = __builtin_amdgcn_permlane16_swap(a0, b0, false, false);
                auto s1 = __builtin_amdgcn_permlane16_swap(a1, b1, false, false);
                const int d2 = 16 * dl + i16;
                if (d2 < 20) *(u32x4*)(orow + 32 * d2 + (g & 1) * 16 + (g >> 1) * 8) = u32x4{s0[0], s1[0], s0[1], s1[1]};
            }
        }
    }
    STAMP();
    { const bool PROBE_ROLE_OK = (PROBE_A && ai == 0); STAMP_OUT(P.ws); }
}

DI void role_b(const Args& P, char* smem, const int t, const int w, const int xcd, const int jx, const u32 tag) {
    const int l = t & 63, g = l >> 4, i16 = l & 15;
    const int chb = 20 * xcd + jx;
    const rsrc_t rws = mkrs(P.ws, WS_USED);
    STAMP_DECL STAMP();
    if (t == 0) *(u32*)(smem + L_CNT) = 0u;
    __syncthreads();
    u32x2 gv[8];
    u32 pw0 = 0, pw1 = 0;
    if (w >= 4) {
        const int tt = t - 256;
        const rsrc_t ru = mkrs(P.u, 2048 * 1280);
        {
            const int b0 = (160 * xcd) & ~127, nl = (160 * xcd + 160 - b0 + 127) >> 7;
            const int m0 = tt, m1 = min(tt + 256, 103 * 3 - 1);
            pw0 = __builtin_amdgcn_raw_buffer_load_b32(ru, min(103 * jx + m0 / 3, 2047) * 1280 + b0 + min(m0 % 3, nl - 1) * 128, 0, 0);
            pw1 = __builtin_amdgcn_raw_buffer_load_b32(ru, min(103 * jx + m1 / 3, 2047) * 1280 + b0 + min(m1 % 3, nl - 1) * 128, 0, 0);
        }
#pragma unroll
        for (int e = 0; e < 8; ++e) gv[e] = ld8(ru, (8 * tt + e) * 1280 + chb * 8, 0);
        fence();
        if (tt < 128) *(u32x4*)(smem + L_U + (tt >> 5) * PB_U + (tt & 31) * 16) = u32x4{0u, 0u, 0u, 0u};
        else if (tt < 144) {
            const int v = tt - 128;
            *(u32x4*)(smem + L_K + (v >> 2) * PB_KS + ((v >> 1) & 1) * PB_K1 + 4096 + (v & 1) * 16) = u32x4{0u, 0u, 0u, 0u};
        }
        u32x4 o[4];
        tr8x4(gv, o);
#pragma unroll
        for (int c = 0; c < 4; ++c) *(u32x4*)(smem + L_U + c * PB_U + 512 + tt * 16) = o[c];
        asm volatile("" :: "v"(pw0), "v"(pw1));
        STAMP();
    } else {
        const int ch = 4 * chb + w;
        u32 pv[12];
        {
            const u32 nb = 640 * 128 * 2;
            const rsrc_t r0 = mkrs(P.Are, nb), r1 = mkrs(P.Aim, nb), r2 = mkrs(P.Bre, nb), r3 = mkrs(P.Bim, nb), r4 = mkrs(P.Cre, nb), r5 = mkrs(P.Cim, nb);
#pragma unroll
            for (int h = 0; h < 2; ++h) {
                const u32 off = (ch * 128 + 64 * h + l) * 2;
                pv[6 * h + 0] = ld2(r0, off); pv[6 * h + 1] = ld2(r1, off); pv[6 * h + 2] = ld2(r2, off);
                pv[6 * h + 3] = ld2(r3, off); pv[6 * h + 4] = ld2(r4, off); pv[6 * h + 5] = ld2(r5, off);
            }
        }
        const u32 ldt = ld2(mkrs(P.logdt, 1280), ch * 2);
        {
            const u32x4 s4 = {SENT, SENT, SENT, SENT};
#pragma unroll
            for (int i = 0; i < 5; ++i) st16sc(rws, OFF_Y1 + min(chb * 1076 + min(t + 256 * i, 1075), 172031) * 16, s4);
        }
        fence();
        char* sc = smem + L_G + w * KG_SZ;
        const float dt = rbf(expf(bf2f(ldt)));
        f32x4 kacc[3];
#pragma unroll
        for (int T = 0; T < 3; ++T) kacc[T] = f32x4{0.f, 0.f, 0.f, 0.f};
#pragma unroll 1
        for (int h = 0; h < 2; ++h) {
            const float are = bf2f(h ? pv[6] : pv[0]), aim = bf2f(h ? pv[7] : pv[1]), bre = bf2f(h ? pv[8] : pv[2]);
            const float bim = bf2f(h ? pv[9] : pv[3]), cre = bf2f(h ? pv[10] : pv[4]), cim = bf2f(h ? pv[11] : pv[5]);
            const float lre = dt * are, lim = dt * aim * 0.15915494309189535f;
            const float dbr = dt * bre, dbi = dt * bim;
            const cf c = {cre * dbr - cim * dbi, cre * dbi + cim * dbr};
            char* rowp = sc + l * 4;
            const cf z1 = cexpk(lre, lim, 1.f), z8 = cexpk(lre, lim, 8.f), z128 = cexpk(lre, lim, 128.f);
            const cf z2 = cmul(z1, z1), z4 = cmul(z2, z2), z6 = cmul(z2, z4);
            cf v = {1.f, 0.f}, r0 = c, r1 = cmul(c, z128);
            cf q = cmul(r1, z128);
#pragma unroll
            for (int j = 0; j < 8; ++j) {
                *(u32*)(rowp + KG_B + (2 * j) * KG_RS) = pk(v.re, v.im);
                v = cmul(v, z8);
                *(u32*)(rowp + KG_B + (2 * j + 1) * KG_RS) = pk(v.re, v.im);
                v = cmul(v, z8);
                *(u32*)(rowp + j * KG_RS) = pk(r0.re, -r0.im);
                *(u32*)(rowp + (8 + j) * KG_RS) = pk(r1.re, -r1.im);
                r0 = cmul(r0, z1);
                r1 = cmul(r1, z1);
#pragma unroll
                for (int e = 0; e < 2; ++e) {
                    const int hh = 2 + 2 * j + e;
                    if (hh < 4) {
                        const int r = 16 + (hh - 2) * 4;
                        const cf a = cmul(q, z2), bq = cmul(q, z4), cq = cmul(q, z6);
                        *(u32*)(rowp + r * KG_RS) = pk(q.re, -q.im);
                        *(u32*)(rowp + (r + 1) * KG_RS) = pk(a.re, -a.im);
                        *(u32*)(rowp + (r + 2) * KG_RS) = pk(bq.re, -bq.im);
                        *(u32*)(rowp + (r + 3) * KG_RS) = pk(cq.re, -cq.im);
                    } else if (hh < 8) {
                        const int r = 24 + (hh - 4) * 2;
                        const cf bq = cmul(q, z4);
                        *(u32*)(rowp + r * KG_RS) = pk(q.re, -q.im);
                        *(u32*)(rowp + (r + 1) * KG_RS) = pk(bq.re, -bq.im);
                    } else if (hh <= 16) {
                        *(u32*)(rowp + (32 + hh - 8) * KG_RS) = pk(q.re, -q.im);
                    }
                    if (hh < 16) q = cmul(q, z128);
                }
            }
            const char* bp = sc + KG_B + i16 * KG_RS + g * 16;
#pragma unroll
            for (int ks = 0; ks < 4; ++ks) {
                const u32x4 bf = *(const u32x4*)(bp + ks * 64);
#pragma unroll
                for (int T = 0; T < 3; ++T)
                    kacc[T] = mfma16(*(const u32x4*)(sc + (16 * T + i16) * KG_RS + g * 16 + ks * 64), bf, kacc[T]);
            }
        }
#pragma unroll
        for (int T = 0; T < 3; ++T)
#pragma unroll
            for (int i = 0; i < 4; ++i) {
                const int row = 16 * T + 4 * g + i;
                int e;
                if (T == 0) e = 128 * (row >> 3) + (row & 7);
                else if (T == 1) e = (row < 24) ? 256 + 128 * ((row - 16) >> 2) + 2 * ((row - 16) & 3) : 512 + 128 * ((row - 24) >> 1) + 4 * ((row - 24) & 1);
                else e = 1024 + 128 * (row - 32);
                if (row < 40 || (row == 40 && i16 == 0))
                    *(u16*)(sc + KG_T + (e + 8 * i16) * 2) = (u16)(pk(kacc[T][i], kacc[T][i]) & 0xffffu);
            }
        drain();
        if (l == 0) {
            const u32 old = __hip_atomic_fetch_add((LAS u32*)(smem + L_CNT), 1u, __ATOMIC_RELAXED, __HIP_MEMORY_SCOPE_WORKGROUP);
            if (old == 3u) __hip_atomic_store((GAS u32*)(P.ws + OFF_SYNC + 2048) + chb, tag, __ATOMIC_RELAXED, __HIP_MEMORY_SCOPE_AGENT);
        }
#pragma unroll 1
        for (int it = 0; it < 4; ++it) {
            const int tt = l + 64 * it, a = 255 - tt;
            u32 code;
            if (a < 32) code = 0x76543210u;
            else if (a < 64) code = 0x86444200u;
            else if (a < 128) code = 0x88444000u;
            else code = (a & 1) ? 0x88880000u : 0x88800000u;
            const int lastoff = (a <= 32) ? 7 : 8;
            const char* tb = sc + KG_T + 16 * a;
            u32 v[8];
#pragma unroll
            for (int e = 0; e < 8; ++e) v[e] = *(const u16*)(tb + 2 * ((code >> (4 * e)) & 15));
            u32 vm = *(const u16*)(tb - 16 + 2 * lastoff);
            vm = (a == 0) ? 0u : vm;
            char* kb = smem + L_K + w * PB_KS + tt * 16;
            *(u32x4*)(kb) = u32x4{v[7] | (v[6] << 16), v[5] | (v[4] << 16), v[3] | (v[2] << 16), v[1] | (v[0] << 16)};
            *(u32x4*)(kb + PB_K1) = u32x4{v[6] | (v[5] << 16), v[4] | (v[3] << 16), v[2] | (v[1] << 16), v[0] | (vm << 16)};
        }
        STAMP();
    }
    const int ch = w & 3, rh = w >> 2;
    const float dsk = bf2f(ld2(mkrs(P.dskip, 1280), (4 * chb + ch) * 2));
    __syncthreads();
    STAMP();
    const int mu = g >> 1, hp = g & 1;
    const int x0 = 2047 - 16 * mu - i16 + 8 * hp;
    const char* ap = smem + L_K + ch * PB_KS + ((x0 & 1) ? PB_K1 + (x0 - 1) * 2 : x0 * 2);
    const char* bp = smem + L_U + ch * PB_U + (256 + 16 * (i16 - mu) + 8 * hp) * 2;
    f32x4 acc[8];
#pragma unroll
    for (int c = 0; c < 8; ++c) acc[c] = f32x4{0.f, 0.f, 0.f, 0.f};
#pragma unroll 1
    for (int r = 4 * rh; r < 4 * rh + 4; ++r) {
        u32x4 A[8], B[8];
#pragma unroll
        for (int p = 0; p < 8; ++p) {
            const u32* a = (const u32*)(ap - 64 * r - 512 * p);
            A[p] = u32x4{a[0], a[1], a[2], a[3]};
            B[p] = *(const u32x4*)(bp - 64 * r + 512 * p);
        }
#pragma unroll
        for (int p = 0; p < 8; ++p)
#pragma unroll
            for (int q = 0; q + p < 8; ++q) acc[p + q] = mfma16(A[p], B[q], acc[p + q]);
    }
    STAMP();
    char* rb = smem + L_G;
#pragma unroll
    for (int c = 0; c < 4; ++c) *(f32x4*)(rb + (w * 4 + c) * 1024 + l * 16) = rh ? acc[c] : acc[4 + c];
    __syncthreads();
    char* st = smem + L_G + 32768;
#pragma unroll
    for (int c = 0; c < 4; ++c) {
        const f32x4 o = *(const f32x4*)(rb + ((w ^ 4) * 4 + c) * 1024 + l * 16);
        const f32x4 mine = rh ? acc[4 + c] : acc[c];
        const int tt = 256 * (4 * rh + c) + 16 * i16 + 4 * g;
        const u32x2 uu = *(const u32x2*)(smem + L_U + ch * PB_U + (256 + tt) * 2);
#pragma unroll
        for (int i = 0; i < 4; ++i) {
            const float uf = (i & 1) ? bfhi(i < 2 ? uu.x : uu.y) : bflo(i < 2 ? uu.x : uu.y);
            const float v = rbf(mine[i] + o[i]) + rbf(uf * dsk);
            *(u16*)(st + (tt >> 4) * 128 + (((4 * g + i) ^ i16) * 8) + ch * 2) = (u16)(pk(v, v) & 0xffffu);
        }
    }
    __syncthreads();
    STAMP();
    {
        const rsrc_t ro = mkrs(P.out, 2048 * 2560);
#pragma unroll
        for (int i = 0; i < 4; ++i) {
            const int row = t + 512 * i;
            __builtin_amdgcn_raw_buffer_store_b64(*(const u32x2*)(st + (row >> 4) * 128 + (((row & 15) ^ ((row >> 4) & 15)) * 8)), ro,
                                                  row * 2560 + 1280 + chb * 8, 0, 0);
        }
    }
    STAMP();
    { const bool PROBE_ROLE_OK = (!PROBE_A && chb == 0); STAMP_OUT(P.ws); }
}

__global__ void __launch_bounds__(512, 2) fused_kernel(Args P) {
    extern __shared__ __attribute__((aligned(16))) char smem[];
    const int t = threadIdx.x;
    const int w = __builtin_amdgcn_readfirstlane(t >> 6);
    const int b = blockIdx.x, xcd = b & 7, jx = b >> 3;
    const u32 tag = __hip_atomic_load((GAS u32*)(P.ws + OFF_SYNC), __ATOMIC_RELAXED, __HIP_MEMORY_SCOPE_AGENT) + 1u;
    if (jx < 20) role_b(P, smem, t, w, xcd, jx, tag);
    else role_a(P, smem, t, w, xcd, jx - 20, tag);
}

extern "C" void kernel_launch(void* const* d_in, const int* in_sizes, int n_in,
                              void* d_out, int out_size, void* d_ws, size_t ws_size,
                              hipStream_t stream) {
    Args a;
    a.x = (const u16*)d_in[0];
    a.wr = (const u16*)d_in[1];
    a.wi = (const u16*)d_in[2];
    a.u = (const u16*)d_in[3];
    a.Are = (const u16*)d_in[4];
    a.Aim = (const u16*)d_in[5];
    a.Bre = (const u16*)d_in[6];
    a.Bim = (const u16*)d_in[7];
    a.Cre = (const u16*)d_in[8];
    a.Cim = (const u16*)d_in[9];
    a.logdt = (const u16*)d_in[10];
    a.dskip = (const u16*)d_in[11];
    a.ws = (char*)d_ws;
    a.out = (u16*)d_out;
    (void)hipFuncSetAttribute((const void*)fused_kernel, hipFuncAttributeMaxDynamicSharedMemorySize, LDS_BYTES);
    fused_kernel<<<256, 512, LDS_BYTES, stream>>>(a);
}
```

```cpp
#include <hip/hip_runtime.h>
#include <hip/hip_bf16.h>

typedef unsigned short u16;
typedef unsigned int u32;
typedef __attribute__((ext_vector_type(2))) __bf16 bf16x2;
typedef __attribute__((ext_vector_type(8))) __bf16 bf16x8;
typedef __attribute__((ext_vector_type(2))) float f32x2;
typedef __attribute__((ext_vector_type(4))) float f32x4;
typedef __attribute__((ext_vector_type(4))) u32 u32x4;
typedef __attribute__((ext_vector_type(2))) u32 u32x2;
typedef __attribute__((ext_vector_type(4))) short s16x4;
typedef __amdgpu_buffer_rsrc_t rsrc_t;
#define LAS __attribute__((address_space(3)))
#define GAS __attribute__((address_space(1)))
#define DI __device__ __forceinline__
#define SC1 16

constexpr u32 X1_ROW = 10368;
constexpr u32 OFF_X1 = 0;
constexpr u32 Y1_BLK = 28672;
constexpr u32 OFF_Y1 = 256 * X1_ROW;
constexpr u32 OFF_SYNC = OFF_Y1 + 96 * Y1_BLK;
constexpr u32 WS_USED = OFF_SYNC + 4096;
constexpr u32 SENT = 0xFFFFFFFFu;
constexpr u32 SPIN_MAX = 1u << 16;
constexpr int OUTA = 16, OUTB = 16;

constexpr int L_X = 0;
constexpr int L_S = 32256;
constexpr int L_W = 64512;
constexpr int L_C = L_W + 28672;
constexpr int PB_U = 4608;
constexpr int L_U = 0;
constexpr int L_K = L_U + 4 * PB_U;
constexpr int PB_K1 = 4160, PB_KS = 8320;
constexpr int L_G = L_K + 4 * PB_KS;
constexpr int KG_RS = 272, KG_B = 41 * KG_RS, KG_T = 57 * KG_RS, KG_SZ = 19616;
constexpr int L_CNT = L_G + 4 * KG_SZ;
constexpr int LDS_BYTES = L_CNT + 16;

#define STAMP_DECL
#define STAMP()
#define STAMP_OUT(ws)

DI u32 pk(float a, float b) {
    f32x2 v = {a, b};
    return __builtin_bit_cast(u32, __builtin_convertvector(v, bf16x2));
}
DI u32x4 pk8(const float* v) { return u32x4{pk(v[0], v[1]), pk(v[2], v[3]), pk(v[4], v[5]), pk(v[6], v[7])}; }
DI float bflo(u32 v) { return __uint_as_float(v << 16); }
DI float bfhi(u32 v) { return __uint_as_float(v & 0xffff0000u); }
DI float bf2f(u32 v) { return __uint_as_float(v << 16); }
DI float rbf(float a) { return bflo(pk(a, a)); }
DI float cosr(float rev) { return __builtin_amdgcn_cosf(rev); }
DI float sinr(float rev) { return __builtin_amdgcn_sinf(rev); }
DI f32x4 mfma16(u32x4 a, u32x4 b, f32x4 c) {
    return __builtin_amdgcn_mfma_f32_16x16x32_bf16(__builtin_bit_cast(bf16x8, a), __builtin_bit_cast(bf16x8, b), c, 0, 0, 0);
}
DI f32x4 mfma16k16(u32x2 a, u32x2 b, f32x4 c) {
    return __builtin_amdgcn_mfma_f32_16x16x16bf16_1k(__builtin_bit_cast(s16x4, a), __builtin_bit_cast(s16x4, b), c, 0, 0, 0);
}
DI u32x2 trread(const char* p) {
    return __builtin_bit_cast(u32x2, __builtin_amdgcn_ds_read_tr16_b64_v4i16((LAS s16x4*)(p)));
}
DI u32 permlo(u32 a, u32 b) { return __builtin_amdgcn_perm(b, a, 0x05040100u); }
DI u32 permhi(u32 a, u32 b) { return __builtin_amdgcn_perm(b, a, 0x07060302u); }
DI rsrc_t mkrs(const void* p, u32 bytes) { return __builtin_amdgcn_make_buffer_rsrc((void*)p, 0, bytes, 0x00020000); }
DI u32x4 ld16(rsrc_t r, u32 off, int aux) { return aux ? __builtin_amdgcn_raw_buffer_load_b128(r, off, 0, SC1) : __builtin_amdgcn_raw_buffer_load_b128(r, off, 0, 0); }
DI u32x2 ld8(rsrc_t r, u32 off, int aux) { return aux ? __builtin_amdgcn_raw_buffer_load_b64(r, off, 0, SC1) : __builtin_amdgcn_raw_buffer_load_b64(r, off, 0, 0); }
DI u32 ld2(rsrc_t r, u32 off) { return __builtin_amdgcn_raw_buffer_load_b16(r, off, 0, 0); }
DI void st16sc(rsrc_t r, u32 off, u32x4 v) { __builtin_amdgcn_raw_buffer_store_b128(v, r, off, 0, SC1); }
DI u32x4 sane(u32x4 v) { return u32x4{min(v.x, SENT - 1), min(v.y, SENT - 1), min(v.z, SENT - 1), min(v.w, SENT - 1)}; }
DI bool fresh4(u32x4 v) { return max(max(v.x, v.y), max(v.z, v.w)) != SENT; }
DI bool fresh2(u32x2 v) { return max(v.x, v.y) != SENT; }
DI void fence() { asm volatile("" ::: "memory"); }
DI void drain() { asm volatile("s_waitcnt vmcnt(0)" ::: "memory"); }
DI void tr8x4(const u32x2* v, u32x4* o) {
    o[0] = u32x4{permlo(v[0].x, v[1].x), permlo(v[2].x, v[3].x), permlo(v[4].x, v[5].x), permlo(v[6].x, v[7].x)};
    o[1] = u32x4{permhi(v[0].x, v[1].x), permhi(v[2].x, v[3].x), permhi(v[4].x, v[5].x), permhi(v[6].x, v[7].x)};
    o[2] = u32x4{permlo(v[0].y, v[1].y), permlo(v[2].y, v[3].y), permlo(v[4].y, v[5].y), permlo(v[6].y, v[7].y)};
    o[3] = u32x4{permhi(v[0].y, v[1].y), permhi(v[2].y, v[3].y), permhi(v[4].y, v[5].y), permhi(v[6].y, v[7].y)};
}
template <int NF>
DI void wait_flags(rsrc_t rws, u32 off, u32 tag, int w, int l) {
    if (w == 0) {
        const int q = min(l, NF / 4 - 1);
        for (u32 spins = 0; spins < SPIN_MAX; ++spins) {
            fence();
            const u32x4 f = ld16(rws, off + q * 16, 1);
            if (__all(f.x == tag && f.y == tag && f.z == tag && f.w == tag)) break;
            __builtin_amdgcn_s_sleep(2);
        }
    }
    __syncthreads();
}

struct cf { float re, im; };
DI cf cmul(cf a, cf b) { return cf{a.re * b.re - a.im * b.im, a.re * b.im + a.im * b.re}; }
DI cf cexpk(float lre, float lim_rev, float k) {
    const float m = __builtin_amdgcn_exp2f(k * lre * 1.44269504088896341f);
    const float a = __builtin_amdgcn_fractf(k * lim_rev);
    return cf{m * cosr(a), m * sinr(a)};
}

struct Args {
    const u16 *x, *wr, *wi, *u, *Are, *Aim, *Bre, *Bim, *Cre, *Cim, *logdt, *dskip;
    char* ws;
    u16* out;
};

DI void role_a(const Args& P, char* smem, const int t, const int w, const int ka, const int cg, const u32 tag) {
    const int l = t & 63, g = l >> 4, i16 = l & 15;
    const int ai = ka * 12 + cg, nset = ai < 64 ? 3 : 2;
    const int colbase = min(28 * cg, 296);
    const rsrc_t rws = mkrs(P.ws, WS_USED);
    STAMP_DECL STAMP();
    {
        const u32x4 s4 = {SENT, SENT, SENT, SENT};
#pragma unroll
        for (int i = 0; i < 4; ++i) {
            const int m = min(t + 512 * i, nset * 648 - 1), j = m / 648;
            st16sc(rws, OFF_X1 + (ai + 96 * j) * X1_ROW + (m - 648 * j) * 16, s4);
        }
    }
    u32x4 xv[2][3];
    {
        const rsrc_t rx = mkrs(P.x, 2048 * 1280);
#pragma unroll
        for (int it = 0; it < 2; ++it) {
            const int task = min(w + 8 * it, 4 * nset - 1), s0 = ai + 96 * (task >> 2);
#pragma unroll
            for (int i = 0; i < 3; ++i) {
                const int ci = min(l + 64 * i, 159), row = ci >= 80;
                xv[it][i] = ld16(rx, (s0 + 256 * (2 * (task & 3) + row)) * 1280 + (ci - 80 * row) * 16, 0);
            }
        }
    }
    fence();
    if (t < 90) *(u32x4*)(smem + L_S + (t / 30) * 10752 + 321 * 32 + (t % 30) * 16) = u32x4{0u, 0u, 0u, 0u};

    {
        u32x4 f20c, f20s, c32, s32;
        float twc[8], tws[8];
        {
            float c[8], s[8];
#pragma unroll
            for (int j = 0; j < 8; ++j) {
                const int d2 = (j < 4) ? 4 * g + j : 16 + 4 * g + j - 4;
                const bool ok = (d2 < 20) && (i16 <= 10);
                const float a = (float)d2 * (float)i16 * 0.05f;
                c[j] = ok ? cosr(a) : 0.f;
                s[j] = ok ? -sinr(a) : 0.f;
            }
            f20c = pk8(c); f20s = pk8(s);
#pragma unroll
            for (int j = 0; j < 8; ++j) {
                const int d1 = (j < 4) ? 4 * g + j : 16 + 4 * g + j - 4;
                const float pr = (float)d1 * (float)i16;
                c[j] = cosr(pr * 0.03125f);
                s[j] = sinr(pr * 0.03125f);
                twc[j] = cosr(pr * (1.0f / 640.0f));
                tws[j] = sinr(pr * (1.0f / 640.0f));
            }
            c32 = pk8(c); s32 = pk8(s);
        }
        const u32x4 ns32 = s32 ^ 0x80008000u, c32b = c32 ^ 0x80000000u, s32b = s32 ^ 0x80000000u, nc32b = c32b ^ 0x80008000u;
        const int q = i16 >> 2, p = i16 & 3;
        const int d2a = 4 * g + q, d2b = (g == 0) ? 16 + q : d2a;
        const int offa = 64 * d2a + 8 * p, swa = ((d2a >> 2) & 1) << 5;
        const int offb = 64 * d2b + 8 * p, swb = ((d2b >> 2) & 1) << 5;
#pragma unroll
        for (int it = 0; it < 2; ++it) {
            const int task = w + 8 * it;
            if (task < 4 * nset) {
                char* xs = smem + L_X + task * 2560;
#pragma unroll
                for (int i = 0; i < 3; ++i) {
                    const int ci = min(l + 64 * i, 159), c = ci >= 80 ? ci - 80 : ci;
                    *(u32x4*)(xs + ((ci * 16) ^ (((c >> 4) & 1) << 5))) = xv[it][i];
                }
                f32x4 z[2][4];
#pragma unroll
                for (int rr = 0; rr < 2; ++rr) {
                    const char* xr = xs + rr * 1280;
                    f32x4 tre[2], tim[2];
                    const f32x4 zero = {0.f, 0.f, 0.f, 0.f};
#pragma unroll
                    for (int tau = 0; tau < 2; ++tau) {
                        const u32x2 a0 = trread(xr + offa + ((32 * tau) ^ swa));
                        const u32x2 a1 = trread(xr + offb + ((32 * tau) ^ swb));
                        const u32x4 a = {a0.x, a0.y, a1.x, a1.y};
                        tre[tau] = mfma16(a, f20c, zero);
                        tim[tau] = mfma16(a, f20s, zero);
                    }
                    float pr[8], pi[8];
#pragma unroll
                    for (int j = 0; j < 8; ++j) {
                        const float a = tre[j >> 2][j & 3], bb = tim[j >> 2][j & 3];
                        pr[j] = a * twc[j] + bb * tws[j];
                        pi[j] = bb * twc[j] - a * tws[j];
                    }
                    const u32x4 bre = pk8(pr), bim = pk8(pi);
                    z[rr][0] = mfma16(s32, bim, mfma16(c32, bre, zero));
                    z[rr][1] = mfma16(ns32, bre, mfma16(c32, bim, zero));
                    z[rr][2] = mfma16(s32b, bim, mfma16(c32b, bre, zero));
                    z[rr][3] = mfma16(s32b, bre, mfma16(nc32b, bim, zero));
                }
                char* st = smem + L_S + (task >> 2) * 10752 + (task & 3) * 4;
                if (i16 <= 10) {
#pragma unroll
                    for (int i = 0; i < 4; ++i) {
                        const int k = i16 + 20 * (4 * g + i);
                        *(u32*)(st + k * 32) = pk(z[0][0][i], z[1][0][i]);
                        *(u32*)(st + k * 32 + 16) = pk(z[0][1][i], z[1][1][i]);
                    }
#pragma unroll
                    for (int i = 0; i < 4; ++i) {
                        const int k = 320 - i16 - 20 * (4 * g + i);
                        const bool ok = (i16 >= 1 && i16 <= 9) || (i16 == 0 && g == 0 && i == 0);
                        if (ok) {
                            *(u32*)(st + k * 32) = pk(z[0][2][i], z[1][2][i]);
                            *(u32*)(st + k * 32 + 16) = pk(z[0][3][i], z[1][3][i]);
                        }
                    }
                }
            }
        }
        drain();
    }
    STAMP();
    __syncthreads();
    if (t == 0) __hip_atomic_store((GAS u32*)(P.ws + OFF_SYNC + 1024) + ai, tag, __ATOMIC_RELAXED, __HIP_MEMORY_SCOPE_AGENT);
    {
        const int kap = i16 >> 1, plo = i16 & 1, pl = g >> 1;
#pragma unroll 1
        for (int j = 0; j < nset; ++j) {
            const int s0 = ai + 96 * j;
            float f[4];
#pragma unroll
            for (int e = 0; e < 4; ++e) {
                const int a = 4 * (g & 1) + e;
                const float th = (float)kap * (float)(256 * a + s0) * (1.0f / 2048.0f) - ((pl != plo) ? 0.25f : 0.f);
                const float v = cosr(th);
                f[e] = (plo == 1 && pl == 0) ? -v : v;
            }
            const u32x2 fb = {pk(f[0], f[1]), pk(f[2], f[3])};
#pragma unroll
            for (int it = 0; it < 3; ++it) {
                const int T = w + 8 * it;
                if (T < 21) {
                    const u32x2 a = *(const u32x2*)(smem + L_S + j * 10752 + (16 * T + i16) * 32 + g * 8);
                    const f32x4 d = mfma16k16(a, fb, f32x4{0.f, 0.f, 0.f, 0.f});
                    if (T < 20 || g == 0)
                        *(u32x2*)(smem + L_X + j * 10368 + i16 * 648 + (16 * T + 4 * g) * 2) = u32x2{pk(d[0], d[1]), pk(d[2], d[3])};
                }
            }
        }
    }
    __syncthreads();
#pragma unroll
    for (int i = 0; i < 4; ++i) {
        const int m = min(t + 512 * i, nset * 648 - 1), j = m / 648;
        st16sc(rws, OFF_X1 + (ai + 96 * j) * X1_ROW + (m - 648 * j) * 16, sane(*(const u32x4*)(smem + L_X + m * 16)));
    }
    STAMP();
    u32x2 wv[8];
    const int wpl = w >> 2, wtask = min(t & 255, 223), wquad = wtask % 7, wr_ = wtask / 7, wk1 = wr_ & 15, woc = wr_ >> 4;
    {
        const rsrc_t rw = mkrs(wpl ? P.wi : P.wr, 2048 * 642);
        const int cs = min(colbase + 4 * wquad, 317);
#pragma unroll
        for (int e = 0; e < 8; ++e) wv[e] = ld8(rw, ((ka + 8 * (wk1 + 16 * (8 * woc + e))) * 321 + cs) * 2, 0);
    }
    fence();
    u32x4 f1re, f1im, f2re, f2im;
    float t2c[4], t2s[4];
    {
        float c[8], s[8];
#pragma unroll
        for (int j = 0; j < 8; ++j) {
            const float a = (float)(8 * (g & 1) + j) * (float)i16 * 0.0625f;
            const float co = cosr(a), si = sinr(a);
            c[j] = (g < 2) ? co : si;
            s[j] = (g < 2) ? -si : co;
        }
        f1re = pk8(c); f1im = pk8(s);
#pragma unroll
        for (int j = 0; j < 4; ++j) {
            const float pr = (float)(4 * g + j) * (float)i16;
            const float co = cosr(pr * 0.0625f), si = sinr(pr * 0.0625f);
            c[j] = co; c[4 + j] = si;
            s[j] = -si; s[4 + j] = co;
            t2c[j] = cosr(pr * (1.0f / 256.0f));
            t2s[j] = sinr(pr * (1.0f / 256.0f));
        }
        f2re = pk8(c); f2im = pk8(s);
    }
    const u32x4 ire = {f2re.x, f2re.y, f2re.z ^ 0x80008000u, f2re.w ^ 0x80008000u};
    const u32x4 iim = {f2im.x ^ 0x80008000u, f2im.y ^ 0x80008000u, f2im.z, f2im.w};
    wait_flags<96>(rws, OFF_SYNC + 1024, tag, w, l);
    STAMP();
    {
        const int task = min(t, 447), quad = task % 7, r = task / 7, pl = r >> 5, oc = r & 31;
        const u32 base = OFF_X1 + (8 * oc) * X1_ROW + (ka * 2 + pl) * 648 + (colbase + 4 * quad) * 2;
        u32x2 v[8];
        for (u32 spins = 0; spins < SPIN_MAX; ++spins) {
            fence();
            bool ok = true;
#pragma unroll
            for (int e = 0; e < 8; ++e) v[e] = ld8(rws, base + e * X1_ROW, 1);
#pragma unroll
            for (int e = 0; e < 8; ++e) ok &= fresh2(v[e]);
            if (__all(ok)) break;
            __builtin_amdgcn_s_sleep(1);
        }
        u32x4 o[4];
        tr8x4(v, o);
#pragma unroll
        for (int c = 0; c < 4; ++c) {
            const int m = (4 * quad + c) * 2 + pl;
            *(u32x4*)(smem + L_C + m * 512 + ((oc ^ ((m >> 3) & 7)) * 16)) = o[c];
        }
    }
    {
        if (colbase + 4 * wquad > 317) {
#pragma unroll
            for (int e = 0; e < 8; ++e) wv[e] = u32x2{wv[e].y >> 16, 0u};
        }
        u32x4 o[4];
        tr8x4(wv, o);
#pragma unroll
        for (int c = 0; c < 4; ++c) {
            const int m = (4 * wquad + c) * 2 + wpl;
            *(u32x4*)(smem + L_W + m * 512 + (((wk1 * 2 + woc) ^ ((m >> 3) & 7)) * 16)) = o[c];
        }
    }
    STAMP();
    __syncthreads();
    {
        const int q = i16 >> 2, p = i16 & 3;
#pragma unroll 1
        for (int col = w; col < 28; col += 8) {
            const f32x4 zero = {0.f, 0.f, 0.f, 0.f};
            const int mi = col * 2 + (g >> 1), swi = (mi >> 3) & 7;
            const char* ib = smem + L_C + mi * 512 + (p & 1) * 8;
            const u32x2 a0 = trread(ib + (((2 * (8 * (g & 1) + q) + (p >> 1)) ^ swi) * 16));
            const u32x2 a1 = trread(ib + (((2 * (8 * (g & 1) + 4 + q) + (p >> 1)) ^ swi) * 16));
            const u32x4 a = {a0.x, a0.y, a1.x, a1.y};
            const int m0 = col * 2, sw = (m0 >> 3) & 7;
            const int woff = (((i16 * 2 + (g >> 1)) ^ sw) * 16) + (g & 1) * 8;
            const u32x2 wre = *(const u32x2*)(smem + L_W + m0 * 512 + woff);
            const u32x2 wim = *(const u32x2*)(smem + L_W + (m0 + 1) * 512 + woff);
            f32x4 re = mfma16(a, f1re, zero), im = mfma16(a, f1im, zero);
            float x[8];
#pragma unroll
            for (int i = 0; i < 4; ++i) {
                x[i] = re[i] * t2c[i] + im[i] * t2s[i];
                x[4 + i] = im[i] * t2c[i] - re[i] * t2s[i];
            }
            u32x4 d = pk8(x);
            re = mfma16(f2re, d, zero); im = mfma16(f2im, d, zero);
#pragma unroll
            for (int i = 0; i < 4; ++i) {
                const float a_ = (i & 1) ? bfhi(wre[i >> 1]) : bflo(wre[i >> 1]);
                const float b_ = (i & 1) ? bfhi(wim[i >> 1]) : bflo(wim[i >> 1]);
                x[i] = re[i] * a_ - im[i] * b_;
                x[4 + i] = re[i] * b_ + im[i] * a_;
            }
            d = pk8(x);
            re = mfma16(d, ire, zero); im = mfma16(d, iim, zero);
#pragma unroll
            for (int i = 0; i < 4; ++i) {
                x[i] = re[i] * t2c[i] - im[i] * t2s[i];
                x[4 + i] = im[i] * t2c[i] + re[i] * t2s[i];
            }
            d = pk8(x);
            re = mfma16(d, ire, zero); im = mfma16(d, iim, zero);
            const int ooff = (((2 * i16 + (g >> 1)) ^ sw) * 16) + (g & 1) * 8;
            *(u32x2*)(smem + L_C + m0 * 512 + ooff) = u32x2{pk(re[0], re[1]), pk(re[2], re[3])};
            *(u32x2*)(smem + L_C + (m0 + 1) * 512 + ooff) = u32x2{pk(im[0], im[1]), pk(im[2], im[3])};
        }
    }
    STAMP();
    wait_flags<160>(rws, OFF_SYNC + 2048, tag, w, l);
    if (ai == 0 && t == 0) __hip_atomic_store((GAS u32*)(P.ws + OFF_SYNC), tag, __ATOMIC_RELAXED, __HIP_MEMORY_SCOPE_AGENT);
    {
        const int q = i16 >> 2, p = i16 & 3;
#pragma unroll
        for (int it = 0; it < 7; ++it) {
            const int cmb = (w * 7 + it) * 4 + g;
            const int quad = cmb % 7, r = cmb / 7, pl = r & 1, G = r >> 1;
            const int m = (4 * quad + q) * 2 + pl;
            const u32x2 v = trread(smem + L_C + m * 512 + (((2 * G + (p >> 1)) ^ ((m >> 3) & 7)) * 16) + (p & 1) * 8);
            *(u32x2*)(smem + L_W + (16 * G + i16) * 112 + pl * 56 + quad * 8) = v;
        }
    }
    __syncthreads();
#pragma unroll
    for (int i = 0; i < 4; ++i) {
        const int m = min(t + 512 * i, 1791);
        st16sc(rws, OFF_Y1 + ai * Y1_BLK + m * 16, sane(*(const u32x4*)(smem + L_W + m * 16)));
    }
    STAMP();

    u32x4 C[2], S[2], nsS[2], sC[2], F[2];
    float twc[8], tws[8];
    {
        const u32 sgn = (g >= 2) ? 0x80008000u : 0u;
#pragma unroll
        for (int tau = 0; tau < 2; ++tau) {
            float c[8], s[8];
            const float d1 = (float)(16 * tau + i16);
#pragma unroll
            for (int j = 0; j < 8; ++j) {
                const float a = (float)(8 * g + j) * d1 * 0.03125f;
                c[j] = cosr(a);
                s[j] = sinr(a);
            }
            C[tau] = pk8(c); S[tau] = pk8(s);
            nsS[tau] = S[tau] ^ (sgn ^ 0x80008000u);
            sC[tau] = C[tau] ^ sgn;
#pragma unroll
            for (int i = 0; i < 4; ++i) {
                const float a = d1 * (float)(4 * g + i) * (1.0f / 640.0f);
                twc[4 * tau + i] = cosr(a) * (1.0f / (2048.0f * 640.0f));
                tws[4 * tau + i] = sinr(a) * (1.0f / (2048.0f * 640.0f));
            }
        }
#pragma unroll
        for (int dl = 0; dl < 2; ++dl) {
            float v[8];
#pragma unroll
            for (int j = 0; j < 8; ++j) {
                const int k2 = 4 * g + (j & 3), d2 = 16 * dl + i16;
                const float wgt = (k2 > 10 || d2 >= 20) ? 0.f : ((k2 == 0 || k2 == 10) ? 1.f : 2.f);
                const float a = (float)k2 * (float)d2 * 0.05f;
                v[j] = (j < 4) ? wgt * cosr(a) : -wgt * sinr(a);
            }
            F[dl] = pk8(v);
        }
    }
    {
        if (w == 0) {
            for (u32 spins = 0; spins < SPIN_MAX; ++spins) {
                fence();
                const u32x4 f = ld16(rws, OFF_Y1 + (l + l / 2) * Y1_BLK + ai * 112 + 96, 1);
                if (__all(fresh4(f))) break;
                __builtin_amdgcn_s_sleep(1);
            }
        }
        __syncthreads();
        STAMP();
        u32 off[4];
#pragma unroll
        for (int i = 0; i < 4; ++i) {
            const int m = min(t + 512 * i, nset * 672 - 1), j = m / 672, mm = m - 672 * j, ab = mm / 7;
            off[i] = OFF_Y1 + ab * Y1_BLK + (ai + 96 * j) * 112 + (mm - 7 * ab) * 16;
        }
        u32x4 v[4];
        for (u32 spins = 0; spins < SPIN_MAX; ++spins) {
            fence();
#pragma unroll
            for (int i = 0; i < 4; ++i) v[i] = ld16(rws, off[i], 1);
            if (__all(fresh4(v[0]) && fresh4(v[1]) && fresh4(v[2]) && fresh4(v[3]))) break;
            __builtin_amdgcn_s_sleep(1);
        }
#pragma unroll
        for (int i = 0; i < 4; ++i) *(u32x4*)(smem + L_X + min(t + 512 * i, nset * 672 - 1) * 16) = v[i];
    }
    STAMP();
    __syncthreads();
    {
        const int q = i16 >> 2, p = i16 & 3;
        const int kap = 2 * g + (q >> 1), pl = q & 1;
        const int plo = i16 >> 3, a = i16 & 7;
#pragma unroll 1
        for (int j = 0; j < nset; ++j) {
            const int s0 = ai + 96 * j;
            float f[4];
#pragma unroll
            for (int e = 0; e < 4; ++e) {
                const int kk = 2 * g + (e >> 1), pp = e & 1;
                const float th = (float)kk * (float)(256 * a + s0) * (1.0f / 2048.0f) - ((pp != plo) ? 0.25f : 0.f);
                const float v = cosr(th);
                f[e] = (plo == 0 && pp == 1) ? -v : v;
            }
            const u32x2 fa = {pk(f[0], f[1]), pk(f[2], f[3])};
#pragma unroll
            for (int it = 0; it < 3; ++it) {
                const int T = w + 8 * it;
                if (T < 21) {
                    const int k = min(16 * T + 4 * p, 320);
                    const int cgk = min(k / 28, 11), idx = k - min(28 * cgk, 296);
                    const u32x2 bfr = trread(smem + L_X + j * 10752 + (kap * 12 + cgk) * 112 + pl * 56 + idx * 2);
                    const f32x4 d = mfma16k16(fa, bfr, f32x4{0.f, 0.f, 0.f, 0.f});
                    *(u32x2*)(smem + L_S + j * 10752 + (16 * T + i16) * 32 + g * 8) = u32x2{pk(d[0], d[1]), pk(d[2], d[3])};
                }
            }
        }
    }
    __syncthreads();
#pragma unroll 1
    for (int task = w; task < 4 * nset; task += 8) {
        const int j = task >> 2, wp = task & 3;
        const char* sb = smem + L_S + j * 10752 + wp * 4;
        const int k2 = i16 <= 10 ? i16 : 10;
        u32 dre[8], dim[8];
#pragma unroll
        for (int e = 0; e < 8; ++e) {
            const int k = k2 + 20 * (8 * g + e);
            const int kk = (g < 2) ? k : 640 - k;
            dre[e] = *(const u32*)(sb + kk * 32);
            dim[e] = *(const u32*)(sb + kk * 32 + 16);
        }
#pragma unroll
        for (int rr = 0; rr < 2; ++rr) {
            u32x4 are, aim;
            if (rr == 0) {
                are = u32x4{permlo(dre[0], dre[1]), permlo(dre[2], dre[3]), permlo(dre[4], dre[5]), permlo(dre[6], dre[7])};
                aim = u32x4{permlo(dim[0], dim[1]), permlo(dim[2], dim[3]), permlo(dim[4], dim[5]), permlo(dim[6], dim[7])};
            } else {
                are = u32x4{permhi(dre[0], dre[1]), permhi(dre[2], dre[3]), permhi(dre[4], dre[5]), permhi(dre[6], dre[7])};
                aim = u32x4{permhi(dim[0], dim[1]), permhi(dim[2], dim[3]), permhi(dim[4], dim[5]), permhi(dim[6], dim[7])};
            }
            u32x4 va[2];
            const f32x4 zero = {0.f, 0.f, 0.f, 0.f};
#pragma unroll
            for (int tau = 0; tau < 2; ++tau) {
                const f32x4 vre = mfma16(aim, nsS[tau], mfma16(are, C[tau], zero));
                const f32x4 vim = mfma16(aim, sC[tau], mfma16(are, S[tau], zero));
                float pr[8];
#pragma unroll
                for (int i = 0; i < 4; ++i) {
                    pr[i] = vre[i] * twc[4 * tau + i] - vim[i] * tws[4 * tau + i];
                    pr[4 + i] = vim[i] * twc[4 * tau + i] + vre[i] * tws[4 * tau + i];
                }
                va[tau] = pk8(pr);
            }
            const u32 orow = (ai + 96 * j + 256 * (2 * wp + rr)) * 2560;
#pragma unroll
            for (int dl = 0; dl < 2; ++dl) {
                const f32x4 y0 = mfma16(va[0], F[dl], zero);
                const f32x4 y1 = mfma16(va[1], F[dl], zero);
                u32 a0 = pk(y0[0], y0[1]), a1 = pk(y0[2], y0[3]);
                u32 b0 = pk(y1[0], y1[1]), b1 = pk(y1[2], y1[3]);
                auto s0 = __builtin_amdgcn_permlane16_swap(a0, b0, false, false);
                auto s1 = __builtin_amdgcn_permlane16_swap(a1, b1, false, false);
                const int d2 = 16 * dl + i16;
                if (d2 < 20) __builtin_amdgcn_raw_buffer_store_b128(u32x4{s0[0], s1[0], s0[1], s1[1]}, mkrs(P.out, 2048 * 2560), orow + (32 * d2 + (g & 1) * 16 + (g >> 1) * 8) * 2, 0, OUTA);
            }
        }
    }
    STAMP();
    STAMP_OUT(P.ws);
}

DI void role_b(const Args& P, char* smem, const int t, const int w, const int xcd, const int jx, const u32 tag) {
    const int l = t & 63, g = l >> 4, i16 = l & 15;
    const int chb = 20 * xcd + jx;
    const rsrc_t rws = mkrs(P.ws, WS_USED);
    STAMP_DECL STAMP();
    if (t == 0) *(u32*)(smem + L_CNT) = 0u;
    __syncthreads();
    u32x2 gv[8];
    u32 pw0 = 0, pw1 = 0;
    if (w >= 4) {
        const int tt = t - 256;
        const rsrc_t ru = mkrs(P.u, 2048 * 1280);
        {
            const int b0 = (160 * xcd) & ~127, nl = (160 * xcd + 160 - b0 + 127) >> 7;
            const int m0 = tt, m1 = min(tt + 256, 103 * 3 - 1);
            pw0 = __builtin_amdgcn_raw_buffer_load_b32(ru, min(103 * jx + m0 / 3, 2047) * 1280 + b0 + min(m0 % 3, nl - 1) * 128, 0, 0);
            pw1 = __builtin_amdgcn_raw_buffer_load_b32(ru, min(103 * jx + m1 / 3, 2047) * 1280 + b0 + min(m1 % 3, nl - 1) * 128, 0, 0);
        }
#pragma unroll
        for (int e = 0; e < 8; ++e) gv[e] = ld8(ru, (8 * tt + e) * 1280 + chb * 8, 0);
        fence();
        if (tt < 128) *(u32x4*)(smem + L_U + (tt >> 5) * PB_U + (tt & 31) * 16) = u32x4{0u, 0u, 0u, 0u};
        else if (tt < 144) {
            const int v = tt - 128;
            *(u32x4*)(smem + L_K + (v >> 2) * PB_KS + ((v >> 1) & 1) * PB_K1 + 4096 + (v & 1) * 16) = u32x4{0u, 0u, 0u, 0u};
        }
        u32x4 o[4];
        tr8x4(gv, o);
#pragma unroll
        for (int c = 0; c < 4; ++c) *(u32x4*)(smem + L_U + c * PB_U + 512 + tt * 16) = o[c];
        asm volatile("" :: "v"(pw0), "v"(pw1));
        STAMP();
    } else {
        const int ch = 4 * chb + w;
        u32 pv[12];
        {
            const u32 nb = 640 * 128 * 2;
            const rsrc_t r0 = mkrs(P.Are, nb), r1 = mkrs(P.Aim, nb), r2 = mkrs(P.Bre, nb), r3 = mkrs(P.Bim, nb), r4 = mkrs(P.Cre, nb), r5 = mkrs(P.Cim, nb);
#pragma unroll
            for (int h = 0; h < 2; ++h) {
                const u32 off = (ch * 128 + 64 * h + l) * 2;
                pv[6 * h + 0] = ld2(r0, off); pv[6 * h + 1] = ld2(r1, off); pv[6 * h + 2] = ld2(r2, off);
                pv[6 * h + 3] = ld2(r3, off); pv[6 * h + 4] = ld2(r4, off); pv[6 * h + 5] = ld2(r5, off);
            }
        }
        const u32 ldt = ld2(mkrs(P.logdt, 1280), ch * 2);
        {
            const u32x4 s4 = {SENT, SENT, SENT, SENT};
#pragma unroll
            for (int i = 0; i < 5; ++i) st16sc(rws, OFF_Y1 + min(chb * 1076 + min(t + 256 * i, 1075), 172031) * 16, s4);
        }
        fence();
        char* sc = smem + L_G + w * KG_SZ;
        const float dt = rbf(expf(bf2f(ldt)));
        f32x4 kacc[3];
#pragma unroll
        for (int T = 0; T < 3; ++T) kacc[T] = f32x4{0.f, 0.f, 0.f, 0.f};
#pragma unroll 1
        for (int h = 0; h < 2; ++h) {
            const float are = bf2f(h ? pv[6] : pv[0]), aim = bf2f(h ? pv[7] : pv[1]), bre = bf2f(h ? pv[8] : pv[2]);
            const float bim = bf2f(h ? pv[9] : pv[3]), cre = bf2f(h ? pv[10] : pv[4]), cim = bf2f(h ? pv[11] : pv[5]);
            const float lre = dt * are, lim = dt * aim * 0.15915494309189535f;
            const float dbr = dt * bre, dbi = dt * bim;
            const cf c = {cre * dbr - cim * dbi, cre * dbi + cim * dbr};
            char* rowp = sc + l * 4;
            const cf z1 = cexpk(lre, lim, 1.f), z8 = cexpk(lre, lim, 8.f), z128 = cexpk(lre, lim, 128.f);
            const cf z2 = cmul(z1, z1), z4 = cmul(z2, z2), z6 = cmul(z2, z4);
            cf v = {1.f, 0.f}, r0 = c, r1 = cmul(c, z128);
            cf q = cmul(r1, z128);
#pragma unroll
            for (int j = 0; j < 8; ++j) {
                *(u32*)(rowp + KG_B + (2 * j) * KG_RS) = pk(v.re, v.im);
                v = cmul(v, z8);
                *(u32*)(rowp + KG_B + (2 * j + 1) * KG_RS) = pk(v.re, v.im);
                v = cmul(v, z8);
                *(u32*)(rowp + j * KG_RS) = pk(r0.re, -r0.im);
                *(u32*)(rowp + (8 + j) * KG_RS) = pk(r1.re, -r1.im);
                r0 = cmul(r0, z1);
                r1 = cmul(r1, z1);
#pragma unroll
                for (int e = 0; e < 2; ++e) {
                    const int hh = 2 + 2 * j + e;
                    if (hh < 4) {
                        const int r = 16 + (hh - 2) * 4;
                        const cf a = cmul(q, z2), bq = cmul(q, z4), cq = cmul(q, z6);
                        *(u32*)(rowp + r * KG_RS) = pk(q.re, -q.im);
                        *(u32*)(rowp + (r + 1) * KG_RS) = pk(a.re, -a.im);
                        *(u32*)(rowp + (r + 2) * KG_RS) = pk(bq.re, -bq.im);
                        *(u32*)(rowp + (r + 3) * KG_RS) = pk(cq.re, -cq.im);
                    } else if (hh < 8) {
                        const int r = 24 + (hh - 4) * 2;
                        const cf bq = cmul(q, z4);
                        *(u32*)(rowp + r * KG_RS) = pk(q.re, -q.im);
                        *(u32*)(rowp + (r + 1) * KG_RS) = pk(bq.re, -bq.im);
                    } else if (hh <= 16) {
                        *(u32*)(rowp + (32 + hh - 8) * KG_RS) = pk(q.re, -q.im);
                    }
                    if (hh < 16) q = cmul(q, z128);
                }
            }
            const char* bp = sc + KG_B + i16 * KG_RS + g * 16;
#pragma unroll
            for (int ks = 0; ks < 4; ++ks) {
                const u32x4 bf = *(const u32x4*)(bp + ks * 64);
#pragma unroll
                for (int T = 0; T < 3; ++T)
                    kacc[T] = mfma16(*(const u32x4*)(sc + (16 * T + i16) * KG_RS + g * 16 + ks * 64), bf, kacc[T]);
            }
        }
#pragma unroll
        for (int T = 0; T < 3; ++T)
#pragma unroll
            for (int i = 0; i < 4; ++i) {
                const int row = 16 * T + 4 * g + i;
                int e;
                if (T == 0) e = 128 * (row >> 3) + (row & 7);
                else if (T == 1) e = (row < 24) ? 256 + 128 * ((row - 16) >> 2) + 2 * ((row - 16) & 3) : 512 + 128 * ((row - 24) >> 1) + 4 * ((row - 24) & 1);
                else e = 1024 + 128 * (row - 32);
                if (row < 40 || (row == 40 && i16 == 0))
                    *(u16*)(sc + KG_T + (e + 8 * i16) * 2) = (u16)(pk(kacc[T][i], kacc[T][i]) & 0xffffu);
            }
        drain();
        if (l == 0) {
            const u32 old = __hip_atomic_fetch_add((LAS u32*)(smem + L_CNT), 1u, __ATOMIC_RELAXED, __HIP_MEMORY_SCOPE_WORKGROUP);
            if (old == 3u) __hip_atomic_store((GAS u32*)(P.ws + OFF_SYNC + 2048) + chb, tag, __ATOMIC_RELAXED, __HIP_MEMORY_SCOPE_AGENT);
        }
#pragma unroll 1
        for (int it = 0; it < 4; ++it) {
            const int tt = l + 64 * it, a = 255 - tt;
            u32 code;
            if (a < 32) code = 0x76543210u;
            else if (a < 64) code = 0x86444200u;
            else if (a < 128) code = 0x88444000u;
            else code = (a & 1) ? 0x88880000u : 0x88800000u;
            const int lastoff = (a <= 32) ? 7 : 8;
            const char* tb = sc + KG_T + 16 * a;
            u32 v[8];
#pragma unroll
            for (int e = 0; e < 8; ++e) v[e] = *(const u16*)(tb + 2 * ((code >> (4 * e)) & 15));
            u32 vm = *(const u16*)(tb - 16 + 2 * lastoff);
            vm = (a == 0) ? 0u : vm;
            char* kb = smem + L_K + w * PB_KS + tt * 16;
            *(u32x4*)(kb) = u32x4{v[7] | (v[6] << 16), v[5] | (v[4] << 16), v[3] | (v[2] << 16), v[1] | (v[0] << 16)};
            *(u32x4*)(kb + PB_K1) = u32x4{v[6] | (v[5] << 16), v[4] | (v[3] << 16), v[2] | (v[1] << 16), v[0] | (vm << 16)};
        }
        STAMP();
    }
    const int ch = w & 3, rh = w >> 2;
    const float dsk = bf2f(ld2(mkrs(P.dskip, 1280), (4 * chb + ch) * 2));
    __syncthreads();
    STAMP();
    const int mu = g >> 1, hp = g & 1;
    const int x0 = 2047 - 16 * mu - i16 + 8 * hp;
    const char* ap = smem + L_K + ch * PB_KS + ((x0 & 1) ? PB_K1 + (x0 - 1) * 2 : x0 * 2);
    const char* bp = smem + L_U + ch * PB_U + (256 + 16 * (i16 - mu) + 8 * hp) * 2;
    f32x4 acc[8];
#pragma unroll
    for (int c = 0; c < 8; ++c) acc[c] = f32x4{0.f, 0.f, 0.f, 0.f};
#pragma unroll 1
    for (int r = 4 * rh; r < 4 * rh + 4; ++r) {
        u32x4 A[8], B[8];
#pragma unroll
        for (int p = 0; p < 8; ++p) {
            const u32* a = (const u32*)(ap - 64 * r - 512 * p);
            A[p] = u32x4{a[0], a[1], a[2], a[3]};
            B[p] = *(const u32x4*)(bp - 64 * r + 512 * p);
        }
#pragma unroll
        for (int p = 0; p < 8; ++p)
#pragma unroll
            for (int q = 0; q + p < 8; ++q) acc[p + q] = mfma16(A[p], B[q], acc[p + q]);
    }
    STAMP();
    char* rb = smem + L_G;
#pragma unroll
    for (int c = 0; c < 4; ++c) *(f32x4*)(rb + (w * 4 + c) * 1024 + l * 16) = rh ? acc[c] : acc[4 + c];
    __syncthreads();
    char* st = smem + L_G + 32768;
#pragma unroll
    for (int c = 0; c < 4; ++c) {
        const f32x4 o = *(const f32x4*)(rb + ((w ^ 4) * 4 + c) * 1024 + l * 16);
        const f32x4 mine = rh ? acc[4 + c] : acc[c];
        const int tt = 256 * (4 * rh + c) + 16 * i16 + 4 * g;
        const u32x2 uu = *(const u32x2*)(smem + L_U + ch * PB_U + (256 + tt) * 2);
#pragma unroll
        for (int i = 0; i < 4; ++i) {
            const float uf = (i & 1) ? bfhi(i < 2 ? uu.x : uu.y) : bflo(i < 2 ? uu.x : uu.y);
            const float v = rbf(mine[i] + o[i]) + rbf(uf * dsk);
            *(u16*)(st + (tt >> 4) * 128 + (((4 * g + i) ^ i16) * 8) + ch * 2) = (u16)(pk(v, v) & 0xffffu);
        }
    }
    __syncthreads();
    STAMP();
    {
        const rsrc_t ro = mkrs(P.out, 2048 * 2560);
#pragma unroll
        for (int i = 0; i < 4; ++i) {
            const int row = t + 512 * i;
            __builtin_amdgcn_raw_buffer_store_b64(*(const u32x2*)(st + (row >> 4) * 128 + (((row & 15) ^ ((row >> 4) & 15)) * 8)), ro,
                                                  row * 2560 + 1280 + chb * 8, 0, OUTB);
        }
    }
    STAMP();
    STAMP_OUT(P.ws);
}

__global__ void __launch_bounds__(512, 2) fused_kernel(Args P) {
    extern __shared__ __attribute__((aligned(16))) char smem[];
    const int t = threadIdx.x;
    const int w = __builtin_amdgcn_readfirstlane(t >> 6);
    const int b = blockIdx.x, xcd = b & 7, jx = b >> 3;
    const u32 tag = __hip_atomic_load((GAS u32*)(P.ws + OFF_SYNC), __ATOMIC_RELAXED, __HIP_MEMORY_SCOPE_AGENT) + 1u;
    if (jx < 20) role_b(P, smem, t, w, xcd, jx, tag);
    else role_a(P, smem, t, w, xcd, jx - 20, tag);
}

extern "C" void kernel_launch(void* const* d_in, const int* in_sizes, int n_in,
                              void* d_out, int out_size, void* d_ws, size_t ws_size,
                              hipStream_t stream) {
    Args a;
    a.x = (const u16*)d_in[0];
    a.wr = (const u16*)d_in[1];
    a.wi = (const u16*)d_in[2];
    a.u = (const u16*)d_in[3];
    a.Are = (const u16*)d_in[4];
    a.Aim = (const u16*)d_in[5];
    a.Bre = (const u16*)d_in[6];
    a.Bim = (const u16*)d_in[7];
    a.Cre = (const u16*)d_in[8];
    a.Cim = (const u16*)d_in[9];
    a.logdt = (const u16*)d_in[10];
    a.dskip = (const u16*)d_in[11];
    a.ws = (char*)d_ws;
    a.out = (u16*)d_out;
    (void)hipFuncSetAttribute((const void*)fused_kernel, hipFuncAttributeMaxDynamicSharedMemorySize, LDS_BYTES);
    fused_kernel<<<256, 512, LDS_BYTES, stream>>>(a);
}
```

```cpp
#include <hip/hip_runtime.h>
#include <hip/hip_bf16.h>

typedef unsigned short u16;
typedef unsigned int u32;
typedef __attribute__((ext_vector_type(2))) __bf16 bf16x2;
typedef __attribute__((ext_vector_type(8))) __bf16 bf16x8;
typedef __attribute__((ext_vector_type(2))) float f32x2;
typedef __attribute__((ext_vector_type(4))) float f32x4;
typedef __attribute__((ext_vector_type(4))) u32 u32x4;
typedef __attribute__((ext_vector_type(2))) u32 u32x2;
typedef __attribute__((ext_vector_type(4))) short s16x4;
typedef __amdgpu_buffer_rsrc_t rsrc_t;
#define LAS __attribute__((address_space(3)))
#define GAS __attribute__((address_space(1)))
#define DI __device__ __forceinline__
#define SC1 16

constexpr u32 X1_ROW = 10368;
constexpr u32 OFF_X1 = 0;
constexpr u32 Y1_BLK = 28672;
constexpr u32 OFF_Y1 = 256 * X1_ROW;
constexpr u32 OFF_SYNC = OFF_Y1 + 96 * Y1_BLK;
constexpr u32 WS_USED = OFF_SYNC + 4096;
constexpr u32 SENT = 0xFFFFFFFFu;
constexpr u32 SPIN_MAX = 1u << 16;

constexpr int L_X = 0;
constexpr int L_S = 32256;
constexpr int L_W = 64512;
constexpr int L_C = L_W + 28672;
constexpr int PB_U = 4608;
constexpr int L_U = 0;
constexpr int L_K = L_U + 4 * PB_U;
constexpr int PB_K1 = 4160, PB_KS = 8320;
constexpr int L_G = L_K + 4 * PB_KS;
constexpr int KG_RS = 272, KG_B = 41 * KG_RS, KG_T = 57 * KG_RS, KG_SZ = 19616;
constexpr int L_CNT = L_G + 4 * KG_SZ;
constexpr int LDS_BYTES = 163840;

#define STAMP_DECL
#define STAMP()
#define STAMP_OUT(ws)

DI u32 pk(float a, float b) {
    f32x2 v = {a, b};
    return __builtin_bit_cast(u32, __builtin_convertvector(v, bf16x2));
}
DI u32x4 pk8(const float* v) { return u32x4{pk(v[0], v[1]), pk(v[2], v[3]), pk(v[4], v[5]), pk(v[6], v[7])}; }
DI float bflo(u32 v) { return __uint_as_float(v << 16); }
DI float bfhi(u32 v) { return __uint_as_float(v & 0xffff0000u); }
DI float bf2f(u32 v) { return __uint_as_float(v << 16); }
DI float rbf(float a) { return bflo(pk(a, a)); }
DI float cosr(float rev) { return __builtin_amdgcn_cosf(rev); }
DI float sinr(float rev) { return __builtin_amdgcn_sinf(rev); }
DI f32x4 mfma16(u32x4 a, u32x4 b, f32x4 c) {
    return __builtin_amdgcn_mfma_f32_16x16x32_bf16(__builtin_bit_cast(bf16x8, a), __builtin_bit_cast(bf16x8, b), c, 0, 0, 0);
}
DI f32x4 mfma16k16(u32x2 a, u32x2 b, f32x4 c) {
    return __builtin_amdgcn_mfma_f32_16x16x16bf16_1k(__builtin_bit_cast(s16x4, a), __builtin_bit_cast(s16x4, b), c, 0, 0, 0);
}
DI u32x2 trread(const char* p) {
    return __builtin_bit_cast(u32x2, __builtin_amdgcn_ds_read_tr16_b64_v4i16((LAS s16x4*)(p)));
}
DI u32 permlo(u32 a, u32 b) { return __builtin_amdgcn_perm(b, a, 0x05040100u); }
DI u32 permhi(u32 a, u32 b) { return __builtin_amdgcn_perm(b, a, 0x07060302u); }
DI rsrc_t mkrs(const void* p, u32 bytes) { return __builtin_amdgcn_make_buffer_rsrc((void*)p, 0, bytes, 0x00020000); }
DI u32x4 ld16(rsrc_t r, u32 off, int aux) { return aux ? __builtin_amdgcn_raw_buffer_load_b128(r, off, 0, SC1) : __builtin_amdgcn_raw_buffer_load_b128(r, off, 0, 0); }
DI u32x2 ld8(rsrc_t r, u32 off, int aux) { return aux ? __builtin_amdgcn_raw_buffer_load_b64(r, off, 0, SC1) : __builtin_amdgcn_raw_buffer_load_b64(r, off, 0, 0); }
DI u32 ld2(rsrc_t r, u32 off) { return __builtin_amdgcn_raw_buffer_load_b16(r, off, 0, 0); }
DI void st16sc(rsrc_t r, u32 off, u32x4 v) { __builtin_amdgcn_raw_buffer_store_b128(v, r, off, 0, SC1); }
DI u32x4 sane(u32x4 v) { return u32x4{min(v.x, SENT - 1), min(v.y, SENT - 1), min(v.z, SENT - 1), min(v.w, SENT - 1)}; }
DI bool fresh4(u32x4 v) { return max(max(v.x, v.y), max(v.z, v.w)) != SENT; }
DI bool fresh2(u32x2 v) { return max(v.x, v.y) != SENT; }
DI void fence() { asm volatile("" ::: "memory"); }
DI void drain() { asm volatile("s_waitcnt vmcnt(0)" ::: "memory"); }
DI void tr8x4(const u32x2* v, u32x4* o) {
    o[0] = u32x4{permlo(v[0].x, v[1].x), permlo(v[2].x, v[3].x), permlo(v[4].x, v[5].x), permlo(v[6].x, v[7].x)};
    o[1] = u32x4{permhi(v[0].x, v[1].x), permhi(v[2].x, v[3].x), permhi(v[4].x, v[5].x), permhi(v[6].x, v[7].x)};
    o[2] = u32x4{permlo(v[0].y, v[1].y), permlo(v[2].y, v[3].y), permlo(v[4].y, v[5].y), permlo(v[6].y, v[7].y)};
    o[3] = u32x4{permhi(v[0].y, v[1].y), permhi(v[2].y, v[3].y), permhi(v[4].y, v[5].y), permhi(v[6].y, v[7].y)};
}
template <int NF>
DI void wait_flags(rsrc_t rws, u32 off, u32 tag, int w, int l) {
    if (w == 0) {
        const int q = min(l, NF / 4 - 1);
        for (u32 spins = 0; spins < SPIN_MAX; ++spins) {
            fence();
            const u32x4 f = ld16(rws, off + q * 16, 1);
            if (__all(f.x == tag && f.y == tag && f.z == tag && f.w == tag)) break;
            __builtin_amdgcn_s_sleep(2);
        }
    }
    __syncthreads();
}

struct cf { float re, im; };
DI cf cmul(cf a, cf b) { return cf{a.re * b.re - a.im * b.im, a.re * b.im + a.im * b.re}; }
DI cf cexpk(float lre, float lim_rev, float k) {
    const float m = __builtin_amdgcn_exp2f(k * lre * 1.44269504088896341f);
    const float a = __builtin_amdgcn_fractf(k * lim_rev);
    return cf{m * cosr(a), m * sinr(a)};
}

struct Args {
    const u16 *x, *wr, *wi, *u, *Are, *Aim, *Bre, *Bim, *Cre, *Cim, *logdt, *dskip;
    char* ws;
    u16* out;
};

DI void role_a(const Args& P, char* smem, const int t, const int w, const int ka, const int cg, const u32 tag) {
    const int l = t & 63, g = l >> 4, i16 = l & 15;
    const int ai = ka * 12 + cg, nset = ai < 64 ? 3 : 2;
    const int colbase = min(28 * cg, 296);
    const rsrc_t rws = mkrs(P.ws, WS_USED);
    STAMP_DECL STAMP();
    {
        const u32x4 s4 = {SENT, SENT, SENT, SENT};
#pragma unroll
        for (int i = 0; i < 4; ++i) {
            const int m = min(t + 512 * i, nset * 648 - 1), j = m / 648;
            st16sc(rws, OFF_X1 + (ai + 96 * j) * X1_ROW + (m - 648 * j) * 16, s4);
        }
    }
    u32x4 xv[2][3];
    {
        const rsrc_t rx = mkrs(P.x, 2048 * 1280);
#pragma unroll
        for (int it = 0; it < 2; ++it) {
            const int task = min(w + 8 * it, 4 * nset - 1), s0 = ai + 96 * (task >> 2);
#pragma unroll
            for (int i = 0; i < 3; ++i) {
                const int ci = min(l + 64 * i, 159), row = ci >= 80;
                xv[it][i] = ld16(rx, (s0 + 256 * (2 * (task & 3) + row)) * 1280 + (ci - 80 * row) * 16, 0);
            }
        }
    }
    fence();
    if (t < 90) *(u32x4*)(smem + L_S + (t / 30) * 10752 + 321 * 32 + (t % 30) * 16) = u32x4{0u, 0u, 0u, 0u};

    {
        u32x4 f20c, f20s, c32, s32;
        float twc[8], tws[8];
        {
            float c[8], s[8];
#pragma unroll
            for (int j = 0; j < 8; ++j) {
                const int d2 = (j < 4) ? 4 * g + j : 16 + 4 * g + j - 4;
                const bool ok = (d2 < 20) && (i16 <= 10);
                const float a = (float)d2 * (float)i16 * 0.05f;
                c[j] = ok ? cosr(a) : 0.f;
                s[j] = ok ? -sinr(a) : 0.f;
            }
            f20c = pk8(c); f20s = pk8(s);
#pragma unroll
            for (int j = 0; j < 8; ++j) {
                const int d1 = (j < 4) ? 4 * g + j : 16 + 4 * g + j - 4;
                const float pr = (float)d1 * (float)i16;
                c[j] = cosr(pr * 0.03125f);
                s[j] = sinr(pr * 0.03125f);
                twc[j] = cosr(pr * (1.0f / 640.0f));
                tws[j] = sinr(pr * (1.0f / 640.0f));
            }
            c32 = pk8(c); s32 = pk8(s);
        }
        const u32x4 ns32 = s32 ^ 0x80008000u, c32b = c32 ^ 0x80000000u, s32b = s32 ^ 0x80000000u, nc32b = c32b ^ 0x80008000u;
        const int q = i16 >> 2, p = i16 & 3;
        const int d2a = 4 * g + q, d2b = (g == 0) ? 16 + q : d2a;
        const int offa = 64 * d2a + 8 * p, swa = ((d2a >> 2) & 1) << 5;
        const int offb = 64 * d2b + 8 * p, swb = ((d2b >> 2) & 1) << 5;
#pragma unroll
        for (int it = 0; it < 2; ++it) {
            const int task = w + 8 * it;
            if (task < 4 * nset) {
                char* xs = smem + L_X + task * 2560;
#pragma unroll
                for (int i = 0; i < 3; ++i) {
                    const int ci = min(l + 64 * i, 159), c = ci >= 80 ? ci - 80 : ci;
                    *(u32x4*)(xs + ((ci * 16) ^ (((c >> 4) & 1) << 5))) = xv[it][i];
                }
                f32x4 z[2][4];
#pragma unroll
                for (int rr = 0; rr < 2; ++rr) {
                    const char* xr = xs + rr * 1280;
                    f32x4 tre[2], tim[2];
                    const f32x4 zero = {0.f, 0.f, 0.f, 0.f};
#pragma unroll
                    for (int tau = 0; tau < 2; ++tau) {
                        const u32x2 a0 = trread(xr + offa + ((32 * tau) ^ swa));
                        const u32x2 a1 = trread(xr + offb + ((32 * tau) ^ swb));
                        const u32x4 a = {a0.x, a0.y, a1.x, a1.y};
                        tre[tau] = mfma16(a, f20c, zero);
                        tim[tau] = mfma16(a, f20s, zero);
                    }
                    float pr[8], pi[8];
#pragma unroll
                    for (int j = 0; j < 8; ++j) {
                        const float a = tre[j >> 2][j & 3], bb = tim[j >> 2][j & 3];
                        pr[j] = a * twc[j] + bb * tws[j];
                        pi[j] = bb * twc[j] - a * tws[j];
                    }
                    const u32x4 bre = pk8(pr), bim = pk8(pi);
                    z[rr][0] = mfma16(s32, bim, mfma16(c32, bre, zero));
                    z[rr][1] = mfma16(ns32, bre, mfma16(c32, bim, zero));
                    z[rr][2] = mfma16(s32b, bim, mfma16(c32b, bre, zero));
                    z[rr][3] = mfma16(s32b, bre, mfma16(nc32b, bim, zero));
                }
                char* st = smem + L_S + (task >> 2) * 10752 + (task & 3) * 4;
                if (i16 <= 10) {
#pragma unroll
                    for (int i = 0; i < 4; ++i) {
                        const int k = i16 + 20 * (4 * g + i);
                        *(u32*)(st + k * 32) = pk(z[0][0][i], z[1][0][i]);
                        *(u32*)(st + k * 32 + 16) = pk(z[0][1][i], z[1][1][i]);
                    }
#pragma unroll
                    for (int i = 0; i < 4; ++i) {
                        const int k = 320 - i16 - 20 * (4 * g + i);
                        const bool ok = (i16 >= 1 && i16 <= 9) || (i16 == 0 && g == 0 && i == 0);
                        if (ok) {
                            *(u32*)(st + k * 32) = pk(z[0][2][i], z[1][2][i]);
                            *(u32*)(st + k * 32 + 16) = pk(z[0][3][i], z[1][3][i]);
                        }
                    }
                }
            }
        }
        drain();
    }
    STAMP();
    __syncthreads();
    if (t == 0) __hip_atomic_store((GAS u32*)(P.ws + OFF_SYNC + 1024) + ai, tag, __ATOMIC_RELAXED, __HIP_MEMORY_SCOPE_AGENT);
    {
        const int kap = i16 >> 1, plo = i16 & 1, pl = g >> 1;
#pragma unroll 1
        for (int j = 0; j < nset; ++j) {
            const int s0 = ai + 96 * j;
            float f[4];
#pragma unroll
            for (int e = 0; e < 4; ++e) {
                const int a = 4 * (g & 1) + e;
                const float th = (float)kap * (float)(256 * a + s0) * (1.0f / 2048.0f) - ((pl != plo) ? 0.25f : 0.f);
                const float v = cosr(th);
                f[e] = (plo == 1 && pl == 0) ? -v : v;
            }
            const u32x2 fb = {pk(f[0], f[1]), pk(f[2], f[3])};
#pragma unroll
            for (int it = 0; it < 3; ++it) {
                const int T = w + 8 * it;
                if (T < 21) {
                    const u32x2 a = *(const u32x2*)(smem + L_S + j * 10752 + (16 * T + i16) * 32 + g * 8);
                    const f32x4 d = mfma16k16(a, fb, f32x4{0.f, 0.f, 0.f, 0.f});
                    if (T < 20 || g == 0)
                        *(u32x2*)(smem + L_X + j * 10368 + i16 * 648 + (16 * T + 4 * g) * 2) = u32x2{pk(d[0], d[1]), pk(d[2], d[3])};
                }
            }
        }
    }
    __syncthreads();
#pragma unroll
    for (int i = 0; i < 4; ++i) {
        const int m = min(t + 512 * i, nset * 648 - 1), j = m / 648;
        st16sc(rws, OFF_X1 + (ai + 96 * j) * X1_ROW + (m - 648 * j) * 16, sane(*(const u32x4*)(smem + L_X + m * 16)));
    }
    STAMP();
    u32x2 wv[8];
    const int wpl = w >> 2, wtask = min(t & 255, 223), wquad = wtask % 7, wr_ = wtask / 7, wk1 = wr_ & 15, woc = wr_ >> 4;
    {
        const rsrc_t rw = mkrs(wpl ? P.wi : P.wr, 2048 * 642);
        const int cs = min(colbase + 4 * wquad, 317);
#pragma unroll
        for (int e = 0; e < 8; ++e) wv[e] = ld8(rw, ((ka + 8 * (wk1 + 16 * (8 * woc + e))) * 321 + cs) * 2, 0);
    }
    fence();
    u32x4 f1re, f1im, f2re, f2im;
    float t2c[4], t2s[4];
    {
        float c[8], s[8];
#pragma unroll
        for (int j = 0; j < 8; ++j) {
            const float a = (float)(8 * (g & 1) + j) * (float)i16 * 0.0625f;
            const float co = cosr(a), si = sinr(a);
            c[j] = (g < 2) ? co : si;
            s[j] = (g < 2) ? -si : co;
        }
        f1re = pk8(c); f1im = pk8(s);
#pragma unroll
        for (int j = 0; j < 4; ++j) {
            const float pr = (float)(4 * g + j) * (float)i16;
            const float co = cosr(pr * 0.0625f), si = sinr(pr * 0.0625f);
            c[j] = co; c[4 + j] = si;
            s[j] = -si; s[4 + j] = co;
            t2c[j] = cosr(pr * (1.0f / 256.0f));
            t2s[j] = sinr(pr * (1.0f / 256.0f));
        }
        f2re = pk8(c); f2im = pk8(s);
    }
    const u32x4 ire = {f2re.x, f2re.y, f2re.z ^ 0x80008000u, f2re.w ^ 0x80008000u};
    const u32x4 iim = {f2im.x ^ 0x80008000u, f2im.y ^ 0x80008000u, f2im.z, f2im.w};
    wait_flags<96>(rws, OFF_SYNC + 1024, tag, w, l);
    STAMP();
    {
        const int task = min(t, 447), quad = task % 7, r = task / 7, pl = r >> 5, oc = r & 31;
        const u32 base = OFF_X1 + (8 * oc) * X1_ROW + (ka * 2 + pl) * 648 + (colbase + 4 * quad) * 2;
        u32x2 v[8];
        for (u32 spins = 0; spins < SPIN_MAX; ++spins) {
            fence();
            bool ok = true;
#pragma unroll
            for (int e = 0; e < 8; ++e) v[e] = ld8(rws, base + e * X1_ROW, 1);
#pragma unroll
            for (int e = 0; e < 8; ++e) ok &= fresh2(v[e]);
            if (__all(ok)) break;
            __builtin_amdgcn_s_sleep(1);
        }
        u32x4 o[4];
        tr8x4(v, o);
#pragma unroll
        for (int c = 0; c < 4; ++c) {
            const int m = (4 * quad + c) * 2 + pl;
            *(u32x4*)(smem + L_C + m * 512 + ((oc ^ ((m >> 3) & 7)) * 16)) = o[c];
        }
    }
    {
        if (colbase + 4 * wquad > 317) {
#pragma unroll
            for (int e = 0; e < 8; ++e) wv[e] = u32x2{wv[e].y >> 16, 0u};
        }
        u32x4 o[4];
        tr8x4(wv, o);
#pragma unroll
        for (int c = 0; c < 4; ++c) {
            const int m = (4 * wquad + c) * 2 + wpl;
            *(u32x4*)(smem + L_W + m * 512 + (((wk1 * 2 + woc) ^ ((m >> 3) & 7)) * 16)) = o[c];
        }
    }
    STAMP();
    __syncthreads();
    {
        const int q = i16 >> 2, p = i16 & 3;
#pragma unroll 1
        for (int col = w; col < 28; col += 8) {
            const f32x4 zero = {0.f, 0.f, 0.f, 0.f};
            const int mi = col * 2 + (g >> 1), swi = (mi >> 3) & 7;
            const char* ib = smem + L_C + mi * 512 + (p & 1) * 8;
            const u32x2 a0 = trread(ib + (((2 * (8 * (g & 1) + q) + (p >> 1)) ^ swi) * 16));
            const u32x2 a1 = trread(ib + (((2 * (8 * (g & 1) + 4 + q) + (p >> 1)) ^ swi) * 16));
            const u32x4 a = {a0.x, a0.y, a1.x, a1.y};
            const int m0 = col * 2, sw = (m0 >> 3) & 7;
            const int woff = (((i16 * 2 + (g >> 1)) ^ sw) * 16) + (g & 1) * 8;
            const u32x2 wre = *(const u32x2*)(smem + L_W + m0 * 512 + woff);
            const u32x2 wim = *(const u32x2*)(smem + L_W + (m0 + 1) * 512 + woff);
            f32x4 re = mfma16(a, f1re, zero), im = mfma16(a, f1im, zero);
            float x[8];
#pragma unroll
            for (int i = 0; i < 4; ++i) {
                x[i] = re[i] * t2c[i] + im[i] * t2s[i];
                x[4 + i] = im[i] * t2c[i] - re[i] * t2s[i];
            }
            u32x4 d = pk8(x);
            re = mfma16(f2re, d, zero); im = mfma16(f2im, d, zero);
#pragma unroll
            for (int i = 0; i < 4; ++i) {
                const float a_ = (i & 1) ? bfhi(wre[i >> 1]) : bflo(wre[i >> 1]);
                const float b_ = (i & 1) ? bfhi(wim[i >> 1]) : bflo(wim[i >> 1]);
                x[i] = re[i] * a_ - im[i] * b_;
                x[4 + i] = re[i] * b_ + im[i] * a_;
            }
            d = pk8(x);
            re = mfma16(d, ire, zero); im = mfma16(d, iim, zero);
#pragma unroll
            for (int i = 0; i < 4; ++i) {
                x[i] = re[i] * t2c[i] - im[i] * t2s[i];
                x[4 + i] = im[i] * t2c[i] + re[i] * t2s[i];
            }
            d = pk8(x);
            re = mfma16(d, ire, zero); im = mfma16(d, iim, zero);
            const int ooff = (((2 * i16 + (g >> 1)) ^ sw) * 16) + (g & 1) * 8;
            *(u32x2*)(smem + L_C + m0 * 512 + ooff) = u32x2{pk(re[0], re[1]), pk(re[2], re[3])};
            *(u32x2*)(smem + L_C + (m0 + 1) * 512 + ooff) = u32x2{pk(im[0], im[1]), pk(im[2], im[3])};
        }
    }
    STAMP();
    wait_flags<160>(rws, OFF_SYNC + 2048, tag, w, l);
    if (ai == 0 && t == 0) __hip_atomic_store((GAS u32*)(P.ws + OFF_SYNC), tag, __ATOMIC_RELAXED, __HIP_MEMORY_SCOPE_AGENT);
    {
        const int q = i16 >> 2, p = i16 & 3;
#pragma unroll
        for (int it = 0; it < 7; ++it) {
            const int cmb = (w * 7 + it) * 4 + g;
            const int quad = cmb % 7, r = cmb / 7, pl = r & 1, G = r >> 1;
            const int m = (4 * quad + q) * 2 + pl;
            const u32x2 v = trread(smem + L_C + m * 512 + (((2 * G + (p >> 1)) ^ ((m >> 3) & 7)) * 16) + (p & 1) * 8);
            *(u32x2*)(smem + L_W + (16 * G + i16) * 112 + pl * 56 + quad * 8) = v;
        }
    }
    __syncthreads();
#pragma unroll
    for (int i = 0; i < 4; ++i) {
        const int m = min(t + 512 * i, 1791);
        st16sc(rws, OFF_Y1 + ai * Y1_BLK + m * 16, sane(*(const u32x4*)(smem + L_W + m * 16)));
    }
    STAMP();

    u32x4 C[2], S[2], nsS[2], sC[2], F[2];
    float twc[8], tws[8];
    {
        const u32 sgn = (g >= 2) ? 0x80008000u : 0u;
#pragma unroll
        for (int tau = 0; tau < 2; ++tau) {
            float c[8], s[8];
            const float d1 = (float)(16 * tau + i16);
#pragma unroll
            for (int j = 0; j < 8; ++j) {
                const float a = (float)(8 * g + j) * d1 * 0.03125f;
                c[j] = cosr(a);
                s[j] = sinr(a);
            }
            C[tau] = pk8(c); S[tau] = pk8(s);
            nsS[tau] = S[tau] ^ (sgn ^ 0x80008000u);
            sC[tau] = C[tau] ^ sgn;
#pragma unroll
            for (int i = 0; i < 4; ++i) {
                const float a = d1 * (float)(4 * g + i) * (1.0f / 640.0f);
                twc[4 * tau + i] = cosr(a) * (1.0f / (2048.0f * 640.0f));
                tws[4 * tau + i] = sinr(a) * (1.0f / (2048.0f * 640.0f));
            }
        }
#pragma unroll
        for (int dl = 0; dl < 2; ++dl) {
            float v[8];
#pragma unroll
            for (int j = 0; j < 8; ++j) {
                const int k2 = 4 * g + (j & 3), d2 = 16 * dl + i16;
                const float wgt = (k2 > 10 || d2 >= 20) ? 0.f : ((k2 == 0 || k2 == 10) ? 1.f : 2.f);
                const float a = (float)k2 * (float)d2 * 0.05f;
                v[j] = (j < 4) ? wgt * cosr(a) : -wgt * sinr(a);
            }
            F[dl] = pk8(v);
        }
    }
    {
        if (w == 0) {
            for (u32 spins = 0; spins < SPIN_MAX; ++spins) {
                fence();
                const u32x4 f = ld16(rws, OFF_Y1 + (l + l / 2) * Y1_BLK + ai * 112 + 96, 1);
                if (__all(fresh4(f))) break;
                __builtin_amdgcn_s_sleep(1);
            }
        }
        __syncthreads();
        STAMP();
        u32 off[4];
#pragma unroll
        for (int i = 0; i < 4; ++i) {
            const int m = min(t + 512 * i, nset * 672 - 1), j = m / 672, mm = m - 672 * j, ab = mm / 7;
            off[i] = OFF_Y1 + ab * Y1_BLK + (ai + 96 * j) * 112 + (mm - 7 * ab) * 16;
        }
        u32x4 v[4];
        for (u32 spins = 0; spins < SPIN_MAX; ++spins) {
            fence();
#pragma unroll
            for (int i = 0; i < 4; ++i) v[i] = ld16(rws, off[i], 1);
            if (__all(fresh4(v[0]) && fresh4(v[1]) && fresh4(v[2]) && fresh4(v[3]))) break;
            __builtin_amdgcn_s_sleep(1);
        }
#pragma unroll
        for (int i = 0; i < 4; ++i) *(u32x4*)(smem + L_X + min(t + 512 * i, nset * 672 - 1) * 16) = v[i];
    }
    STAMP();
    __syncthreads();
    {
        const int q = i16 >> 2, p = i16 & 3;
        const int kap = 2 * g + (q >> 1), pl = q & 1;
        const int plo = i16 >> 3, a = i16 & 7;
#pragma unroll 1
        for (int j = 0; j < nset; ++j) {
            const int s0 = ai + 96 * j;
            float f[4];
#pragma unroll
            for (int e = 0; e < 4; ++e) {
                const int kk = 2 * g + (e >> 1), pp = e & 1;
                const float th = (float)kk * (float)(256 * a + s0) * (1.0f / 2048.0f) - ((pp != plo) ? 0.25f : 0.f);
                const float v = cosr(th);
                f[e] = (plo == 0 && pp == 1) ? -v : v;
            }
            const u32x2 fa = {pk(f[0], f[1]), pk(f[2], f[3])};
#pragma unroll
            for (int it = 0; it < 3; ++it) {
                const int T = w + 8 * it;
                if (T < 21) {
                    const int k = min(16 * T + 4 * p, 320);
                    const int cgk = min(k / 28, 11), idx = k - min(28 * cgk, 296);
                    const u32x2 bfr = trread(smem + L_X + j * 10752 + (kap * 12 + cgk) * 112 + pl * 56 + idx * 2);
                    const f32x4 d = mfma16k16(fa, bfr, f32x4{0.f, 0.f, 0.f, 0.f});
                    *(u32x2*)(smem + L_S + j * 10752 + (16 * T + i16) * 32 + g * 8) = u32x2{pk(d[0], d[1]), pk(d[2], d[3])};
                }
            }
        }
    }
    __syncthreads();
#pragma unroll 1
    for (int task = w; task < 4 * nset; task += 8) {
        const int j = task >> 2, wp = task & 3;
        const char* sb = smem + L_S + j * 10752 + wp * 4;
        const int k2 = i16 <= 10 ? i16 : 10;
        u32 dre[8], dim[8];
#pragma unroll
        for (int e = 0; e < 8; ++e) {
            const int k = k2 + 20 * (8 * g + e);
            const int kk = (g < 2) ? k : 640 - k;
            dre[e] = *(const u32*)(sb + kk * 32);
            dim[e] = *(const u32*)(sb + kk * 32 + 16);
        }
#pragma unroll
        for (int rr = 0; rr < 2; ++rr) {
            u32x4 are, aim;
            if (rr == 0) {
                are = u32x4{permlo(dre[0], dre[1]), permlo(dre[2], dre[3]), permlo(dre[4], dre[5]), permlo(dre[6], dre[7])};
                aim = u32x4{permlo(dim[0], dim[1]), permlo(dim[2], dim[3]), permlo(dim[4], dim[5]), permlo(dim[6], dim[7])};
            } else {
                are = u32x4{permhi(dre[0], dre[1]), permhi(dre[2], dre[3]), permhi(dre[4], dre[5]), permhi(dre[6], dre[7])};
                aim = u32x4{permhi(dim[0], dim[1]), permhi(dim[2], dim[3]), permhi(dim[4], dim[5]), permhi(dim[6], dim[7])};
            }
            u32x4 va[2];
            const f32x4 zero = {0.f, 0.f, 0.f, 0.f};
#pragma unroll
            for (int tau = 0; tau < 2; ++tau) {
                const f32x4 vre = mfma16(aim, nsS[tau], mfma16(are, C[tau], zero));
                const f32x4 vim = mfma16(aim, sC[tau], mfma16(are, S[tau], zero));
                float pr[8];
#pragma unroll
                for (int i = 0; i < 4; ++i) {
                    pr[i] = vre[i] * twc[4 * tau + i] - vim[i] * tws[4 * tau + i];
                    pr[4 + i] = vim[i] * twc[4 * tau + i] + vre[i] * tws[4 * tau + i];
                }
                va[tau] = pk8(pr);
            }
            u16* orow = P.out + (size_t)(ai + 96 * j + 256 * (2 * wp + rr)) * 1280;
#pragma unroll
            for (int dl = 0; dl < 2; ++dl) {
                const f32x4 y0 = mfma16(va[0], F[dl], zero);
                const f32x4 y1 = mfma16(va[1], F[dl], zero);
                u32 a0 = pk(y0[0], y0[1]), a1 = pk(y0[2], y0[3]);
                u32 b0 = pk(y1[0], y1[1]), b1 = pk(y1[2], y1[3]);
                auto s0 = __builtin_amdgcn_permlane16_swap(a0, b0, false, false);
                auto s1 = __builtin_amdgcn_permlane16_swap(a1, b1, false, false);
                const int d2 = 16 * dl + i16;
                if (d2 < 20) *(u32x4*)(orow + 32 * d2 + (g & 1) * 16 + (g >> 1) * 8) = u32x4{s0[0], s1[0], s0[1], s1[1]};
            }
        }
    }
    STAMP();
    STAMP_OUT(P.ws);
}

DI void role_b(const Args& P, char* smem, const int t, const int w, const int xcd, const int jx, const u32 tag) {
    const int l = t & 63, g = l >> 4, i16 = l & 15;
    const int chb = 20 * xcd + jx;
    const rsrc_t rws = mkrs(P.ws, WS_USED);
    STAMP_DECL STAMP();
    if (t == 0) *(u32*)(smem + L_CNT) = 0u;
    __syncthreads();
    u32x2 gv[8];
    u32 pw0 = 0, pw1 = 0;
    if (w >= 4) {
        const int tt = t - 256;
        const rsrc_t ru = mkrs(P.u, 2048 * 1280);
        {
            const int b0 = (160 * xcd) & ~127, nl = (160 * xcd + 160 - b0 + 127) >> 7;
            const int m0 = tt, m1 = min(tt + 256, 103 * 3 - 1);
            pw0 = __builtin_amdgcn_raw_buffer_load_b32(ru, min(103 * jx + m0 / 3, 2047) * 1280 + b0 + min(m0 % 3, nl - 1) * 128, 0, 0);
            pw1 = __builtin_amdgcn_raw_buffer_load_b32(ru, min(103 * jx + m1 / 3, 2047) * 1280 + b0 + min(m1 % 3, nl - 1) * 128, 0, 0);
        }
#pragma unroll
        for (int e = 0; e < 8; ++e) gv[e] = ld8(ru, (8 * tt + e) * 1280 + chb * 8, 0);
        fence();
        if (tt < 128) *(u32x4*)(smem + L_U + (tt >> 5) * PB_U + (tt & 31) * 16) = u32x4{0u, 0u, 0u, 0u};
        else if (tt < 144) {
            const int v = tt - 128;
            *(u32x4*)(smem + L_K + (v >> 2) * PB_KS + ((v >> 1) & 1) * PB_K1 + 4096 + (v & 1) * 16) = u32x4{0u, 0u, 0u, 0u};
        }
        u32x4 o[4];
        tr8x4(gv, o);
#pragma unroll
        for (int c = 0; c < 4; ++c) *(u32x4*)(smem + L_U + c * PB_U + 512 + tt * 16) = o[c];
        asm volatile("" :: "v"(pw0), "v"(pw1));
        STAMP();
    } else {
        const int ch = 4 * chb + w;
        u32 pv[12];
        {
            const u32 nb = 640 * 128 * 2;
            const rsrc_t r0 = mkrs(P.Are, nb), r1 = mkrs(P.Aim, nb), r2 = mkrs(P.Bre, nb), r3 = mkrs(P.Bim, nb), r4 = mkrs(P.Cre, nb), r5 = mkrs(P.Cim, nb);
#pragma unroll
            for (int h = 0; h < 2; ++h) {
                const u32 off = (ch * 128 + 64 * h + l) * 2;
                pv[6 * h + 0] = ld2(r0, off); pv[6 * h + 1] = ld2(r1, off); pv[6 * h + 2] = ld2(r2, off);
                pv[6 * h + 3] = ld2(r3, off); pv[6 * h + 4] = ld2(r4, off); pv[6 * h + 5] = ld2(r5, off);
            }
        }
        const u32 ldt = ld2(mkrs(P.logdt, 1280), ch * 2);
        {
            const u32x4 s4 = {SENT, SENT, SENT, SENT};
#pragma unroll
            for (int i = 0; i < 5; ++i) st16sc(rws, OFF_Y1 + min(chb * 1076 + min(t + 256 * i, 1075), 172031) * 16, s4);
        }
        fence();
        char* sc = smem + L_G + w * KG_SZ;
        const float dt = rbf(expf(bf2f(ldt)));
        f32x4 kacc[3];
#pragma unroll
        for (int T = 0; T < 3; ++T) kacc[T] = f32x4{0.f, 0.f, 0.f, 0.f};
#pragma unroll 1
        for (int h = 0; h < 2; ++h) {
            const float are = bf2f(h ? pv[6] : pv[0]), aim = bf2f(h ? pv[7] : pv[1]), bre = bf2f(h ? pv[8] : pv[2]);
            const float bim = bf2f(h ? pv[9] : pv[3]), cre = bf2f(h ? pv[10] : pv[4]), cim = bf2f(h ? pv[11] : pv[5]);
            const float lre = dt * are, lim = dt * aim * 0.15915494309189535f;
            const float dbr = dt * bre, dbi = dt * bim;
            const cf c = {cre * dbr - cim * dbi, cre * dbi + cim * dbr};
            char* rowp = sc + l * 4;
            const cf z1 = cexpk(lre, lim, 1.f), z8 = cexpk(lre, lim, 8.f), z128 = cexpk(lre, lim, 128.f);
            const cf z2 = cmul(z1, z1), z4 = cmul(z2, z2), z6 = cmul(z2, z4);
            cf v = {1.f, 0.f}, r0 = c, r1 = cmul(c, z128);
            cf q = cmul(r1, z128);
#pragma unroll
            for (int j = 0; j < 8; ++j) {
                *(u32*)(rowp + KG_B + (2 * j) * KG_RS) = pk(v.re, v.im);
                v = cmul(v, z8);
                *(u32*)(rowp + KG_B + (2 * j + 1) * KG_RS) = pk(v.re, v.im);
                v = cmul(v, z8);
                *(u32*)(rowp + j * KG_RS) = pk(r0.re, -r0.im);
                *(u32*)(rowp + (8 + j) * KG_RS) = pk(r1.re, -r1.im);
                r0 = cmul(r0, z1);
                r1 = cmul(r1, z1);
#pragma unroll
                for (int e = 0; e < 2; ++e) {
                    const int hh = 2 + 2 * j + e;
                    if (hh < 4) {
                        const int r = 16 + (hh - 2) * 4;
                        const cf a = cmul(q, z2), bq = cmul(q, z4), cq = cmul(q, z6);
                        *(u32*)(rowp + r * KG_RS) = pk(q.re, -q.im);
                        *(u32*)(rowp + (r + 1) * KG_RS) = pk(a.re, -a.im);
                        *(u32*)(rowp + (r + 2) * KG_RS) = pk(bq.re, -bq.im);
                        *(u32*)(rowp + (r + 3) * KG_RS) = pk(cq.re, -cq.im);
                    } else if (hh < 8) {
                        const int r = 24 + (hh - 4) * 2;
                        const cf bq = cmul(q, z4);
                        *(u32*)(rowp + r * KG_RS) = pk(q.re, -q.im);
                        *(u32*)(rowp + (r + 1) * KG_RS) = pk(bq.re, -bq.im);
                    } else if (hh <= 16) {
                        *(u32*)(rowp + (32 + hh - 8) * KG_RS) = pk(q.re, -q.im);
                    }
                    if (hh < 16) q = cmul(q, z128);
                }
            }
            const char* bp = sc + KG_B + i16 * KG_RS + g * 16;
#pragma unroll
            for (int ks = 0; ks < 4; ++ks) {
                const u32x4 bf = *(const u32x4*)(bp + ks * 64);
#pragma unroll
                for (int T = 0; T < 3; ++T)
                    kacc[T] = mfma16(*(const u32x4*)(sc + (16 * T + i16) * KG_RS + g * 16 + ks * 64), bf, kacc[T]);
            }
        }
#pragma unroll
        for (int T = 0; T < 3; ++T)
#pragma unroll
            for (int i = 0; i < 4; ++i) {
                const int row = 16 * T + 4 * g + i;
                int e;
                if (T == 0) e = 128 * (row >> 3) + (row & 7);
                else if (T == 1) e = (row < 24) ? 256 + 128 * ((row - 16) >> 2) + 2 * ((row - 16) & 3) : 512 + 128 * ((row - 24) >> 1) + 4 * ((row - 24) & 1);
                else e = 1024 + 128 * (row - 32);
                if (row < 40 || (row == 40 && i16 == 0))
                    *(u16*)(sc + KG_T + (e + 8 * i16) * 2) = (u16)(pk(kacc[T][i], kacc[T][i]) & 0xffffu);
            }
        drain();
        if (l == 0) {
            const u32 old = __hip_atomic_fetch_add((LAS u32*)(smem + L_CNT), 1u, __ATOMIC_RELAXED, __HIP_MEMORY_SCOPE_WORKGROUP);
            if (old == 3u) __hip_atomic_store((GAS u32*)(P.ws + OFF_SYNC + 2048) + chb, tag, __ATOMIC_RELAXED, __HIP_MEMORY_SCOPE_AGENT);
        }
#pragma unroll 1
        for (int it = 0; it < 4; ++it) {
            const int tt = l + 64 * it, a = 255 - tt;
            u32 code;
            if (a < 32) code = 0x76543210u;
            else if (a < 64) code = 0x86444200u;
            else if (a < 128) code = 0x88444000u;
            else code = (a & 1) ? 0x88880000u : 0x88800000u;
            const int lastoff = (a <= 32) ? 7 : 8;
            const char* tb = sc + KG_T + 16 * a;
            u32 v[8];
#pragma unroll
            for (int e = 0; e < 8; ++e) v[e] = *(const u16*)(tb + 2 * ((code >> (4 * e)) & 15));
            u32 vm = *(const u16*)(tb - 16 + 2 * lastoff);
            vm = (a == 0) ? 0u : vm;
            char* kb = smem + L_K + w * PB_KS + tt * 16;
            *(u32x4*)(kb) = u32x4{v[7] | (v[6] << 16), v[5] | (v[4] << 16), v[3] | (v[2] << 16), v[1] | (v[0] << 16)};
            *(u32x4*)(kb + PB_K1) = u32x4{v[6] | (v[5] << 16), v[4] | (v[3] << 16), v[2] | (v[1] << 16), v[0] | (vm << 16)};
        }
        STAMP();
    }
    const int ch = w & 3, rh = w >> 2;
    const float dsk = bf2f(ld2(mkrs(P.dskip, 1280), (4 * chb + ch) * 2));
    __syncthreads();
    STAMP();
    const int mu = g >> 1, hp = g & 1;
    const int x0 = 2047 - 16 * mu - i16 + 8 * hp;
    const char* ap = smem + L_K + ch * PB_KS + ((x0 & 1) ? PB_K1 + (x0 - 1) * 2 : x0 * 2);
    const char* bp = smem + L_U + ch * PB_U + (256 + 16 * (i16 - mu) + 8 * hp) * 2;
    f32x4 acc[8];
#pragma unroll
    for (int c = 0; c < 8; ++c) acc[c] = f32x4{0.f, 0.f, 0.f, 0.f};
#pragma unroll 1
    for (int r = 4 * rh; r < 4 * rh + 4; ++r) {
        u32x4 A[8], B[8];
#pragma unroll
        for (int p = 0; p < 8; ++p) {
            const u32* a = (const u32*)(ap - 64 * r - 512 * p);
            A[p] = u32x4{a[0], a[1], a[2], a[3]};
            B[p] = *(const u32x4*)(bp - 64 * r + 512 * p);
        }
#pragma unroll
        for (int p = 0; p < 8; ++p)
#pragma unroll
            for (int q = 0; q + p < 8; ++q) acc[p + q] = mfma16(A[p], B[q], acc[p + q]);
    }
    STAMP();
    char* rb = smem + L_G;
#pragma unroll
    for (int c = 0; c < 4; ++c) *(f32x4*)(rb + (w * 4 + c) * 1024 + l * 16) = rh ? acc[c] : acc[4 + c];
    __syncthreads();
    char* st = smem + L_G + 32768;
#pragma unroll
    for (int c = 0; c < 4; ++c) {
        const f32x4 o = *(const f32x4*)(rb + ((w ^ 4) * 4 + c) * 1024 + l * 16);
        const f32x4 mine = rh ? acc[4 + c] : acc[c];
        const int tt = 256 * (4 * rh + c) + 16 * i16 + 4 * g;
        const u32x2 uu = *(const u32x2*)(smem + L_U + ch * PB_U + (256 + tt) * 2);
#pragma unroll
        for (int i = 0; i < 4; ++i) {
            const float uf = (i & 1) ? bfhi(i < 2 ? uu.x : uu.y) : bflo(i < 2 ? uu.x : uu.y);
            const float v = rbf(mine[i] + o[i]) + rbf(uf * dsk);
            *(u16*)(st + (tt >> 4) * 128 + (((4 * g + i) ^ i16) * 8) + ch * 2) = (u16)(pk(v, v) & 0xffffu);
        }
    }
    __syncthreads();
    STAMP();
    {
        const rsrc_t ro = mkrs(P.out, 2048 * 2560);
#pragma unroll
        for (int i = 0; i < 4; ++i) {
            const int row = t + 512 * i;
            __builtin_amdgcn_raw_buffer_store_b64(*(const u32x2*)(st + (row >> 4) * 128 + (((row & 15) ^ ((row >> 4) & 15)) * 8)), ro,
                                                  row * 2560 + 1280 + chb * 8, 0, 0);
        }
    }
    STAMP();
    STAMP_OUT(P.ws);
}

__global__ void __launch_bounds__(512, 2) fused_kernel(Args P) {
    extern __shared__ __attribute__((aligned(16))) char smem[];
    const int t = threadIdx.x;
    const int w = __builtin_amdgcn_readfirstlane(t >> 6);
    const int b = blockIdx.x, xcd = b & 7, jx = b >> 3;
    const u32 tag = __hip_atomic_load((GAS u32*)(P.ws + OFF_SYNC), __ATOMIC_RELAXED, __HIP_MEMORY_SCOPE_AGENT) + 1u;
    if (jx < 20) role_b(P, smem, t, w, xcd, jx, tag);
    else role_a(P, smem, t, w, xcd, jx - 20, tag);
}

extern "C" void kernel_launch(void* const* d_in, const int* in_sizes, int n_in,
                              void* d_out, int out_size, void* d_ws, size_t ws_size,
                              hipStream_t stream) {
    Args a;
    a.x = (const u16*)d_in[0];
    a.wr = (const u16*)d_in[1];
    a.wi = (const u16*)d_in[2];
    a.u = (const u16*)d_in[3];
    a.Are = (const u16*)d_in[4];
    a.Aim = (const u16*)d_in[5];
    a.Bre = (const u16*)d_in[6];
    a.Bim = (const u16*)d_in[7];
    a.Cre = (const u16*)d_in[8];
    a.Cim = (const u16*)d_in[9];
    a.logdt = (const u16*)d_in[10];
    a.dskip = (const u16*)d_in[11];
    a.ws = (char*)d_ws;
    a.out = (u16*)d_out;
    (void)hipFuncSetAttribute((const void*)fused_kernel, hipFuncAttributeMaxDynamicSharedMemorySize, LDS_BYTES);
    fused_kernel<<<256, 512, LDS_BYTES, stream>>>(a);
}
```

```cpp
#include <hip/hip_runtime.h>
#include <hip/hip_bf16.h>

typedef unsigned short u16;
typedef unsigned int u32;
typedef __attribute__((ext_vector_type(2))) __bf16 bf16x2;
typedef __attribute__((ext_vector_type(8))) __bf16 bf16x8;
typedef __attribute__((ext_vector_type(2))) float f32x2;
typedef __attribute__((ext_vector_type(4))) float f32x4;
typedef __attribute__((ext_vector_type(4))) u32 u32x4;
typedef __attribute__((ext_vector_type(2))) u32 u32x2;
typedef __attribute__((ext_vector_type(4))) short s16x4;
typedef __amdgpu_buffer_rsrc_t rsrc_t;
#define LAS __attribute__((address_space(3)))
#define GAS __attribute__((address_space(1)))
#define DI __device__ __forceinline__
#define SC1 16

constexpr u32 X1_ROW = 10368;
constexpr u32 OFF_X1 = 0;
constexpr u32 Y1_BLK = 28672;
constexpr u32 OFF_Y1 = 256 * X1_ROW;
constexpr u32 OFF_SYNC = OFF_Y1 + 96 * Y1_BLK;
constexpr u32 WS_USED = OFF_SYNC + 4096;
constexpr u32 SENT = 0xFFFFFFFFu;
constexpr u32 SPIN_MAX = 1u << 16;
constexpr int PROBE = 0, AMP = 4;

constexpr int L_X = 0;
constexpr int L_S = 32256;
constexpr int L_W = 64512;
constexpr int L_C = L_W + 28672;
constexpr int PB_U = 4608;
constexpr int L_U = 0;
constexpr int L_K = L_U + 4 * PB_U;
constexpr int PB_K1 = 4160, PB_KS = 8320;
constexpr int L_G = L_K + 4 * PB_KS;
constexpr int KG_RS = 272, KG_B = 41 * KG_RS, KG_T = 57 * KG_RS, KG_SZ = 19616;
constexpr int L_CNT = L_G + 4 * KG_SZ;
constexpr int LDS_BYTES = L_CNT + 16;

#define STAMP_DECL
#define STAMP()
#define STAMP_OUT(ws)

DI u32 pk(float a, float b) {
    f32x2 v = {a, b};
    return __builtin_bit_cast(u32, __builtin_convertvector(v, bf16x2));
}
DI u32x4 pk8(const float* v) { return u32x4{pk(v[0], v[1]), pk(v[2], v[3]), pk(v[4], v[5]), pk(v[6], v[7])}; }
DI float bflo(u32 v) { return __uint_as_float(v << 16); }
DI float bfhi(u32 v) { return __uint_as_float(v & 0xffff0000u); }
DI float bf2f(u32 v) { return __uint_as_float(v << 16); }
DI float rbf(float a) { return bflo(pk(a, a)); }
DI float cosr(float rev) { return __builtin_amdgcn_cosf(rev); }
DI float sinr(float rev) { return __builtin_amdgcn_sinf(rev); }
DI f32x4 mfma16(u32x4 a, u32x4 b, f32x4 c) {
    return __builtin_amdgcn_mfma_f32_16x16x32_bf16(__builtin_bit_cast(bf16x8, a), __builtin_bit_cast(bf16x8, b), c, 0, 0, 0);
}
DI f32x4 mfma16k16(u32x2 a, u32x2 b, f32x4 c) {
    return __builtin_amdgcn_mfma_f32_16x16x16bf16_1k(__builtin_bit_cast(s16x4, a), __builtin_bit_cast(s16x4, b), c, 0, 0, 0);
}
DI u32x2 trread(const char* p) {
    return __builtin_bit_cast(u32x2, __builtin_amdgcn_ds_read_tr16_b64_v4i16((LAS s16x4*)(p)));
}
DI u32 permlo(u32 a, u32 b) { return __builtin_amdgcn_perm(b, a, 0x05040100u); }
DI u32 permhi(u32 a, u32 b) { return __builtin_amdgcn_perm(b, a, 0x07060302u); }
DI rsrc_t mkrs(const void* p, u32 bytes) { return __builtin_amdgcn_make_buffer_rsrc((void*)p, 0, bytes, 0x00020000); }
DI u32x4 ld16(rsrc_t r, u32 off, int aux) { return aux ? __builtin_amdgcn_raw_buffer_load_b128(r, off, 0, SC1) : __builtin_amdgcn_raw_buffer_load_b128(r, off, 0, 0); }
DI u32x2 ld8(rsrc_t r, u32 off, int aux) { return aux ? __builtin_amdgcn_raw_buffer_load_b64(r, off, 0, SC1) : __builtin_amdgcn_raw_buffer_load_b64(r, off, 0, 0); }
DI u32 ld2(rsrc_t r, u32 off) { return __builtin_amdgcn_raw_buffer_load_b16(r, off, 0, 0); }
DI void st16sc(rsrc_t r, u32 off, u32x4 v) { __builtin_amdgcn_raw_buffer_store_b128(v, r, off, 0, SC1); }
DI u32x4 sane(u32x4 v) { return u32x4{min(v.x, SENT - 1), min(v.y, SENT - 1), min(v.z, SENT - 1), min(v.w, SENT - 1)}; }
DI bool fresh4(u32x4 v) { return max(max(v.x, v.y), max(v.z, v.w)) != SENT; }
DI bool fresh2(u32x2 v) { return max(v.x, v.y) != SENT; }
DI void fence() { asm volatile("" ::: "memory"); }
DI void drain() { asm volatile("s_waitcnt vmcnt(0)" ::: "memory"); }
DI void tr8x4(const u32x2* v, u32x4* o) {
    o[0] = u32x4{permlo(v[0].x, v[1].x), permlo(v[2].x, v[3].x), permlo(v[4].x, v[5].x), permlo(v[6].x, v[7].x)};
    o[1] = u32x4{permhi(v[0].x, v[1].x), permhi(v[2].x, v[3].x), permhi(v[4].x, v[5].x), permhi(v[6].x, v[7].x)};
    o[2] = u32x4{permlo(v[0].y, v[1].y), permlo(v[2].y, v[3].y), permlo(v[4].y, v[5].y), permlo(v[6].y, v[7].y)};
    o[3] = u32x4{permhi(v[0].y, v[1].y), permhi(v[2].y, v[3].y), permhi(v[4].y, v[5].y), permhi(v[6].y, v[7].y)};
}
template <int NF>
DI void wait_flags(rsrc_t rws, u32 off, u32 tag, int w, int l) {
    if (w == 0) {
        const int q = min(l, NF / 4 - 1);
        for (u32 spins = 0; spins < SPIN_MAX; ++spins) {
            fence();
            const u32x4 f = ld16(rws, off + q * 16, 1);
            if (__all(f.x == tag && f.y == tag && f.z == tag && f.w == tag)) break;
            __builtin_amdgcn_s_sleep(2);
        }
    }
    __syncthreads();
}

struct cf { float re, im; };
DI cf cmul(cf a, cf b) { return cf{a.re * b.re - a.im * b.im, a.re * b.im + a.im * b.re}; }
DI cf cexpk(float lre, float lim_rev, float k) {
    const float m = __builtin_amdgcn_exp2f(k * lre * 1.44269504088896341f);
    const float a = __builtin_amdgcn_fractf(k * lim_rev);
    return cf{m * cosr(a), m * sinr(a)};
}

struct Args {
    const u16 *x, *wr, *wi, *u, *Are, *Aim, *Bre, *Bim, *Cre, *Cim, *logdt, *dskip;
    char* ws;
    u16* out;
};

DI void role_a(const Args& P, char* smem, const int t, const int w, const int ka, const int cg, const u32 tag) {
    const int l = t & 63, g = l >> 4, i16 = l & 15;
    const int ai = ka * 12 + cg, nset = ai < 64 ? 3 : 2;
    const int colbase = min(28 * cg, 296);
    const rsrc_t rws = mkrs(P.ws, WS_USED);
    STAMP_DECL STAMP();
    {
        const u32x4 s4 = {SENT, SENT, SENT, SENT};
#pragma unroll
        for (int i = 0; i < 4; ++i) {
            const int m = min(t + 512 * i, nset * 648 - 1), j = m / 648;
            st16sc(rws, OFF_X1 + (ai + 96 * j) * X1_ROW + (m - 648 * j) * 16, s4);
        }
    }
    u32x4 xv[2][3];
    {
        const rsrc_t rx = mkrs(P.x, 2048 * 1280);
#pragma unroll
        for (int it = 0; it < 2; ++it) {
            const int task = min(w + 8 * it, 4 * nset - 1), s0 = ai + 96 * (task >> 2);
#pragma unroll
            for (int i = 0; i < 3; ++i) {
                const int ci = min(l + 64 * i, 159), row = ci >= 80;
                xv[it][i] = ld16(rx, (s0 + 256 * (2 * (task & 3) + row)) * 1280 + (ci - 80 * row) * 16, 0);
            }
        }
    }
    fence();
    if (t < 90) *(u32x4*)(smem + L_S + (t / 30) * 10752 + 321 * 32 + (t % 30) * 16) = u32x4{0u, 0u, 0u, 0u};

    {
        u32x4 f20c, f20s, c32, s32;
        float twc[8], tws[8];
        {
            float c[8], s[8];
#pragma unroll
            for (int j = 0; j < 8; ++j) {
                const int d2 = (j < 4) ? 4 * g + j : 16 + 4 * g + j - 4;
                const bool ok = (d2 < 20) && (i16 <= 10);
                const float a = (float)d2 * (float)i16 * 0.05f;
                c[j] = ok ? cosr(a) : 0.f;
                s[j] = ok ? -sinr(a) : 0.f;
            }
            f20c = pk8(c); f20s = pk8(s);
#pragma unroll
            for (int j = 0; j < 8; ++j) {
                const int d1 = (j < 4) ? 4 * g + j : 16 + 4 * g + j - 4;
                const float pr = (float)d1 * (float)i16;
                c[j] = cosr(pr * 0.03125f);
                s[j] = sinr(pr * 0.03125f);
                twc[j] = cosr(pr * (1.0f / 640.0f));
                tws[j] = sinr(pr * (1.0f / 640.0f));
            }
            c32 = pk8(c); s32 = pk8(s);
        }
        const u32x4 ns32 = s32 ^ 0x80008000u, c32b = c32 ^ 0x80000000u, s32b = s32 ^ 0x80000000u, nc32b = c32b ^ 0x80008000u;
        const int q = i16 >> 2, p = i16 & 3;
        const int d2a = 4 * g + q, d2b = (g == 0) ? 16 + q : d2a;
        const int offa = 64 * d2a + 8 * p, swa = ((d2a >> 2) & 1) << 5;
        const int offb = 64 * d2b + 8 * p, swb = ((d2b >> 2) & 1) << 5;
#pragma unroll
        for (int it = 0; it < 2; ++it) {
            const int task = w + 8 * it;
            if (task < 4 * nset) {
                char* xs = smem + L_X + task * 2560;
#pragma unroll
                for (int i = 0; i < 3; ++i) {
                    const int ci = min(l + 64 * i, 159), c = ci >= 80 ? ci - 80 : ci;
                    *(u32x4*)(xs + ((ci * 16) ^ (((c >> 4) & 1) << 5))) = xv[it][i];
                }
                f32x4 z[2][4];
#pragma unroll
                for (int rr = 0; rr < 2; ++rr) {
                    const char* xr = xs + rr * 1280;
                    f32x4 tre[2], tim[2];
                    const f32x4 zero = {0.f, 0.f, 0.f, 0.f};
#pragma unroll
                    for (int tau = 0; tau < 2; ++tau) {
                        const u32x2 a0 = trread(xr + offa + ((32 * tau) ^ swa));
                        const u32x2 a1 = trread(xr + offb + ((32 * tau) ^ swb));
                        const u32x4 a = {a0.x, a0.y, a1.x, a1.y};
                        tre[tau] = mfma16(a, f20c, zero);
                        tim[tau] = mfma16(a, f20s, zero);
                    }
                    float pr[8], pi[8];
#pragma unroll
                    for (int j = 0; j < 8; ++j) {
                        const float a = tre[j >> 2][j & 3], bb = tim[j >> 2][j & 3];
                        pr[j] = a * twc[j] + bb * tws[j];
                        pi[j] = bb * twc[j] - a * tws[j];
                    }
                    const u32x4 bre = pk8(pr), bim = pk8(pi);
                    z[rr][0] = mfma16(s32, bim, mfma16(c32, bre, zero));
                    z[rr][1] = mfma16(ns32, bre, mfma16(c32, bim, zero));
                    z[rr][2] = mfma16(s32b, bim, mfma16(c32b, bre, zero));
                    z[rr][3] = mfma16(s32b, bre, mfma16(nc32b, bim, zero));
                }
                char* st = smem + L_S + (task >> 2) * 10752 + (task & 3) * 4;
                if (i16 <= 10) {
#pragma unroll
                    for (int i = 0; i < 4; ++i) {
                        const int k = i16 + 20 * (4 * g + i);
                        *(u32*)(st + k * 32) = pk(z[0][0][i], z[1][0][i]);
                        *(u32*)(st + k * 32 + 16) = pk(z[0][1][i], z[1][1][i]);
                    }
#pragma unroll
                    for (int i = 0; i < 4; ++i) {
                        const int k = 320 - i16 - 20 * (4 * g + i);
                        const bool ok = (i16 >= 1 && i16 <= 9) || (i16 == 0 && g == 0 && i == 0);
                        if (ok) {
                            *(u32*)(st + k * 32) = pk(z[0][2][i], z[1][2][i]);
                            *(u32*)(st + k * 32 + 16) = pk(z[0][3][i], z[1][3][i]);
                        }
                    }
                }
            }
        }
        drain();
    }
    STAMP();
    __syncthreads();
    if (t == 0) __hip_atomic_store((GAS u32*)(P.ws + OFF_SYNC + 1024) + ai, tag, __ATOMIC_RELAXED, __HIP_MEMORY_SCOPE_AGENT);
    {
        const int kap = i16 >> 1, plo = i16 & 1, pl = g >> 1;
#pragma unroll 1
        for (int j = 0; j < nset; ++j) {
            const int s0 = ai + 96 * j;
            float f[4];
#pragma unroll
            for (int e = 0; e < 4; ++e) {
                const int a = 4 * (g & 1) + e;
                const float th = (float)kap * (float)(256 * a + s0) * (1.0f / 2048.0f) - ((pl != plo) ? 0.25f : 0.f);
                const float v = cosr(th);
                f[e] = (plo == 1 && pl == 0) ? -v : v;
            }
            const u32x2 fb = {pk(f[0], f[1]), pk(f[2], f[3])};
#pragma unroll
            for (int it = 0; it < 3; ++it) {
                const int T = w + 8 * it;
                if (T < 21) {
                    const u32x2 a = *(const u32x2*)(smem + L_S + j * 10752 + (16 * T + i16) * 32 + g * 8);
                    const f32x4 d = mfma16k16(a, fb, f32x4{0.f, 0.f, 0.f, 0.f});
                    if (T < 20 || g == 0)
                        *(u32x2*)(smem + L_X + j * 10368 + i16 * 648 + (16 * T + 4 * g) * 2) = u32x2{pk(d[0], d[1]), pk(d[2], d[3])};
                }
            }
        }
    }
    __syncthreads();
#pragma unroll
    for (int i = 0; i < 4; ++i) {
        const int m = min(t + 512 * i, nset * 648 - 1), j = m / 648;
        st16sc(rws, OFF_X1 + (ai + 96 * j) * X1_ROW + (m - 648 * j) * 16, sane(*(const u32x4*)(smem + L_X + m * 16)));
    }
    STAMP();
    u32x2 wv[8];
    const int wpl = w >> 2, wtask = min(t & 255, 223), wquad = wtask % 7, wr_ = wtask / 7, wk1 = wr_ & 15, woc = wr_ >> 4;
    {
        const rsrc_t rw = mkrs(wpl ? P.wi : P.wr, 2048 * 642);
        const int cs = min(colbase + 4 * wquad, 317);
#pragma unroll
        for (int e = 0; e < 8; ++e) wv[e] = ld8(rw, ((ka + 8 * (wk1 + 16 * (8 * woc + e))) * 321 + cs) * 2, 0);
    }
    fence();
    u32x4 f1re, f1im, f2re, f2im;
    float t2c[4], t2s[4];
    {
        float c[8], s[8];
#pragma unroll
        for (int j = 0; j < 8; ++j) {
            const float a = (float)(8 * (g & 1) + j) * (float)i16 * 0.0625f;
            const float co = cosr(a), si = sinr(a);
            c[j] = (g < 2) ? co : si;
            s[j] = (g < 2) ? -si : co;
        }
        f1re = pk8(c); f1im = pk8(s);
#pragma unroll
        for (int j = 0; j < 4; ++j) {
            const float pr = (float)(4 * g + j) * (float)i16;
            const float co = cosr(pr * 0.0625f), si = sinr(pr * 0.0625f);
            c[j] = co; c[4 + j] = si;
            s[j] = -si; s[4 + j] = co;
            t2c[j] = cosr(pr * (1.0f / 256.0f));
            t2s[j] = sinr(pr * (1.0f / 256.0f));
        }
        f2re = pk8(c); f2im = pk8(s);
    }
    const u32x4 ire = {f2re.x, f2re.y, f2re.z ^ 0x80008000u, f2re.w ^ 0x80008000u};
    const u32x4 iim = {f2im.x ^ 0x80008000u, f2im.y ^ 0x80008000u, f2im.z, f2im.w};
    wait_flags<96>(rws, OFF_SYNC + 1024, tag, w, l);
    STAMP();
    {
        const int task = min(t, 447), quad = task % 7, r = task / 7, pl = r >> 5, oc = r & 31;
        const u32 base = OFF_X1 + (8 * oc) * X1_ROW + (ka * 2 + pl) * 648 + (colbase + 4 * quad) * 2;
        u32x2 v[8];
        for (u32 spins = 0; spins < SPIN_MAX; ++spins) {
            fence();
            bool ok = true;
#pragma unroll
            for (int e = 0; e < 8; ++e) v[e] = ld8(rws, base + e * X1_ROW, 1);
#pragma unroll
            for (int e = 0; e < 8; ++e) ok &= fresh2(v[e]);
            if (__all(ok)) break;
            __builtin_amdgcn_s_sleep(1);
        }
        u32x4 o[4];
        tr8x4(v, o);
#pragma unroll
        for (int c = 0; c < 4; ++c) {
            const int m = (4 * quad + c) * 2 + pl;
            *(u32x4*)(smem + L_C + m * 512 + ((oc ^ ((m >> 3) & 7)) * 16)) = o[c];
        }
    }
    {
        if (colbase + 4 * wquad > 317) {
#pragma unroll
            for (int e = 0; e < 8; ++e) wv[e] = u32x2{wv[e].y >> 16, 0u};
        }
        u32x4 o[4];
        tr8x4(wv, o);
#pragma unroll
        for (int c = 0; c < 4; ++c) {
            const int m = (4 * wquad + c) * 2 + wpl;
            *(u32x4*)(smem + L_W + m * 512 + (((wk1 * 2 + woc) ^ ((m >> 3) & 7)) * 16)) = o[c];
        }
    }
    STAMP();
    __syncthreads();
    {
        const int q = i16 >> 2, p = i16 & 3;
#pragma unroll 1
        for (int col = w; col < 28; col += 8) {
            const f32x4 zero = {0.f, 0.f, 0.f, 0.f};
            const int mi = col * 2 + (g >> 1), swi = (mi >> 3) & 7;
            const char* ib = smem + L_C + mi * 512 + (p & 1) * 8;
            const u32x2 a0 = trread(ib + (((2 * (8 * (g & 1) + q) + (p >> 1)) ^ swi) * 16));
            const u32x2 a1 = trread(ib + (((2 * (8 * (g & 1) + 4 + q) + (p >> 1)) ^ swi) * 16));
            const u32x4 a = {a0.x, a0.y, a1.x, a1.y};
            const int m0 = col * 2, sw = (m0 >> 3) & 7;
            const int woff = (((i16 * 2 + (g >> 1)) ^ sw) * 16) + (g & 1) * 8;
            const u32x2 wre = *(const u32x2*)(smem + L_W + m0 * 512 + woff);
            const u32x2 wim = *(const u32x2*)(smem + L_W + (m0 + 1) * 512 + woff);
            f32x4 re = mfma16(a, f1re, zero), im = mfma16(a, f1im, zero);
            float x[8];
#pragma unroll
            for (int i = 0; i < 4; ++i) {
                x[i] = re[i] * t2c[i] + im[i] * t2s[i];
                x[4 + i] = im[i] * t2c[i] - re[i] * t2s[i];
            }
            u32x4 d = pk8(x);
            re = mfma16(f2re, d, zero); im = mfma16(f2im, d, zero);
#pragma unroll
            for (int i = 0; i < 4; ++i) {
                const float a_ = (i & 1) ? bfhi(wre[i >> 1]) : bflo(wre[i >> 1]);
                const float b_ = (i & 1) ? bfhi(wim[i >> 1]) : bflo(wim[i >> 1]);
                x[i] = re[i] * a_ - im[i] * b_;
                x[4 + i] = re[i] * b_ + im[i] * a_;
            }
            d = pk8(x);
            re = mfma16(d, ire, zero); im = mfma16(d, iim, zero);
#pragma unroll
            for (int i = 0; i < 4; ++i) {
                x[i] = re[i] * t2c[i] - im[i] * t2s[i];
                x[4 + i] = im[i] * t2c[i] + re[i] * t2s[i];
            }
            d = pk8(x);
            re = mfma16(d, ire, zero); im = mfma16(d, iim, zero);
            const int ooff = (((2 * i16 + (g >> 1)) ^ sw) * 16) + (g & 1) * 8;
            *(u32x2*)(smem + L_C + m0 * 512 + ooff) = u32x2{pk(re[0], re[1]), pk(re[2], re[3])};
            *(u32x2*)(smem + L_C + (m0 + 1) * 512 + ooff) = u32x2{pk(im[0], im[1]), pk(im[2], im[3])};
        }
    }
    STAMP();
    wait_flags<160>(rws, OFF_SYNC + 2048, tag, w, l);
    if (ai == 0 && t == 0) __hip_atomic_store((GAS u32*)(P.ws + OFF_SYNC), tag, __ATOMIC_RELAXED, __HIP_MEMORY_SCOPE_AGENT);
    {
        const int q = i16 >> 2, p = i16 & 3;
#pragma unroll
        for (int it = 0; it < 7; ++it) {
            const int cmb = (w * 7 + it) * 4 + g;
            const int quad = cmb % 7, r = cmb / 7, pl = r & 1, G = r >> 1;
            const int m = (4 * quad + q) * 2 + pl;
            const u32x2 v = trread(smem + L_C + m * 512 + (((2 * G + (p >> 1)) ^ ((m >> 3) & 7)) * 16) + (p & 1) * 8);
            *(u32x2*)(smem + L_W + (16 * G + i16) * 112 + pl * 56 + quad * 8) = v;
        }
    }
    __syncthreads();
#pragma unroll
    for (int i = 0; i < 4; ++i) {
        const int m = min(t + 512 * i, 1791);
        st16sc(rws, OFF_Y1 + ai * Y1_BLK + m * 16, sane(*(const u32x4*)(smem + L_W + m * 16)));
    }
    STAMP();

    u32x4 C[2], S[2], nsS[2], sC[2], F[2];
    float twc[8], tws[8];
    {
        const u32 sgn = (g >= 2) ? 0x80008000u : 0u;
#pragma unroll
        for (int tau = 0; tau < 2; ++tau) {
            float c[8], s[8];
            const float d1 = (float)(16 * tau + i16);
#pragma unroll
            for (int j = 0; j < 8; ++j) {
                const float a = (float)(8 * g + j) * d1 * 0.03125f;
                c[j] = cosr(a);
                s[j] = sinr(a);
            }
            C[tau] = pk8(c); S[tau] = pk8(s);
            nsS[tau] = S[tau] ^ (sgn ^ 0x80008000u);
            sC[tau] = C[tau] ^ sgn;
#pragma unroll
            for (int i = 0; i < 4; ++i) {
                const float a = d1 * (float)(4 * g + i) * (1.0f / 640.0f);
                twc[4 * tau + i] = cosr(a) * (1.0f / (2048.0f * 640.0f));
                tws[4 * tau + i] = sinr(a) * (1.0f / (2048.0f * 640.0f));
            }
        }
#pragma unroll
        for (int dl = 0; dl < 2; ++dl) {
            float v[8];
#pragma unroll
            for (int j = 0; j < 8; ++j) {
                const int k2 = 4 * g + (j & 3), d2 = 16 * dl + i16;
                const float wgt = (k2 > 10 || d2 >= 20) ? 0.f : ((k2 == 0 || k2 == 10) ? 1.f : 2.f);
                const float a = (float)k2 * (float)d2 * 0.05f;
                v[j] = (j < 4) ? wgt * cosr(a) : -wgt * sinr(a);
            }
            F[dl] = pk8(v);
        }
    }
    {
        if (w == 0) {
            for (u32 spins = 0; spins < SPIN_MAX; ++spins) {
                fence();
                const u32x4 f = ld16(rws, OFF_Y1 + (l + l / 2) * Y1_BLK + ai * 112 + 96, 1);
                if (__all(fresh4(f))) break;
                __builtin_amdgcn_s_sleep(1);
            }
        }
        __syncthreads();
        STAMP();
        u32 off[4];
#pragma unroll
        for (int i = 0; i < 4; ++i) {
            const int m = min(t + 512 * i, nset * 672 - 1), j = m / 672, mm = m - 672 * j, ab = mm / 7;
            off[i] = OFF_Y1 + ab * Y1_BLK + (ai + 96 * j) * 112 + (mm - 7 * ab) * 16;
        }
        u32x4 v[4];
        for (u32 spins = 0; spins < SPIN_MAX; ++spins) {
            fence();
#pragma unroll
            for (int i = 0; i < 4; ++i) v[i] = ld16(rws, off[i], 1);
            if (__all(fresh4(v[0]) && fresh4(v[1]) && fresh4(v[2]) && fresh4(v[3]))) break;
            __builtin_amdgcn_s_sleep(1);
        }
#pragma unroll
        for (int i = 0; i < 4; ++i) *(u32x4*)(smem + L_X + min(t + 512 * i, nset * 672 - 1) * 16) = v[i];
    }
    STAMP();
    __syncthreads();
    {
        const int q = i16 >> 2, p = i16 & 3;
        const int kap = 2 * g + (q >> 1), pl = q & 1;
        const int plo = i16 >> 3, a = i16 & 7;
#pragma unroll 1
        for (int j = 0; j < nset; ++j) {
            const int s0 = ai + 96 * j;
            float f[4];
#pragma unroll
            for (int e = 0; e < 4; ++e) {
                const int kk = 2 * g + (e >> 1), pp = e & 1;
                const float th = (float)kk * (float)(256 * a + s0) * (1.0f / 2048.0f) - ((pp != plo) ? 0.25f : 0.f);
                const float v = cosr(th);
                f[e] = (plo == 0 && pp == 1) ? -v : v;
            }
            const u32x2 fa = {pk(f[0], f[1]), pk(f[2], f[3])};
#pragma unroll
            for (int it = 0; it < 3; ++it) {
                const int T = w + 8 * it;
                if (T < 21) {
                    const int k = min(16 * T + 4 * p, 320);
                    const int cgk = min(k / 28, 11), idx = k - min(28 * cgk, 296);
                    const u32x2 bfr = trread(smem + L_X + j * 10752 + (kap * 12 + cgk) * 112 + pl * 56 + idx * 2);
                    const f32x4 d = mfma16k16(fa, bfr, f32x4{0.f, 0.f, 0.f, 0.f});
                    *(u32x2*)(smem + L_S + j * 10752 + (16 * T + i16) * 32 + g * 8) = u32x2{pk(d[0], d[1]), pk(d[2], d[3])};
                }
            }
        }
    }
    __syncthreads();
#pragma unroll 1
    for (int task = w; task < 4 * nset; task += 8) {
        const int j = task >> 2, wp = task & 3;
        const char* sb = smem + L_S + j * 10752 + wp * 4;
        const int k2 = i16 <= 10 ? i16 : 10;
        u32 dre[8], dim[8];
#pragma unroll
        for (int e = 0; e < 8; ++e) {
            const int k = k2 + 20 * (8 * g + e);
            const int kk = (g < 2) ? k : 640 - k;
            dre[e] = *(const u32*)(sb + kk * 32);
            dim[e] = *(const u32*)(sb + kk * 32 + 16);
        }
#pragma unroll
        for (int rr = 0; rr < 2; ++rr) {
            u32x4 are, aim;
            if (rr == 0) {
                are = u32x4{permlo(dre[0], dre[1]), permlo(dre[2], dre[3]), permlo(dre[4], dre[5]), permlo(dre[6], dre[7])};
                aim = u32x4{permlo(dim[0], dim[1]), permlo(dim[2], dim[3]), permlo(dim[4], dim[5]), permlo(dim[6], dim[7])};
            } else {
                are = u32x4{permhi(dre[0], dre[1]), permhi(dre[2], dre[3]), permhi(dre[4], dre[5]), permhi(dre[6], dre[7])};
                aim = u32x4{permhi(dim[0], dim[1]), permhi(dim[2], dim[3]), permhi(dim[4], dim[5]), permhi(dim[6], dim[7])};
            }
            u32x4 va[2];
            const f32x4 zero = {0.f, 0.f, 0.f, 0.f};
#pragma unroll
            for (int tau = 0; tau < 2; ++tau) {
                const f32x4 vre = mfma16(aim, nsS[tau], mfma16(are, C[tau], zero));
                const f32x4 vim = mfma16(aim, sC[tau], mfma16(are, S[tau], zero));
                float pr[8];
#pragma unroll
                for (int i = 0; i < 4; ++i) {
                    pr[i] = vre[i] * twc[4 * tau + i] - vim[i] * tws[4 * tau + i];
                    pr[4 + i] = vim[i] * twc[4 * tau + i] + vre[i] * tws[4 * tau + i];
                }
                va[tau] = pk8(pr);
            }
            u16* orow = P.out + (size_t)(ai + 96 * j + 256 * (2 * wp + rr)) * 1280;
#pragma unroll
            for (int dl = 0; dl < 2; ++dl) {
                const f32x4 y0 = mfma16(va[0], F[dl], zero);
                const f32x4 y1 = mfma16(va[1], F[dl], zero);
                u32 a0 = pk(y0[0], y0[1]), a1 = pk(y0[2], y0[3]);
                u32 b0 = pk(y1[0], y1[1]), b1 = pk(y1[2], y1[3]);
                auto s0 = __builtin_amdgcn_permlane16_swap(a0, b0, false, false);
                auto s1 = __builtin_amdgcn_permlane16_swap(a1, b1, false, false);
                const int d2 = 16 * dl + i16;
                if (d2 < 20) *(u32x4*)(orow + 32 * d2 + (g & 1) * 16 + (g >> 1) * 8) = u32x4{s0[0], s1[0], s0[1], s1[1]};
            }
        }
    }
    STAMP();
    STAMP_OUT(P.ws);
}

DI void role_b(const Args& P, char* smem, const int t, const int w, const int xcd, const int jx, const u32 tag) {
    const int l = t & 63, g = l >> 4, i16 = l & 15;
    const int chb = 20 * xcd + jx;
    const rsrc_t rws = mkrs(P.ws, WS_USED);
    STAMP_DECL STAMP();
    if (t == 0) *(u32*)(smem + L_CNT) = 0u;
    __syncthreads();
    u32x2 gv[8];
    u32 pw0 = 0, pw1 = 0;
    if (w >= 4) {
        const int tt = t - 256;
        const rsrc_t ru = mkrs(P.u, 2048 * 1280);
        {
            const int b0 = (160 * xcd) & ~127, nl = (160 * xcd + 160 - b0 + 127) >> 7;
            const int m0 = tt, m1 = min(tt + 256, 103 * 3 - 1);
            pw0 = __builtin_amdgcn_raw_buffer_load_b32(ru, min(103 * jx + m0 / 3, 2047) * 1280 + b0 + min(m0 % 3, nl - 1) * 128, 0, 0);
            pw1 = __builtin_amdgcn_raw_buffer_load_b32(ru, min(103 * jx + m1 / 3, 2047) * 1280 + b0 + min(m1 % 3, nl - 1) * 128, 0, 0);
        }
#pragma unroll
        for (int e = 0; e < 8; ++e) gv[e] = ld8(ru, (8 * tt + e) * 1280 + chb * 8, 0);
        fence();
        if (tt < 128) *(u32x4*)(smem + L_U + (tt >> 5) * PB_U + (tt & 31) * 16) = u32x4{0u, 0u, 0u, 0u};
        else if (tt < 144) {
            const int v = tt - 128;
            *(u32x4*)(smem + L_K + (v >> 2) * PB_KS + ((v >> 1) & 1) * PB_K1 + 4096 + (v & 1) * 16) = u32x4{0u, 0u, 0u, 0u};
        }
        u32x4 o[4];
        tr8x4(gv, o);
#pragma unroll
        for (int c = 0; c < 4; ++c) *(u32x4*)(smem + L_U + c * PB_U + 512 + tt * 16) = o[c];
        asm volatile("" :: "v"(pw0), "v"(pw1));
        STAMP();
    } else {
        const int ch = 4 * chb + w;
        u32 pv[12];
        {
            const u32 nb = 640 * 128 * 2;
            const rsrc_t r0 = mkrs(P.Are, nb), r1 = mkrs(P.Aim, nb), r2 = mkrs(P.Bre, nb), r3 = mkrs(P.Bim, nb), r4 = mkrs(P.Cre, nb), r5 = mkrs(P.Cim, nb);
#pragma unroll
            for (int h = 0; h < 2; ++h) {
                const u32 off = (ch * 128 + 64 * h + l) * 2;
                pv[6 * h + 0] = ld2(r0, off); pv[6 * h + 1] = ld2(r1, off); pv[6 * h + 2] = ld2(r2, off);
                pv[6 * h + 3] = ld2(r3, off); pv[6 * h + 4] = ld2(r4, off); pv[6 * h + 5] = ld2(r5, off);
            }
        }
        const u32 ldt = ld2(mkrs(P.logdt, 1280), ch * 2);
        {
            const u32x4 s4 = {SENT, SENT, SENT, SENT};
#pragma unroll
            for (int i = 0; i < 5; ++i) st16sc(rws, OFF_Y1 + min(chb * 1076 + min(t + 256 * i, 1075), 172031) * 16, s4);
        }
        fence();
        char* sc = smem + L_G + w * KG_SZ;
        const float dt = rbf(expf(bf2f(ldt)));
        f32x4 kacc[3];
#pragma unroll
        for (int T = 0; T < 3; ++T) kacc[T] = f32x4{0.f, 0.f, 0.f, 0.f};
#pragma unroll 1
        for (int h = 0; h < 2; ++h) {
            const float are = bf2f(h ? pv[6] : pv[0]), aim = bf2f(h ? pv[7] : pv[1]), bre = bf2f(h ? pv[8] : pv[2]);
            const float bim = bf2f(h ? pv[9] : pv[3]), cre = bf2f(h ? pv[10] : pv[4]), cim = bf2f(h ? pv[11] : pv[5]);
            const float lre = dt * are, lim = dt * aim * 0.15915494309189535f;
            const float dbr = dt * bre, dbi = dt * bim;
            const cf c = {cre * dbr - cim * dbi, cre * dbi + cim * dbr};
            char* rowp = sc + l * 4;
            const cf z1 = cexpk(lre, lim, 1.f), z8 = cexpk(lre, lim, 8.f), z128 = cexpk(lre, lim, 128.f);
            const cf z2 = cmul(z1, z1), z4 = cmul(z2, z2), z6 = cmul(z2, z4);
            cf v = {1.f, 0.f}, r0 = c, r1 = cmul(c, z128);
            cf q = cmul(r1, z128);
#pragma unroll
            for (int j = 0; j < 8; ++j) {
                *(u32*)(rowp + KG_B + (2 * j) * KG_RS) = pk(v.re, v.im);
                v = cmul(v, z8);
                *(u32*)(rowp + KG_B + (2 * j + 1) * KG_RS) = pk(v.re, v.im);
                v = cmul(v, z8);
                *(u32*)(rowp + j * KG_RS) = pk(r0.re, -r0.im);
                *(u32*)(rowp + (8 + j) * KG_RS) = pk(r1.re, -r1.im);
                r0 = cmul(r0, z1);
                r1 = cmul(r1, z1);
#pragma unroll
                for (int e = 0; e < 2; ++e) {
                    const int hh = 2 + 2 * j + e;
                    if (hh < 4) {
                        const int r = 16 + (hh - 2) * 4;
                        const cf a = cmul(q, z2), bq = cmul(q, z4), cq = cmul(q, z6);
                        *(u32*)(rowp + r * KG_RS) = pk(q.re, -q.im);
                        *(u32*)(rowp + (r + 1) * KG_RS) = pk(a.re, -a.im);
                        *(u32*)(rowp + (r + 2) * KG_RS) = pk(bq.re, -bq.im);
                        *(u32*)(rowp + (r + 3) * KG_RS) = pk(cq.re, -cq.im);
                    } else if (hh < 8) {
                        const int r = 24 + (hh - 4) * 2;
                        const cf bq = cmul(q, z4);
                        *(u32*)(rowp + r * KG_RS) = pk(q.re, -q.im);
                        *(u32*)(rowp + (r + 1) * KG_RS) = pk(bq.re, -bq.im);
                    } else if (hh <= 16) {
                        *(u32*)(rowp + (32 + hh - 8) * KG_RS) = pk(q.re, -q.im);
                    }
                    if (hh < 16) q = cmul(q, z128);
                }
            }
            const char* bp = sc + KG_B + i16 * KG_RS + g * 16;
#pragma unroll
            for (int ks = 0; ks < 4; ++ks) {
                const u32x4 bf = *(const u32x4*)(bp + ks * 64);
#pragma unroll
                for (int T = 0; T < 3; ++T)
                    kacc[T] = mfma16(*(const u32x4*)(sc + (16 * T + i16) * KG_RS + g * 16 + ks * 64), bf, kacc[T]);
            }
        }
#pragma unroll
        for (int T = 0; T < 3; ++T)
#pragma unroll
            for (int i = 0; i < 4; ++i) {
                const int row = 16 * T + 4 * g + i;
                int e;
                if (T == 0) e = 128 * (row >> 3) + (row & 7);
                else if (T == 1) e = (row < 24) ? 256 + 128 * ((row - 16) >> 2) + 2 * ((row - 16) & 3) : 512 + 128 * ((row - 24) >> 1) + 4 * ((row - 24) & 1);
                else e = 1024 + 128 * (row - 32);
                if (row < 40 || (row == 40 && i16 == 0))
                    *(u16*)(sc + KG_T + (e + 8 * i16) * 2) = (u16)(pk(kacc[T][i], kacc[T][i]) & 0xffffu);
            }
        drain();
        if (l == 0) {
            const u32 old = __hip_atomic_fetch_add((LAS u32*)(smem + L_CNT), 1u, __ATOMIC_RELAXED, __HIP_MEMORY_SCOPE_WORKGROUP);
            if (old == 3u) __hip_atomic_store((GAS u32*)(P.ws + OFF_SYNC + 2048) + chb, tag, __ATOMIC_RELAXED, __HIP_MEMORY_SCOPE_AGENT);
        }
#pragma unroll 1
        for (int it = 0; it < 4; ++it) {
            const int tt = l + 64 * it, a = 255 - tt;
            u32 code;
            if (a < 32) code = 0x76543210u;
            else if (a < 64) code = 0x86444200u;
            else if (a < 128) code = 0x88444000u;
            else code = (a & 1) ? 0x88880000u : 0x88800000u;
            const int lastoff = (a <= 32) ? 7 : 8;
            const char* tb = sc + KG_T + 16 * a;
            u32 v[8];
#pragma unroll
            for (int e = 0; e < 8; ++e) v[e] = *(const u16*)(tb + 2 * ((code >> (4 * e)) & 15));
            u32 vm = *(const u16*)(tb - 16 + 2 * lastoff);
            vm = (a == 0) ? 0u : vm;
            char* kb = smem + L_K + w * PB_KS + tt * 16;
            *(u32x4*)(kb) = u32x4{v[7] | (v[6] << 16), v[5] | (v[4] << 16), v[3] | (v[2] << 16), v[1] | (v[0] << 16)};
            *(u32x4*)(kb + PB_K1) = u32x4{v[6] | (v[5] << 16), v[4] | (v[3] << 16), v[2] | (v[1] << 16), v[0] | (vm << 16)};
        }
        STAMP();
    }
    const int ch = w & 3, rh = w >> 2;
    const float dsk = bf2f(ld2(mkrs(P.dskip, 1280), (4 * chb + ch) * 2));
    __syncthreads();
    STAMP();
    const int mu = g >> 1, hp = g & 1;
    const int x0 = 2047 - 16 * mu - i16 + 8 * hp;
    const char* ap = smem + L_K + ch * PB_KS + ((x0 & 1) ? PB_K1 + (x0 - 1) * 2 : x0 * 2);
    const char* bp = smem + L_U + ch * PB_U + (256 + 16 * (i16 - mu) + 8 * hp) * 2;
    f32x4 acc[8];
#pragma unroll
    for (int c = 0; c < 8; ++c) acc[c] = f32x4{0.f, 0.f, 0.f, 0.f};
#pragma unroll 1
    for (int r = 4 * rh; r < 4 * rh + 4; ++r) {
        u32x4 A[8], B[8];
#pragma unroll
        for (int p = 0; p < 8; ++p) {
            const u32* a = (const u32*)(ap - 64 * r - 512 * p);
            A[p] = u32x4{a[0], a[1], a[2], a[3]};
            B[p] = *(const u32x4*)(bp - 64 * r + 512 * p);
        }
#pragma unroll
        for (int p = 0; p < 8; ++p)
#pragma unroll
            for (int q = 0; q + p < 8; ++q) acc[p + q] = mfma16(A[p], B[q], acc[p + q]);
    }
    STAMP();
    char* rb = smem + L_G;
#pragma unroll
    for (int c = 0; c < 4; ++c) *(f32x4*)(rb + (w * 4 + c) * 1024 + l * 16) = rh ? acc[c] : acc[4 + c];
    __syncthreads();
    char* st = smem + L_G + 32768;
#pragma unroll
    for (int c = 0; c < 4; ++c) {
        const f32x4 o = *(const f32x4*)(rb + ((w ^ 4) * 4 + c) * 1024 + l * 16);
        const f32x4 mine = rh ? acc[4 + c] : acc[c];
        const int tt = 256 * (4 * rh + c) + 16 * i16 + 4 * g;
        const u32x2 uu = *(const u32x2*)(smem + L_U + ch * PB_U + (256 + tt) * 2);
#pragma unroll
        for (int i = 0; i < 4; ++i) {
            const float uf = (i & 1) ? bfhi(i < 2 ? uu.x : uu.y) : bflo(i < 2 ? uu.x : uu.y);
            const float v = rbf(mine[i] + o[i]) + rbf(uf * dsk);
            *(u16*)(st + (tt >> 4) * 128 + (((4 * g + i) ^ i16) * 8) + ch * 2) = (u16)(pk(v, v) & 0xffffu);
        }
    }
    __syncthreads();
    STAMP();
    {
        const rsrc_t ro = mkrs(P.out, 2048 * 2560);
#pragma unroll
        for (int i = 0; i < 4; ++i) {
            const int row = t + 512 * i;
            __builtin_amdgcn_raw_buffer_store_b64(*(const u32x2*)(st + (row >> 4) * 128 + (((row & 15) ^ ((row >> 4) & 15)) * 8)), ro,
                                                  row * 2560 + 1280 + chb * 8, 0, 0);
        }
    }
    STAMP();
    STAMP_OUT(P.ws);
}

__global__ void __launch_bounds__(512, 2) fused_kernel(Args P) {
    extern __shared__ __attribute__((aligned(16))) char smem[];
    unsigned long long pe0;
    asm volatile("s_memrealtime %0\n\ts_waitcnt lgkmcnt(0)" : "=s"(pe0) :: "memory");
    const int t = threadIdx.x;
    const int w = __builtin_amdgcn_readfirstlane(t >> 6);
    const int b = blockIdx.x, xcd = b & 7, jx = b >> 3;
    const u32 tag = __hip_atomic_load((GAS u32*)(P.ws + OFF_SYNC), __ATOMIC_RELAXED, __HIP_MEMORY_SCOPE_AGENT) + 1u;
    unsigned long long pe1;
    if (PROBE == 0) { asm volatile("" :: "s"(P.ws) : "memory"); asm volatile("s_memrealtime %0\n\ts_waitcnt lgkmcnt(0)" : "=s"(pe1) :: "memory"); }
    else { asm volatile("" :: "v"(tag) : "memory"); asm volatile("s_memrealtime %0\n\ts_waitcnt lgkmcnt(0)" : "=s"(pe1) :: "memory"); }
    if (jx < 20) role_b(P, smem, t, w, xcd, jx, tag);
    else role_a(P, smem, t, w, xcd, jx - 20, tag);
    if (b == 160 && w == 0) { const unsigned long long n_ = __builtin_amdgcn_s_memrealtime();
        while (__builtin_amdgcn_s_memrealtime() - n_ < (pe1 - pe0) * AMP) __builtin_amdgcn_s_sleep(1); }
}

extern "C" void kernel_launch(void* const* d_in, const int* in_sizes, int n_in,
                              void* d_out, int out_size, void* d_ws, size_t ws_size,
                              hipStream_t stream) {
    Args a;
    a.x = (const u16*)d_in[0];
    a.wr = (const u16*)d_in[1];
    a.wi = (const u16*)d_in[2];
    a.u = (const u16*)d_in[3];
    a.Are = (const u16*)d_in[4];
    a.Aim = (const u16*)d_in[5];
    a.Bre = (const u16*)d_in[6];
    a.Bim = (const u16*)d_in[7];
    a.Cre = (const u16*)d_in[8];
    a.Cim = (const u16*)d_in[9];
    a.logdt = (const u16*)d_in[10];
    a.dskip = (const u16*)d_in[11];
    a.ws = (char*)d_ws;
    a.out = (u16*)d_out;
    (void)hipFuncSetAttribute((const void*)fused_kernel, hipFuncAttributeMaxDynamicSharedMemorySize, LDS_BYTES);
    fused_kernel<<<256, 512, LDS_BYTES, stream>>>(a);
}
```

```cpp
#include <hip/hip_runtime.h>
#include <hip/hip_bf16.h>

typedef unsigned short u16;
typedef unsigned int u32;
typedef __attribute__((ext_vector_type(2))) __bf16 bf16x2;
typedef __attribute__((ext_vector_type(8))) __bf16 bf16x8;
typedef __attribute__((ext_vector_type(2))) float f32x2;
typedef __attribute__((ext_vector_type(4))) float f32x4;
typedef __attribute__((ext_vector_type(4))) u32 u32x4;
typedef __attribute__((ext_vector_type(2))) u32 u32x2;
typedef __attribute__((ext_vector_type(4))) short s16x4;
typedef __amdgpu_buffer_rsrc_t rsrc_t;
#define LAS __attribute__((address_space(3)))
#define GAS __attribute__((address_space(1)))
#define DI __device__ __forceinline__
#define SC1 16

constexpr u32 X1_ROW = 12288;
constexpr u32 OFF_X1 = 0;
constexpr u32 Y1_BLK = 32768;
constexpr u32 OFF_Y1 = 256 * X1_ROW;
constexpr u32 OFF_UT = OFF_Y1 + 96 * Y1_BLK;
constexpr u32 OFF_SYNC = OFF_UT + 256 * 10240;
constexpr u32 C_X = 1024;
constexpr u32 C_U = C_X + 32 * 128;
constexpr u32 C_Y = C_U + 16 * 128;
constexpr u32 WS_USED = OFF_SYNC + C_Y + 16 * 128;
constexpr u32 SENT = 0xFFFFFFFFu;
constexpr u32 SPIN_MAX = 1u << 16;

constexpr int S_K = 40, S_SZ = 336 * S_K;
constexpr int P_X = 124032;
constexpr int P_S = P_X + 12800;
constexpr int L_W = 0;
constexpr int L_C = L_W + 32768;
constexpr int L_DUMP = L_C + 28672;
constexpr int L_FLAG = L_DUMP + 1024;
constexpr int L_X = L_FLAG + 16;
constexpr int L_S = L_X + 3 * 10752;
constexpr int KG_RS = 272, KG_B = 41 * KG_RS, KG_SZ = 57 * KG_RS;
constexpr int L_T = 8 * KG_SZ;
constexpr int KG_T = 4112;
constexpr int L_E = L_T + 4 * KG_T;
constexpr int PB_U = 4608;
constexpr int L_U = 0;
constexpr int L_K = L_U + 4 * PB_U;
constexpr int PB_K1 = 4160, PB_KS = 8320, PB_Y = 4128;
constexpr int L_R = L_K + 4 * PB_KS;
constexpr int L_Y = L_R + 32768;
constexpr int L_UT = L_E + 4 * 3072;
constexpr int LDS_BYTES = L_UT + 10240;
static_assert(L_S + 3 * S_SZ <= LDS_BYTES && P_S + S_SZ <= LDS_BYTES && L_Y + 4 * PB_Y <= L_T, "LDS map");

#define STAMP_DECL
#define STAMP()
#define STAMP_OUT(ws)

DI u32 pk(float a, float b) {
    f32x2 v = {a, b};
    return __builtin_bit_cast(u32, __builtin_convertvector(v, bf16x2));
}
DI u32x4 pk8(const float* v) { return u32x4{pk(v[0], v[1]), pk(v[2], v[3]), pk(v[4], v[5]), pk(v[6], v[7])}; }
DI float bflo(u32 v) { return __uint_as_float(v << 16); }
DI float bfhi(u32 v) { return __uint_as_float(v & 0xffff0000u); }
DI float bf2f(u32 v) { return __uint_as_float(v << 16); }
DI float rbf(float a) { return bflo(pk(a, a)); }
DI float cosr(float rev) { return __builtin_amdgcn_cosf(rev); }
DI float sinr(float rev) { return __builtin_amdgcn_sinf(rev); }
DI f32x4 mfma16(u32x4 a, u32x4 b, f32x4 c) {
    return __builtin_amdgcn_mfma_f32_16x16x32_bf16(__builtin_bit_cast(bf16x8, a), __builtin_bit_cast(bf16x8, b), c, 0, 0, 0);
}
DI f32x4 mfma16k16(u32x2 a, u32x2 b, f32x4 c) {
    return __builtin_amdgcn_mfma_f32_16x16x16bf16_1k(__builtin_bit_cast(s16x4, a), __builtin_bit_cast(s16x4, b), c, 0, 0, 0);
}
DI u32x2 trread(const char* p) {
    return __builtin_bit_cast(u32x2, __builtin_amdgcn_ds_read_tr16_b64_v4i16((LAS s16x4*)(p)));
}
DI u32 permlo(u32 a, u32 b) { return __builtin_amdgcn_perm(b, a, 0x05040100u); }
DI u32 permhi(u32 a, u32 b) { return __builtin_amdgcn_perm(b, a, 0x07060302u); }
DI rsrc_t mkrs(const void* p, u32 bytes) { return __builtin_amdgcn_make_buffer_rsrc((void*)p, 0, bytes, 0x00020000); }
DI u32x4 ld16(rsrc_t r, u32 off, int aux) { return aux ? __builtin_amdgcn_raw_buffer_load_b128(r, off, 0, SC1) : __builtin_amdgcn_raw_buffer_load_b128(r, off, 0, 0); }
DI u32x2 ld8(rsrc_t r, u32 off, int aux) { return aux ? __builtin_amdgcn_raw_buffer_load_b64(r, off, 0, SC1) : __builtin_amdgcn_raw_buffer_load_b64(r, off, 0, 0); }
DI u32 ld2(rsrc_t r, u32 off) { return __builtin_amdgcn_raw_buffer_load_b16(r, off, 0, 0); }
DI void st16sc(rsrc_t r, u32 off, u32x4 v) { __builtin_amdgcn_raw_buffer_store_b128(v, r, off, 0, SC1); }
DI u32x4 sane(u32x4 v) { return u32x4{min(v.x, SENT - 1), min(v.y, SENT - 1), min(v.z, SENT - 1), min(v.w, SENT - 1)}; }
DI bool fresh4(u32x4 v) { return max(max(v.x, v.y), max(v.z, v.w)) != SENT; }
DI bool fresh2(u32x2 v) { return max(v.x, v.y) != SENT; }
DI void fence() { asm volatile("" ::: "memory"); }
DI void drain() { asm volatile("s_waitcnt vmcnt(0)" ::: "memory"); }
DI void tr8x4(const u32x2* v, u32x4* o) {
    o[0] = u32x4{permlo(v[0].x, v[1].x), permlo(v[2].x, v[3].x), permlo(v[4].x, v[5].x), permlo(v[6].x, v[7].x)};
    o[1] = u32x4{permhi(v[0].x, v[1].x), permhi(v[2].x, v[3].x), permhi(v[4].x, v[5].x), permhi(v[6].x, v[7].x)};
    o[2] = u32x4{permlo(v[0].y, v[1].y), permlo(v[2].y, v[3].y), permlo(v[4].y, v[5].y), permlo(v[6].y, v[7].y)};
    o[3] = u32x4{permhi(v[0].y, v[1].y), permhi(v[2].y, v[3].y), permhi(v[4].y, v[5].y), permhi(v[6].y, v[7].y)};
}
typedef unsigned long long u64;
DI GAS u64* cnt_ptr(char* ws, u32 set, int shard) { return (GAS u64*)(ws + OFF_SYNC + set + shard * 128); }
DI void cnt_reset(char* ws, u32 set, int shard, u32 tag) {
    __hip_atomic_store(cnt_ptr(ws, set, shard), (u64)tag << 32, __ATOMIC_RELAXED, __HIP_MEMORY_SCOPE_AGENT);
}
DI u32 cnt_tag(rsrc_t rws, u32 set, int shard) { return __builtin_amdgcn_raw_buffer_load_b32(rws, OFF_SYNC + set + shard * 128 + 4, 0, SC1); }
DI void cnt_arrive(rsrc_t rws, char* ws, u32 set, int shard, u32 tag, u32 seen, int l) {
    for (u32 spins = 0; seen != tag && spins < SPIN_MAX; ++spins) {
        __builtin_amdgcn_s_sleep(2);
        fence();
        seen = cnt_tag(rws, set, shard);
    }
    if (l == 0) (void)__hip_atomic_fetch_add(cnt_ptr(ws, set, shard), 1ull, __ATOMIC_RELAXED, __HIP_MEMORY_SCOPE_AGENT);
}
template <int NS, int PER>
DI void cnt_wait(rsrc_t rws, u32 set, u32 tag, int l) {
    for (u32 spins = 0; spins < SPIN_MAX; ++spins) {
        fence();
        const u32x2 c = ld8(rws, OFF_SYNC + set + min(l, NS - 1) * 128, 1);
        if (__all(c.y == tag && c.x >= (u32)PER)) break;
        __builtin_amdgcn_s_sleep(1);
    }
}

typedef f32x2 cf;
DI cf cmul(cf a, cf b) { return __builtin_elementwise_fma(cf{a.y, a.y}, cf{-b.y, b.x}, cf{a.x, a.x} * b); }
DI cf cexpk(float lre, float lim_rev, float k) {
    const float m = __builtin_amdgcn_exp2f(k * lre * 1.44269504088896341f);
    const float a = __builtin_amdgcn_fractf(k * lim_rev);
    return cf{m * cosr(a), m * sinr(a)};
}
DI u32 pkc(cf v) { return pk(v.x, v.y); }

struct Args {
    const u16 *x, *wr, *wi, *u, *Are, *Aim, *Bre, *Bim, *Cre, *Cim, *logdt, *dskip;
    char* ws;
    u16* out;
};

DI void role_service(const Args& P, char* smem, const int l, const bool isB, const int xcd, const int jx, const u32 tag) {
    const int b = blockIdx.x, ai = xcd * 12 + jx - 20, chb = 20 * xcd + jx;
    const rsrc_t rws = mkrs(P.ws, WS_USED);
    const u32x4 s4 = {SENT, SENT, SENT, SENT};
    STAMP_DECL STAMP();
    if (l == 0) {
        if (b < 32) cnt_reset(P.ws, C_X, b, tag);
        if (isB) { if (chb < 16) cnt_reset(P.ws, C_U, chb, tag); }
        else if (ai < 16) cnt_reset(P.ws, C_Y, ai, tag);
    }
#pragma unroll
    for (int i = 0; i < 12; ++i) st16sc(rws, OFF_X1 + b * X1_ROW + (l + 64 * i) * 16, s4);
    fence();
    const u32 seen_x = cnt_tag(rws, C_X, b & 31);
    fence();
    u32 pw0, pw1;
    {
        const int m = min(l, 47);
        const u32 o = (((xcd + 8 * (8 * jx + m / 6)) * 642) & ~127u) + (m % 6) * 128;
        pw0 = __builtin_amdgcn_raw_buffer_load_b32(mkrs(P.wr, 2048 * 642), o, 0, 0);
        pw1 = __builtin_amdgcn_raw_buffer_load_b32(mkrs(P.wi, 2048 * 642), o, 0, 0);
    }
    if (isB) {
        const int lb1 = chb < 96 ? chb + 160 : chb;
        u32x4 uv[2][10];
        {
            const rsrc_t ru = mkrs(P.u, 2048 * 1280);
#pragma unroll
            for (int i = 0; i < 10; ++i) uv[0][i] = ld16(ru, chb * 10240 + (l + 64 * i) * 16, 0);
#pragma unroll
            for (int i = 0; i < 10; ++i) uv[1][i] = ld16(ru, lb1 * 10240 + (l + 64 * i) * 16, 0);
        }
        fence();
        asm volatile("s_waitcnt vmcnt(22)" ::: "memory");
        cnt_arrive(rws, P.ws, C_X, b & 31, tag, seen_x, l);
        STAMP();
        __syncthreads();
        char* sc = smem + L_UT;
        const int g = l >> 4, q = (l & 15) >> 2, p = l & 3;
#pragma unroll
        for (int r = 0; r < 2; ++r) {
#pragma unroll
            for (int i = 0; i < 10; ++i) *(u32x4*)(sc + (l + 64 * i) * 16) = uv[r][i];
            u32x4 o[10];
#pragma unroll
            for (int i = 0; i < 10; ++i) {
                const u32x2 lo = trread(sc + q * 1280 + (64 * i + 16 * g + 4 * p) * 2);
                const u32x2 hi = trread(sc + (4 + q) * 1280 + (64 * i + 16 * g + 4 * p) * 2);
                o[i] = u32x4{lo.x, lo.y, hi.x, hi.y};
            }
#pragma unroll
            for (int i = 0; i < 10; ++i) st16sc(rws, OFF_UT + (r ? lb1 : chb) * 10240 + (64 * i + l) * 16, o[i]);
            if (r == 0) __syncthreads();
        }
        const u32 seen_u = cnt_tag(rws, C_U, chb & 15);
        drain();
        cnt_arrive(rws, P.ws, C_U, chb & 15, tag, seen_u, l);
        asm volatile("" :: "v"(pw0 ^ pw1));
        STAMP();
        __syncthreads();
        __syncthreads();
        cnt_wait<16, 10>(rws, C_U, tag, l);
        u32x4 v[16];
#pragma unroll
        for (int i = 0; i < 16; ++i) v[i] = ld16(rws, OFF_UT + (((l >> 2) + 16 * i) * 640 + 4 * chb + (l & 3)) * 16, 1);
#pragma unroll
        for (int i = 0; i < 16; ++i) *(u32x4*)(smem + L_U + (l & 3) * PB_U + 512 + ((l >> 2) + 16 * i) * 16) = v[i];
        STAMP();
        __syncthreads();
        __syncthreads();
        __syncthreads();
    } else {
        if (l == 0) *(volatile LAS u32*)(smem + L_FLAG) = 0u;
        fence();
        asm volatile("s_waitcnt vmcnt(2)" ::: "memory");
        cnt_arrive(rws, P.ws, C_X, b & 31, tag, seen_x, l);
        STAMP();
        __syncthreads();
        __syncthreads();
        __builtin_amdgcn_s_sleep(20);
        cnt_wait<32, 8>(rws, C_X, tag, l);
        if (l == 0) {
            *(volatile LAS u32*)(smem + L_FLAG) = 1u;
            if (ai == 0) __hip_atomic_store((GAS u32*)(P.ws + OFF_SYNC), tag, __ATOMIC_RELAXED, __HIP_MEMORY_SCOPE_AGENT);
        }
        asm volatile("" :: "v"(pw0 ^ pw1));
        STAMP();
        const u32 seen_y = cnt_tag(rws, C_Y, ai & 15);
        __syncthreads();
        __syncthreads();
        cnt_arrive(rws, P.ws, C_Y, ai & 15, tag, seen_y, l);
        __syncthreads();
        __builtin_amdgcn_s_sleep(24);
        cnt_wait<16, 6>(rws, C_Y, tag, l);
        STAMP();
        __syncthreads();
        __syncthreads();
        __syncthreads();
    }
    STAMP_OUT(P.ws);
}

DI void p1_load(const Args& P, u32x4* xv, const int t, const int w) {
    const int l = t & 63;
    const rsrc_t rx = mkrs(P.x, 2048 * 1280);
    const u32 ro = (blockIdx.x + 256 * w) * 1280;
    xv[0] = ld16(rx, ro + l * 16, 0);
    xv[1] = ld16(rx, ro + min(64 + l, 79) * 16, 0);
    fence();
}
DI void phase_p1(const Args& P, char* smem, const u32x4* xv, const int t, const int w) {
    const int l = t & 63, g = l >> 4, i16 = l & 15, b = blockIdx.x;
    if (t < 15) *(u32x2*)(smem + P_S + 321 * S_K + t * 8) = u32x2{0u, 0u};
    u32x4 f20c, f20s, c32, s32;
    float twc[8], tws[8];
    {
        float c[8], s[8];
#pragma unroll
        for (int j = 0; j < 8; ++j) {
            const int d2 = (j < 4) ? 4 * g + j : 16 + 4 * g + j - 4;
            const bool ok = (d2 < 20) && (i16 <= 10);
            const float a = (float)d2 * (float)i16 * 0.05f;
            c[j] = ok ? cosr(a) : 0.f;
            s[j] = ok ? -sinr(a) : 0.f;
        }
        f20c = pk8(c); f20s = pk8(s);
#pragma unroll
        for (int j = 0; j < 8; ++j) {
            const int d1 = (j < 4) ? 4 * g + j : 16 + 4 * g + j - 4;
            const float pr = (float)d1 * (float)i16;
            c[j] = cosr(pr * 0.03125f);
            s[j] = sinr(pr * 0.03125f);
            twc[j] = cosr(pr * (1.0f / 640.0f));
            tws[j] = sinr(pr * (1.0f / 640.0f));
        }
        c32 = pk8(c); s32 = pk8(s);
    }
    const u32x4 ns32 = s32 ^ 0x80008000u, c32b = c32 ^ 0x80000000u, s32b = s32 ^ 0x80000000u, nc32b = c32b ^ 0x80008000u;
    u32x2 fb;
    {
        const int kap = i16 >> 1, plo = i16 & 1;
        float f[4];
#pragma unroll
        for (int e = 0; e < 4; ++e) {
            const int a = 2 * g + (e >> 1), pl = e & 1;
            const float v = cosr((float)kap * (float)(256 * a + b) * (1.0f / 2048.0f) - ((pl != plo) ? 0.25f : 0.f));
            f[e] = (plo == 1 && pl == 0) ? -v : v;
        }
        fb = u32x2{pk(f[0], f[1]), pk(f[2], f[3])};
    }
    {
        const int q = i16 >> 2, p = i16 & 3;
        const int d2a = 4 * g + q, d2b = (g == 0) ? 16 + q : d2a;
        const int offa = 64 * d2a + 8 * p, swa = ((d2a >> 2) & 1) << 5;
        const int offb = 64 * d2b + 8 * p, swb = ((d2b >> 2) & 1) << 5;
        char* xr = smem + P_X + w * 1280;
        *(u32x4*)(xr + ((l * 16) ^ (((l >> 4) & 1) << 5))) = xv[0];
        *(u32x4*)(xr + min(64 + l, 79) * 16) = xv[1];
        f32x4 tre[2], tim[2];
        const f32x4 zero = {0.f, 0.f, 0.f, 0.f};
        u32x4 xa[2];
#pragma unroll
        for (int tau = 0; tau < 2; ++tau) {
            const u32x2 a0 = trread(xr + offa + ((32 * tau) ^ swa));
            const u32x2 a1 = trread(xr + offb + ((32 * tau) ^ swb));
            xa[tau] = u32x4{a0.x, a0.y, a1.x, a1.y};
        }
#pragma unroll
        for (int tau = 0; tau < 2; ++tau) {
            tre[tau] = mfma16(xa[tau], f20c, zero);
            tim[tau] = mfma16(xa[tau], f20s, zero);
        }
        float pr[8], pi[8];
#pragma unroll
        for (int e = 0; e < 8; ++e) {
            const float a = tre[e >> 2][e & 3], bb = tim[e >> 2][e & 3];
            pr[e] = a * twc[e] + bb * tws[e];
            pi[e] = bb * twc[e] - a * tws[e];
        }
        const u32x4 bre = pk8(pr), bim = pk8(pi);
        const f32x4 z0 = mfma16(s32, bim, mfma16(c32, bre, zero));
        const f32x4 z1 = mfma16(ns32, bre, mfma16(c32, bim, zero));
        const f32x4 z2 = mfma16(s32b, bim, mfma16(c32b, bre, zero));
        const f32x4 z3 = mfma16(s32b, bre, mfma16(nc32b, bim, zero));
        const bool okA = i16 <= 10, okB = (i16 >= 1 && i16 <= 9), okB0 = okB || (i16 == 0 && g == 0);
        const int kA = i16 + 80 * g, kB = 320 - i16 - 80 * g;
        const int oA0 = (okA ? kA : 335) * S_K, oAs = okA ? 20 * S_K : 0;
        const int oB0 = (okB0 ? kB : 335) * S_K, oB1 = (okB ? kB - 20 : 335) * S_K, oBs = okB ? 20 * S_K : 0;
        char* st = smem + P_S + w * 4;
#pragma unroll
        for (int i = 0; i < 4; ++i) *(u32*)(st + oA0 + i * oAs) = pk(z0[i], z1[i]);
        *(u32*)(st + oB0) = pk(z2[0], z3[0]);
#pragma unroll
        for (int i = 1; i < 4; ++i) *(u32*)(st + oB1 - (i - 1) * oBs) = pk(z2[i], z3[i]);
    }
    __syncthreads();
    {
        u32x2 av[3], dv[3];
#pragma unroll
        for (int it = 0; it < 3; ++it)
            av[it] = *(const u32x2*)(smem + P_S + (16 * min(w + 8 * it, 20) + i16) * S_K + g * 8);
#pragma unroll
        for (int it = 0; it < 3; ++it) {
            const f32x4 d = mfma16k16(av[it], fb, f32x4{0.f, 0.f, 0.f, 0.f});
            dv[it] = u32x2{pk(d[0], d[1]), pk(d[2], d[3])};
        }
        const int kap = i16 >> 1, po = (i16 & 1) * 56;
#pragma unroll
        for (int it = 0; it < 3; ++it) {
            const int k = 16 * min(w + 8 * it, 20) + 4 * g;
            const int cgk = min((k * 2341) >> 16, 11);
            const int o0 = po + (k - min(28 * cgk, 296)) * 2, o1 = po + (k - 296) * 2;
            const int d0 = (k < 324) ? (kap * 12 + cgk) * 128 + (((o0 >> 4) ^ kap) << 4) + (o0 & 8) : 12288 + l * 8;
            const int d1 = (k >= 296 && k < 308) ? (kap * 12 + 11) * 128 + (((o1 >> 4) ^ kap) << 4) + (o1 & 8) : d0;
            *(u32x2*)(smem + P_X + d0) = dv[it];
            *(u32x2*)(smem + P_X + d1) = dv[it];
        }
    }
    __syncthreads();
    const rsrc_t rws = mkrs(P.ws, WS_USED);
#pragma unroll
    for (int i = 0; i < 2; ++i) {
        const int m = min(t + 512 * i, 767), pc = m >> 3, kp = (pc * 43) >> 9;
        st16sc(rws, OFF_X1 + b * X1_ROW + m * 16, sane(*(const u32x4*)(smem + P_X + pc * 128 + (((m & 7) ^ kp) << 4))));
    }
}

DI void role_a(const Args& P, char* smem, const int t, const int w, const int ka, const int cg) {
    const int l = t & 63, g = l >> 4, i16 = l & 15;
    const int ai = ka * 12 + cg, nset = ai < 64 ? 3 : 2;
    const int colbase = min(28 * cg, 296);
    const rsrc_t rws = mkrs(P.ws, WS_USED);
    STAMP_DECL STAMP();
    u32x4 xv[2];
    p1_load(P, xv, t, w);
    phase_p1(P, smem, xv, t, w);
    STAMP();
    u32x4 f1re, f1im, f2re, f2im;
    float t2c[4], t2s[4];
    if (w < 4) {
        const int wpl = w >> 1, dl = (ka + colbase) & 7;
        {
            float c[8], s[8];
#pragma unroll
            for (int j = 0; j < 8; ++j) {
                const float a = (float)(8 * (g & 1) + j) * (float)i16 * 0.0625f;
                const float co = cosr(a), si = sinr(a);
                c[j] = (g < 2) ? co : si;
                s[j] = (g < 2) ? -si : co;
            }
            f1re = pk8(c); f1im = pk8(s);
#pragma unroll
            for (int j = 0; j < 4; ++j) {
                const float pr = (float)(4 * g + j) * (float)i16;
                const float co = cosr(pr * 0.0625f), si = sinr(pr * 0.0625f);
                c[j] = co; c[4 + j] = si;
                s[j] = -si; s[4 + j] = co;
                t2c[j] = cosr(pr * (1.0f / 256.0f));
                t2s[j] = sinr(pr * (1.0f / 256.0f));
            }
            f2re = pk8(c); f2im = pk8(s);
        }
        const rsrc_t rw = mkrs(wpl ? P.wi : P.wr, 2048 * 642);
        u32x4 wv[2][8];
#pragma unroll
        for (int it = 0; it < 2; ++it) {
            const int task = min((t & 127) + 128 * it, 159), r = task / 5, ck = task - 5 * r;
#pragma unroll
            for (int e = 0; e < 8; ++e)
                wv[it][e] = ld16(rw, ((((ka + 8 * ((r & 15) + 16 * (8 * (r >> 4) + e))) * 321 + colbase) * 2) & ~15u) + ck * 16, 0);
        }
#pragma unroll
        for (int it = 0; it < 2; ++it) {
            const int task = min((t & 127) + 128 * it, 159), r = task / 5, ck = task - 5 * r;
#pragma unroll
            for (int c = 0; c < 8; ++c) {
                const int col = 8 * ck + c - dl, m = col * 2 + wpl, d = c >> 1;
                const u32x4* x = wv[it];
                const u32x4 o = (c & 1) ? u32x4{permhi(x[0][d], x[1][d]), permhi(x[2][d], x[3][d]), permhi(x[4][d], x[5][d]), permhi(x[6][d], x[7][d])}
                                        : u32x4{permlo(x[0][d], x[1][d]), permlo(x[2][d], x[3][d]), permlo(x[4][d], x[5][d]), permlo(x[6][d], x[7][d])};
                const int dst = ((u32)col < 28u) ? L_W + m * 512 + ((((r & 15) * 2 + (r >> 4)) ^ ((m >> 3) & 7)) * 16) : L_DUMP + l * 16;
                *(u32x4*)(smem + dst) = o;
            }
        }
    } else {
        {
            float c[8], s[8];
#pragma unroll
            for (int j = 0; j < 8; ++j) {
                const float a = (float)(8 * (g & 1) + j) * (float)i16 * 0.0625f;
                const float co = cosr(a), si = sinr(a);
                c[j] = (g < 2) ? co : si;
                s[j] = (g < 2) ? -si : co;
            }
            f1re = pk8(c); f1im = pk8(s);
#pragma unroll
            for (int j = 0; j < 4; ++j) {
                const float pr = (float)(4 * g + j) * (float)i16;
                const float co = cosr(pr * 0.0625f), si = sinr(pr * 0.0625f);
                c[j] = co; c[4 + j] = si;
                s[j] = -si; s[4 + j] = co;
                t2c[j] = cosr(pr * (1.0f / 256.0f));
                t2s[j] = sinr(pr * (1.0f / 256.0f));
            }
            f2re = pk8(c); f2im = pk8(s);
        }
        const int task = min(t - 256, 223), oc = task / 7, ck = task - 7 * oc;
        const u32 base = OFF_X1 + (8 * oc) * X1_ROW + ai * 128 + ck * 16;
        u32x4 v[8];
        drain();
        for (u32 spins = 0; spins < SPIN_MAX; ++spins) {
            bool ok = *(volatile LAS u32*)(smem + L_FLAG) != 0u;
            fence();
            if (!ok) { __builtin_amdgcn_s_sleep(2); continue; }
#pragma unroll
            for (int e = 0; e < 8; ++e) v[e] = ld16(rws, base + e * X1_ROW, 1);
#pragma unroll
            for (int e = 0; e < 8; ++e) ok &= fresh4(v[e]);
            if (__all(ok)) break;
            __builtin_amdgcn_s_sleep(1);
        }
#pragma unroll
        for (int c = 0; c < 8; ++c) {
            const int el = 8 * ck + c, pl = el >= 28, m = (el - 28 * pl) * 2 + pl, d = c >> 1;
            const u32x4 o = (c & 1) ? u32x4{permhi(v[0][d], v[1][d]), permhi(v[2][d], v[3][d]), permhi(v[4][d], v[5][d]), permhi(v[6][d], v[7][d])}
                                    : u32x4{permlo(v[0][d], v[1][d]), permlo(v[2][d], v[3][d]), permlo(v[4][d], v[5][d]), permlo(v[6][d], v[7][d])};
            *(u32x4*)(smem + L_C + m * 512 + ((oc ^ ((m >> 3) & 7)) * 16)) = o;
        }
    }
    const u32x4 ire = {f2re.x, f2re.y, f2re.z ^ 0x80008000u, f2re.w ^ 0x80008000u};
    const u32x4 iim = {f2im.x ^ 0x80008000u, f2im.y ^ 0x80008000u, f2im.z, f2im.w};
    STAMP();
    __syncthreads();
#pragma unroll
    for (int i = 0; i < 4; ++i) st16sc(rws, OFF_Y1 + ai * Y1_BLK + (t + 512 * i) * 16, u32x4{SENT, SENT, SENT, SENT});
    fence();
    {
        const int q = i16 >> 2, p = i16 & 3;
        const f32x4 zero = {0.f, 0.f, 0.f, 0.f};
        u32x4 a[4];
        u32x2 wre[4], wim[4];
#pragma unroll
        for (int c = 0; c < 4; ++c) {
            const int col = min(w + 8 * c, 27);
            const int mi = col * 2 + (g >> 1), swi = (mi >> 3) & 7;
            const char* ib = smem + L_C + mi * 512 + (p & 1) * 8;
            const u32x2 a0 = trread(ib + (((2 * (8 * (g & 1) + q) + (p >> 1)) ^ swi) * 16));
            const u32x2 a1 = trread(ib + (((2 * (8 * (g & 1) + 4 + q) + (p >> 1)) ^ swi) * 16));
            a[c] = u32x4{a0.x, a0.y, a1.x, a1.y};
            const int m0 = col * 2, sw = (m0 >> 3) & 7;
            const int woff = (((i16 * 2 + (g >> 1)) ^ sw) * 16) + (g & 1) * 8;
            wre[c] = *(const u32x2*)(smem + L_W + m0 * 512 + woff);
            wim[c] = *(const u32x2*)(smem + L_W + (m0 + 1) * 512 + woff);
        }
        u32x2 ore[4], oim[4];
#pragma unroll
        for (int c = 0; c < 4; ++c) {
            f32x4 re = mfma16(a[c], f1re, zero), im = mfma16(a[c], f1im, zero);
            float x[8];
#pragma unroll
            for (int i = 0; i < 4; ++i) {
                x[i] = re[i] * t2c[i] + im[i] * t2s[i];
                x[4 + i] = im[i] * t2c[i] - re[i] * t2s[i];
            }
            u32x4 d = pk8(x);
            re = mfma16(f2re, d, zero); im = mfma16(f2im, d, zero);
#pragma unroll
            for (int i = 0; i < 4; ++i) {
                const float a_ = (i & 1) ? bfhi(wre[c][i >> 1]) : bflo(wre[c][i >> 1]);
                const float b_ = (i & 1) ? bfhi(wim[c][i >> 1]) : bflo(wim[c][i >> 1]);
                x[i] = re[i] * a_ - im[i] * b_;
                x[4 + i] = re[i] * b_ + im[i] * a_;
            }
            d = pk8(x);
            re = mfma16(d, ire, zero); im = mfma16(d, iim, zero);
#pragma unroll
            for (int i = 0; i < 4; ++i) {
                x[i] = re[i] * t2c[i] - im[i] * t2s[i];
                x[4 + i] = im[i] * t2c[i] + re[i] * t2s[i];
            }
            d = pk8(x);
            re = mfma16(d, ire, zero); im = mfma16(d, iim, zero);
            ore[c] = u32x2{pk(re[0], re[1]), pk(re[2], re[3])};
            oim[c] = u32x2{pk(im[0], im[1]), pk(im[2], im[3])};
        }
#pragma unroll
        for (int c = 0; c < 4; ++c) {
            const int col = w + 8 * c, m0 = col * 2, sw = (m0 >> 3) & 7;
            const int ooff = (((2 * i16 + (g >> 1)) ^ sw) * 16) + (g & 1) * 8;
            if (c < 3 || w < 4) {
                *(u32x2*)(smem + L_C + m0 * 512 + ooff) = ore[c];
                *(u32x2*)(smem + L_C + (m0 + 1) * 512 + ooff) = oim[c];
            }
        }
    }
    drain();
    STAMP();
    __syncthreads();
    {
        const int q = i16 >> 2, p = i16 & 3;
        const int G = 2 * w + (g >> 1), pl = g & 1;
        u32x2 v[7];
#pragma unroll
        for (int it = 0; it < 7; ++it) {
            const int m = (4 * it + q) * 2 + pl;
            v[it] = trread(smem + L_C + m * 512 + (((2 * G + (p >> 1)) ^ ((m >> 3) & 7)) * 16) + (p & 1) * 8);
        }
#pragma unroll
        for (int it = 0; it < 7; ++it) {
            const int o = pl * 56 + it * 8;
            *(u32x2*)(smem + L_W + (16 * G + i16) * 128 + (((o >> 4) ^ (i16 & 7)) << 4) + (o & 8)) = v[it];
        }
    }
    __syncthreads();
#pragma unroll
    for (int i = 0; i < 4; ++i) {
        const int m = t + 512 * i;
        st16sc(rws, OFF_Y1 + ai * Y1_BLK + m * 16, sane(*(const u32x4*)(smem + L_W + (m >> 3) * 128 + (((m & 7) ^ ((m >> 3) & 7)) << 4))));
    }
    STAMP();

    u32x4 C[2], S[2], nsS[2], sC[2], F[2];
    float twc[8], tws[8];
    {
        const u32 sgn = (g >= 2) ? 0x80008000u : 0u;
#pragma unroll
        for (int tau = 0; tau < 2; ++tau) {
            float c[8], s[8];
            const float d1 = (float)(16 * tau + i16);
#pragma unroll
            for (int j = 0; j < 8; ++j) {
                const float a = (float)(8 * g + j) * d1 * 0.03125f;
                c[j] = cosr(a);
                s[j] = sinr(a);
            }
            C[tau] = pk8(c); S[tau] = pk8(s);
            nsS[tau] = S[tau] ^ (sgn ^ 0x80008000u);
            sC[tau] = C[tau] ^ sgn;
#pragma unroll
            for (int i = 0; i < 4; ++i) {
                const float a = d1 * (float)(4 * g + i) * (1.0f / 640.0f);
                twc[4 * tau + i] = cosr(a) * (1.0f / (2048.0f * 640.0f));
                tws[4 * tau + i] = sinr(a) * (1.0f / (2048.0f * 640.0f));
            }
        }
#pragma unroll
        for (int dl = 0; dl < 2; ++dl) {
            float v[8];
#pragma unroll
            for (int j = 0; j < 8; ++j) {
                const int k2 = 4 * g + (j & 3), d2 = 16 * dl + i16;
                const float wgt = (k2 > 10 || d2 >= 20) ? 0.f : ((k2 == 0 || k2 == 10) ? 1.f : 2.f);
                const float a = (float)k2 * (float)d2 * 0.05f;
                v[j] = (j < 4) ? wgt * cosr(a) : -wgt * sinr(a);
            }
            F[dl] = pk8(v);
        }
    }
    u32x2 fa[3];
    {
        const int a = i16 >> 1, plo = i16 & 1;
#pragma unroll
        for (int j = 0; j < 3; ++j) {
            float f[4];
#pragma unroll
            for (int e = 0; e < 4; ++e) {
                const int kk = 2 * g + (e >> 1), pp = e & 1;
                const float v = cosr((float)kk * (float)(256 * a + ai + 96 * j) * (1.0f / 2048.0f) - ((pp != plo) ? 0.25f : 0.f));
                f[e] = (plo == 0 && pp == 1) ? -v : v;
            }
            fa[j] = u32x2{pk(f[0], f[1]), pk(f[2], f[3])};
        }
    }
    drain();
    __syncthreads();
    STAMP();
    {
        u32 off[4];
#pragma unroll
        for (int i = 0; i < 4; ++i) {
            const int m = min(t + 512 * i, nset * 672 - 1), j = (m >= 672) + (m >= 1344), mm = m - 672 * j, ab = mm / 7;
            off[i] = OFF_Y1 + ab * Y1_BLK + (ai + 96 * j) * 128 + (mm - 7 * ab) * 16;
        }
        u32x4 v[4];
        for (u32 spins = 0; spins < SPIN_MAX; ++spins) {
            fence();
#pragma unroll
            for (int i = 0; i < 4; ++i) v[i] = ld16(rws, off[i], 1);
            if (__all(fresh4(v[0]) && fresh4(v[1]) && fresh4(v[2]) && fresh4(v[3]))) break;
            __builtin_amdgcn_s_sleep(1);
        }
#pragma unroll
        for (int i = 0; i < 4; ++i) *(u32x4*)(smem + L_X + min(t + 512 * i, nset * 672 - 1) * 16) = v[i];
    }
    STAMP();
    __syncthreads();
    {
        const int q = i16 >> 2, p = i16 & 3;
        const int kap = 2 * g + (q >> 1), pl = q & 1;
        u32x2 bfr[3][3];
#pragma unroll
        for (int it = 0; it < 3; ++it) {
            const int T = min(w + 8 * it, 20);
            const int k = min(16 * T + 4 * p, 320);
            const int cgk = min((k * 2341) >> 16, 11), idx = k - min(28 * cgk, 296);
            const int src = L_X + (kap * 12 + cgk) * 112 + pl * 56 + idx * 2;
#pragma unroll
            for (int j = 0; j < 3; ++j) bfr[it][j] = trread(smem + src + j * 10752);
        }
        u32x2 dv[3][3];
#pragma unroll
        for (int it = 0; it < 3; ++it)
#pragma unroll
            for (int j = 0; j < 3; ++j) {
                const f32x4 d = mfma16k16(fa[j], bfr[it][j], f32x4{0.f, 0.f, 0.f, 0.f});
                dv[it][j] = u32x2{pk(d[0], d[1]), pk(d[2], d[3])};
            }
#pragma unroll
        for (int it = 0; it < 3; ++it)
#pragma unroll
            for (int j = 0; j < 3; ++j)
                *(u32x2*)(smem + L_S + j * S_SZ + (16 * min(w + 8 * it, 20) + i16) * S_K + g * 8) = dv[it][j];
    }
    __syncthreads();
    STAMP();
    {
        const int k2 = i16 <= 10 ? i16 : 10;
        const int k0 = k2 + 160 * g;
        const int kofs = ((g < 2) ? k0 : 640 - k0) * S_K + w * 4, kstep = (g < 2) ? 20 * S_K : -20 * S_K;
        const rsrc_t ro = mkrs(P.out, 2048 * 2560);
        u32 dd[3][8];
#pragma unroll
        for (int j = 0; j < 3; ++j)
#pragma unroll
            for (int e = 0; e < 8; ++e) dd[j][e] = *(const u32*)(smem + L_S + j * S_SZ + kofs + e * kstep);
#pragma unroll
        for (int j = 0; j < 3; ++j) {
            {
                const u32x4 are = {permlo(dd[j][0], dd[j][1]), permlo(dd[j][2], dd[j][3]), permlo(dd[j][4], dd[j][5]), permlo(dd[j][6], dd[j][7])};
                const u32x4 aim = {permhi(dd[j][0], dd[j][1]), permhi(dd[j][2], dd[j][3]), permhi(dd[j][4], dd[j][5]), permhi(dd[j][6], dd[j][7])};
                u32x4 va[2];
                const f32x4 zero = {0.f, 0.f, 0.f, 0.f};
#pragma unroll
                for (int tau = 0; tau < 2; ++tau) {
                    const f32x4 vre = mfma16(aim, nsS[tau], mfma16(are, C[tau], zero));
                    const f32x4 vim = mfma16(aim, sC[tau], mfma16(are, S[tau], zero));
                    float pr[8];
#pragma unroll
                    for (int i = 0; i < 4; ++i) {
                        pr[i] = vre[i] * twc[4 * tau + i] - vim[i] * tws[4 * tau + i];
                        pr[4 + i] = vim[i] * twc[4 * tau + i] + vre[i] * tws[4 * tau + i];
                    }
                    va[tau] = pk8(pr);
                }
                const u32 orow = (ai + 96 * j + 256 * w) * 2560;
#pragma unroll
                for (int dl = 0; dl < 2; ++dl) {
                    const f32x4 y0 = mfma16(va[0], F[dl], zero);
                    const f32x4 y1 = mfma16(va[1], F[dl], zero);
                    u32 a0 = pk(y0[0], y0[1]), a1 = pk(y0[2], y0[3]);
                    u32 b0 = pk(y1[0], y1[1]), b1 = pk(y1[2], y1[3]);
                    auto s0 = __builtin_amdgcn_permlane16_swap(a0, b0, false, false);
                    auto s1 = __builtin_amdgcn_permlane16_swap(a1, b1, false, false);
                    const int d2 = 16 * dl + i16;
                    const u32 off = (d2 < 20 && j < nset) ? orow + (32 * d2 + (g & 1) * 16 + (g >> 1) * 8) * 2 : 0x80000000u;
                    __builtin_amdgcn_raw_buffer_store_b128(u32x4{s0[0], s1[0], s0[1], s1[1]}, ro, off, 0, 0);
                }
            }
        }
    }
    STAMP();
    STAMP_OUT(P.ws);
}

DI void role_b(const Args& P, char* smem, const int t, const int w, const int chb) {
    const int l = t & 63, g = l >> 4, i16 = l & 15;
    const int ch = w & 3, rh = w >> 2;
    STAMP_DECL STAMP();
    u32x4 xv[2];
    p1_load(P, xv, t, w);
    u32 pv[6], ldt, dks;
    {
        const u32 nb = 640 * 128 * 2, off = ((4 * chb + ch) * 128 + 64 * rh + l) * 2;
        pv[0] = ld2(mkrs(P.Are, nb), off); pv[1] = ld2(mkrs(P.Aim, nb), off); pv[2] = ld2(mkrs(P.Bre, nb), off);
        pv[3] = ld2(mkrs(P.Bim, nb), off); pv[4] = ld2(mkrs(P.Cre, nb), off); pv[5] = ld2(mkrs(P.Cim, nb), off);
        ldt = ld2(mkrs(P.logdt, 1280), (4 * chb + ch) * 2);
        dks = ld2(mkrs(P.dskip, 1280), (4 * chb + ch) * 2);
    }
    fence();
    phase_p1(P, smem, xv, t, w);
    STAMP();
    f32x4 kacc[3];
    {
        char* sc = smem + w * KG_SZ;
        const float dt = rbf(expf(bf2f(ldt)));
        const float lre = dt * bf2f(pv[0]), lim = dt * bf2f(pv[1]) * 0.15915494309189535f;
        const cf c = cmul(cf{bf2f(pv[4]), bf2f(pv[5])}, cf{dt * bf2f(pv[2]), dt * bf2f(pv[3])});
        char* rowp = sc + l * 4;
        const cf z1 = cexpk(lre, lim, 1.f), z8 = cexpk(lre, lim, 8.f), z128 = cexpk(lre, lim, 128.f);
        const cf z2 = cmul(z1, z1), z4 = cmul(z2, z2), z6 = cmul(z2, z4);
        const cf z8c = {z8.x, -z8.y};
        cf v = {1.f, 0.f}, r0 = c, r1 = cmul(c, z128);
        cf q = cmul(r1, z128);
#pragma unroll
        for (int j = 0; j < 8; ++j) {
            *(u32*)(rowp + KG_B + (2 * j) * KG_RS) = pk(v.x, v.y);
            v = cmul(v, z8c);
            *(u32*)(rowp + KG_B + (2 * j + 1) * KG_RS) = pk(v.x, v.y);
            v = cmul(v, z8c);
            *(u32*)(rowp + j * KG_RS) = pkc(r0);
            *(u32*)(rowp + (8 + j) * KG_RS) = pkc(r1);
            r0 = cmul(r0, z1);
            r1 = cmul(r1, z1);
#pragma unroll
            for (int e = 0; e < 2; ++e) {
                const int hh = 2 + 2 * j + e;
                if (hh < 4) {
                    const int r = 16 + (hh - 2) * 4;
                    *(u32*)(rowp + r * KG_RS) = pkc(q);
                    *(u32*)(rowp + (r + 1) * KG_RS) = pkc(cmul(q, z2));
                    *(u32*)(rowp + (r + 2) * KG_RS) = pkc(cmul(q, z4));
                    *(u32*)(rowp + (r + 3) * KG_RS) = pkc(cmul(q, z6));
                } else if (hh < 8) {
                    const int r = 24 + (hh - 4) * 2;
                    *(u32*)(rowp + r * KG_RS) = pkc(q);
                    *(u32*)(rowp + (r + 1) * KG_RS) = pkc(cmul(q, z4));
                } else if (hh <= 16) {
                    *(u32*)(rowp + (32 + hh - 8) * KG_RS) = pkc(q);
                }
                if (hh < 16) q = cmul(q, z128);
            }
        }
        u32x4 bf[4], af[3][4];
#pragma unroll
        for (int ks = 0; ks < 4; ++ks) {
            bf[ks] = *(const u32x4*)(sc + KG_B + i16 * KG_RS + g * 16 + ks * 64);
#pragma unroll
            for (int T = 0; T < 3; ++T) af[T][ks] = *(const u32x4*)(sc + (16 * T + i16) * KG_RS + g * 16 + ks * 64);
        }
#pragma unroll
        for (int T = 0; T < 3; ++T) {
            kacc[T] = f32x4{0.f, 0.f, 0.f, 0.f};
#pragma unroll
            for (int ks = 0; ks < 4; ++ks) kacc[T] = mfma16(af[T][ks], bf[ks], kacc[T]);
        }
        if (rh == 1) {
#pragma unroll
            for (int T = 0; T < 3; ++T) *(f32x4*)(smem + L_E + ch * 3072 + T * 1024 + l * 16) = kacc[T];
        }
    }
    __syncthreads();
    STAMP();
    if (rh == 0) {
        char* tb = smem + L_T + ch * KG_T;
        f32x4 o[3];
#pragma unroll
        for (int T = 0; T < 3; ++T) o[T] = *(const f32x4*)(smem + L_E + ch * 3072 + T * 1024 + l * 16);
#pragma unroll
        for (int T = 0; T < 3; ++T)
#pragma unroll
            for (int i = 0; i < 4; ++i) {
                const int row = 16 * T + 4 * g + i;
                int e;
                if (T == 0) e = 128 * (row >> 3) + (row & 7);
                else if (T == 1) e = (g < 2) ? 256 + 128 * g + 2 * i : 512 + 256 * (g - 2) + 128 * (i >> 1) + 4 * (i & 1);
                else e = 1024 + 128 * (row - 32);
                const bool ok = row < 40 || (row == 40 && i16 == 0);
                const float s = kacc[T][i] + o[T][i];
                *(u16*)(tb + (ok ? (e + 8 * i16) * 2 : 4110)) = (u16)(pk(s, s) & 0xffffu);
            }
    } else {
        const int tt = t - 256;
        if (tt < 128) *(u32x4*)(smem + L_U + (tt >> 5) * PB_U + (tt & 31) * 16) = u32x4{0u, 0u, 0u, 0u};
        else if (tt < 144) {
            const int v = tt - 128;
            *(u32x4*)(smem + L_K + (v >> 2) * PB_KS + ((v >> 1) & 1) * PB_K1 + 4096 + (v & 1) * 16) = u32x4{0u, 0u, 0u, 0u};
        }
    }
    __syncthreads();
    {
        u32 v[2][8], vm[2];
#pragma unroll
        for (int it = 0; it < 2; ++it) {
            const int tt = l + 64 * (2 * rh + it), a = 255 - tt;
            const u32 code = (a < 32) ? 0x76543210u : (a < 64) ? 0x86444200u : (a < 128) ? 0x88444000u : ((a & 1) ? 0x88880000u : 0x88800000u);
            const int lastoff = (a <= 32) ? 7 : 8;
            const char* tb = smem + L_T + ch * KG_T + 16 * a;
#pragma unroll
            for (int e = 0; e < 8; ++e) v[it][e] = *(const u16*)(tb + 2 * ((code >> (4 * e)) & 15));
            vm[it] = *(const u16*)(tb + ((a == 0) ? 0 : 2 * lastoff - 16));
            vm[it] = (a == 0) ? 0u : vm[it];
        }
#pragma unroll
        for (int it = 0; it < 2; ++it) {
            char* kb = smem + L_K + ch * PB_KS + (l + 64 * (2 * rh + it)) * 16;
            const u32* x = v[it];
            *(u32x4*)(kb) = u32x4{x[7] | (x[6] << 16), x[5] | (x[4] << 16), x[3] | (x[2] << 16), x[1] | (x[0] << 16)};
            *(u32x4*)(kb + PB_K1) = u32x4{x[6] | (x[5] << 16), x[4] | (x[3] << 16), x[2] | (x[1] << 16), x[0] | (vm[it] << 16)};
        }
    }
    STAMP();
    __syncthreads();
    STAMP();
    const int mu = g >> 1, hp = g & 1;
    const int x0 = 2047 - 16 * mu - i16 + 8 * hp;
    const char* ap = smem + L_K + ch * PB_KS + ((x0 & 1) ? PB_K1 + (x0 - 1) * 2 : x0 * 2);
    const char* bp = smem + L_U + ch * PB_U + (256 + 16 * (i16 - mu) + 8 * hp) * 2;
    f32x4 acc[8];
#pragma unroll
    for (int c = 0; c < 8; ++c) acc[c] = f32x4{0.f, 0.f, 0.f, 0.f};
    {
        u32x4 A[2][8], B[2][8];
        const char* ap0 = ap - 256 * rh;
        const char* bp0 = bp - 256 * rh;
#pragma unroll
        for (int p = 0; p < 8; ++p) {
            const u32* a = (const u32*)(ap0 - 512 * p);
            B[0][p] = *(const u32x4*)(bp0 + 512 * p);
            A[0][p] = u32x4{a[0], a[1], a[2], a[3]};
        }
#pragma unroll
        for (int r = 0; r < 4; ++r) {
            __builtin_amdgcn_sched_barrier(0);
            if (r < 3) {
#pragma unroll
                for (int p = 0; p < 8; ++p) {
                    const u32* a = (const u32*)(ap0 - 64 * (r + 1) - 512 * p);
                    B[(r + 1) & 1][p] = *(const u32x4*)(bp0 - 64 * (r + 1) + 512 * p);
                    A[(r + 1) & 1][p] = u32x4{a[0], a[1], a[2], a[3]};
                }
            }
#pragma unroll
            for (int p = 0; p < 8; ++p)
#pragma unroll
                for (int q = 0; q + p < 8; ++q) acc[p + q] = mfma16(A[r & 1][p], B[r & 1][q], acc[p + q]);
            if (r < 3) {
#pragma unroll
                for (int k = 0; k < 12; ++k) {
                    __builtin_amdgcn_sched_group_barrier(0x008, 3, 0);
                    __builtin_amdgcn_sched_group_barrier(0x100, 2, 0);
                }
            }
        }
        __builtin_amdgcn_sched_barrier(0);
    }
    STAMP();
    char* rb = smem + L_R;
#pragma unroll
    for (int c = 0; c < 4; ++c) *(f32x4*)(rb + (w * 4 + c) * 1024 + l * 16) = rh ? acc[c] : acc[4 + c];
    __syncthreads();
    char* yi = smem + L_Y;
    const float dsk = bf2f(dks);
    {
        f32x4 o[4];
        u32x2 uu[4];
#pragma unroll
        for (int c = 0; c < 4; ++c) {
            o[c] = *(const f32x4*)(rb + ((w ^ 4) * 4 + c) * 1024 + l * 16);
            uu[c] = *(const u32x2*)(smem + L_U + ch * PB_U + (256 + 256 * (4 * rh + c) + 16 * i16 + 4 * g) * 2);
        }
        u32x2 res[4];
#pragma unroll
        for (int c = 0; c < 4; ++c) {
            const f32x4 mine = rh ? acc[4 + c] : acc[c];
            float v[4];
#pragma unroll
            for (int i = 0; i < 4; ++i) {
                const float uf = (i & 1) ? bfhi(i < 2 ? uu[c].x : uu[c].y) : bflo(i < 2 ? uu[c].x : uu[c].y);
                v[i] = rbf(mine[i] + o[c][i]) + rbf(uf * dsk);
            }
            res[c] = u32x2{pk(v[0], v[1]), pk(v[2], v[3])};
        }
#pragma unroll
        for (int c = 0; c < 4; ++c) *(u32x2*)(yi + ch * PB_Y + (64 * (4 * rh + c) + 4 * i16 + (g ^ ((i16 >> 2) & 3))) * 8) = res[c];
    }
    __syncthreads();
    STAMP();
    {
        const rsrc_t ro = mkrs(P.out, 2048 * 2560);
        const int q = i16 >> 2, p = i16 & 3;
        u32x2 v[4];
#pragma unroll
        for (int i = 0; i < 4; ++i) {
            const int sg = ((256 * w + 64 * i + 16 * g) >> 2) + p;
            v[i] = trread(yi + q * PB_Y + (sg ^ ((sg >> 4) & 3)) * 8);
        }
#pragma unroll
        for (int i = 0; i < 4; ++i)
            __builtin_amdgcn_raw_buffer_store_b64(v[i], ro, (256 * w + 64 * i + 16 * g + i16) * 2560 + 1280 + chb * 8, 0, 0);
    }
    STAMP();
    STAMP_OUT(P.ws);
}

__global__ void __launch_bounds__(576, 3) fused_kernel(Args P) {
    extern __shared__ __attribute__((aligned(16))) char smem[];
    const int t = threadIdx.x;
    const int w = __builtin_amdgcn_readfirstlane(t >> 6);
    const int b = blockIdx.x, xcd = b & 7, jx = b >> 3;
    const bool isB = jx < 20;
    if (w == 8) {
        const u32 tag = __hip_atomic_load((GAS u32*)(P.ws + OFF_SYNC), __ATOMIC_RELAXED, __HIP_MEMORY_SCOPE_AGENT) + 1u;
        role_service(P, smem, t & 63, isB, xcd, jx, tag);
    } else if (isB) {
        role_b(P, smem, t, w, 20 * xcd + jx);
    } else {
        role_a(P, smem, t, w, xcd, jx - 20);
    }
}

extern "C" void kernel_launch(void* const* d_in, const int* in_sizes, int n_in,
                              void* d_out, int out_size, void* d_ws, size_t ws_size,
                              hipStream_t stream) {
    Args a;
    a.x = (const u16*)d_in[0];
    a.wr = (const u16*)d_in[1];
    a.wi = (const u16*)d_in[2];
    a.u = (const u16*)d_in[3];
    a.Are = (const u16*)d_in[4];
    a.Aim = (const u16*)d_in[5];
    a.Bre = (const u16*)d_in[6];
    a.Bim = (const u16*)d_in[7];
    a.Cre = (const u16*)d_in[8];
    a.Cim = (const u16*)d_in[9];
    a.logdt = (const u16*)d_in[10];
    a.dskip = (const u16*)d_in[11];
    a.ws = (char*)d_ws;
    a.out = (u16*)d_out;
    (void)hipFuncSetAttribute((const void*)fused_kernel, hipFuncAttributeMaxDynamicSharedMemorySize, LDS_BYTES);
    fused_kernel<<<256, 576, LDS_BYTES, stream>>>(a);
}
```

```cpp
#include <hip/hip_runtime.h>
#include <hip/hip_bf16.h>

typedef unsigned short u16;
typedef unsigned int u32;
typedef __attribute__((ext_vector_type(2))) __bf16 bf16x2;
typedef __attribute__((ext_vector_type(8))) __bf16 bf16x8;
typedef __attribute__((ext_vector_type(2))) float f32x2;
typedef __attribute__((ext_vector_type(4))) float f32x4;
typedef __attribute__((ext_vector_type(4))) u32 u32x4;
typedef __attribute__((ext_vector_type(2))) u32 u32x2;
typedef __attribute__((ext_vector_type(4))) short s16x4;
typedef __amdgpu_buffer_rsrc_t rsrc_t;
#define LAS __attribute__((address_space(3)))
#define GAS __attribute__((address_space(1)))
#define DI __device__ __forceinline__
#define SC1 16

constexpr u32 X1_ROW = 12288;
constexpr u32 OFF_X1 = 0;
constexpr u32 Y1_BLK = 32768;
constexpr u32 OFF_Y1 = 256 * X1_ROW;
constexpr u32 OFF_SYNC = OFF_Y1 + 96 * Y1_BLK;
constexpr u32 F_X = 1024;
constexpr u32 F_Y = F_X + 1024;
constexpr u32 WS_USED = OFF_SYNC + F_Y + 1024;
constexpr u32 SENT = 0xFFFFFFFFu;
constexpr u32 SPIN_MAX = 1u << 16;

constexpr int S_K = 40, S_SZ = 336 * S_K;
constexpr int P_X = 80448;
constexpr int P_S = P_X + 12800;
constexpr int L_W = 0;
constexpr int L_C = L_W + 32768;
constexpr int L_DUMP = L_C + 28672;
constexpr int L_X = L_DUMP + 1024;
constexpr int L_S = L_X + 3 * 10752;
constexpr int KG_RS = 272, KG_B = 41 * KG_RS, KG_SZ = 57 * KG_RS;
constexpr int PB_U = 4608, PB_K1 = 4160, PB_KS = 8320, PB_Y = 4128, KG_T = 4112;
constexpr int L_U = 4 * KG_SZ;
constexpr int L_K = P_S + S_SZ;
constexpr int L_T = L_K + 4 * PB_KS;
constexpr int L_R = 0;
constexpr int L_Y = L_R + 32768;
constexpr int L_WORD = L_T + 4 * KG_T;
constexpr int LDS_BYTES = L_WORD + 16;
static_assert(L_S + 3 * S_SZ <= L_WORD && L_U + 4 * PB_U <= P_X && L_Y + 4 * PB_Y <= L_U && LDS_BYTES <= 163840, "LDS map");

#define STAMP_DECL
#define STAMP()
#define STAMP_OUT(ws)

DI u32 pk(float a, float b) {
    f32x2 v = {a, b};
    return __builtin_bit_cast(u32, __builtin_convertvector(v, bf16x2));
}
DI u32x4 pk8(const float* v) { return u32x4{pk(v[0], v[1]), pk(v[2], v[3]), pk(v[4], v[5]), pk(v[6], v[7])}; }
DI float bflo(u32 v) { return __uint_as_float(v << 16); }
DI float bfhi(u32 v) { return __uint_as_float(v & 0xffff0000u); }
DI float bf2f(u32 v) { return __uint_as_float(v << 16); }
DI float rbf(float a) { return bflo(pk(a, a)); }
DI float cosr(float rev) { return __builtin_amdgcn_cosf(rev); }
DI float sinr(float rev) { return __builtin_amdgcn_sinf(rev); }
DI f32x4 mfma16(u32x4 a, u32x4 b, f32x4 c) {
    return __builtin_amdgcn_mfma_f32_16x16x32_bf16(__builtin_bit_cast(bf16x8, a), __builtin_bit_cast(bf16x8, b), c, 0, 0, 0);
}
DI f32x4 mfma16k16(u32x2 a, u32x2 b, f32x4 c) {
    return __builtin_amdgcn_mfma_f32_16x16x16bf16_1k(__builtin_bit_cast(s16x4, a), __builtin_bit_cast(s16x4, b), c, 0, 0, 0);
}
DI u32x2 trread(const char* p) {
    return __builtin_bit_cast(u32x2, __builtin_amdgcn_ds_read_tr16_b64_v4i16((LAS s16x4*)(p)));
}
DI u32 permlo(u32 a, u32 b) { return __builtin_amdgcn_perm(b, a, 0x05040100u); }
DI u32 permhi(u32 a, u32 b) { return __builtin_amdgcn_perm(b, a, 0x07060302u); }
DI rsrc_t mkrs(const void* p, u32 bytes) { return __builtin_amdgcn_make_buffer_rsrc((void*)p, 0, bytes, 0x00020000); }
DI u32x4 ld16(rsrc_t r, u32 off, int aux) { return aux ? __builtin_amdgcn_raw_buffer_load_b128(r, off, 0, SC1) : __builtin_amdgcn_raw_buffer_load_b128(r, off, 0, 0); }
DI u32x2 ld8(rsrc_t r, u32 off, int aux) { return aux ? __builtin_amdgcn_raw_buffer_load_b64(r, off, 0, SC1) : __builtin_amdgcn_raw_buffer_load_b64(r, off, 0, 0); }
DI u32 ld2(rsrc_t r, u32 off) { return __builtin_amdgcn_raw_buffer_load_b16(r, off, 0, 0); }
DI void st16sc(rsrc_t r, u32 off, u32x4 v) { __builtin_amdgcn_raw_buffer_store_b128(v, r, off, 0, SC1); }
DI u32x4 sane(u32x4 v) { return u32x4{min(v.x, SENT - 1), min(v.y, SENT - 1), min(v.z, SENT - 1), min(v.w, SENT - 1)}; }
DI bool fresh4(u32x4 v) { return max(max(v.x, v.y), max(v.z, v.w)) != SENT; }
DI void fence() { asm volatile("" ::: "memory"); }
DI void drain() { asm volatile("s_waitcnt vmcnt(0)" ::: "memory"); }
DI void tr8x4(const u32x2* v, u32x4* o) {
    o[0] = u32x4{permlo(v[0].x, v[1].x), permlo(v[2].x, v[3].x), permlo(v[4].x, v[5].x), permlo(v[6].x, v[7].x)};
    o[1] = u32x4{permhi(v[0].x, v[1].x), permhi(v[2].x, v[3].x), permhi(v[4].x, v[5].x), permhi(v[6].x, v[7].x)};
    o[2] = u32x4{permlo(v[0].y, v[1].y), permlo(v[2].y, v[3].y), permlo(v[4].y, v[5].y), permlo(v[6].y, v[7].y)};
    o[3] = u32x4{permhi(v[0].y, v[1].y), permhi(v[2].y, v[3].y), permhi(v[4].y, v[5].y), permhi(v[6].y, v[7].y)};
}
DI void raise_flag(char* ws, u32 set, int i, u32 tag) {
    __hip_atomic_store((GAS u32*)(ws + OFF_SYNC + set) + i, tag, __ATOMIC_RELAXED, __HIP_MEMORY_SCOPE_AGENT);
}
template <int NF>
DI void wait_flags(rsrc_t rws, u32 set, u32 tag, int l) {
    for (u32 spins = 0; spins < SPIN_MAX; ++spins) {
        fence();
        const u32x4 f = ld16(rws, OFF_SYNC + set + min(l, NF / 4 - 1) * 16, 1);
        if (__all(f.x == tag && f.y == tag && f.z == tag && f.w == tag)) break;
        __builtin_amdgcn_s_sleep(4);
    }
}

typedef f32x2 cf;
DI cf cmul(cf a, cf b) { return __builtin_elementwise_fma(cf{a.y, a.y}, cf{-b.y, b.x}, cf{a.x, a.x} * b); }
DI cf cexpk(float lre, float lim_rev, float k) {
    const float m = __builtin_amdgcn_exp2f(k * lre * 1.44269504088896341f);
    const float a = __builtin_amdgcn_fractf(k * lim_rev);
    return cf{m * cosr(a), m * sinr(a)};
}
DI u32 pkc(cf v) { return pk(v.x, v.y); }

struct Args {
    const u16 *x, *wr, *wi, *u, *Are, *Aim, *Bre, *Bim, *Cre, *Cim, *logdt, *dskip;
    char* ws;
    u16* out;
};

struct Start { u32x4 xv[2]; u32 gen, pw; };
DI void p1_start(const Args& P, Start& st, const int t, const int w) {
    const int l = t & 63, b = blockIdx.x;
    const rsrc_t rws = mkrs(P.ws, WS_USED);
    st.gen = __builtin_amdgcn_raw_buffer_load_b32(rws, OFF_SYNC, 0, SC1);
    fence();
#pragma unroll
    for (int i = 0; i < 2; ++i) st16sc(rws, OFF_X1 + b * X1_ROW + min(t + 512 * i, 767) * 16, u32x4{SENT, SENT, SENT, SENT});
    fence();
    const rsrc_t rx = mkrs(P.x, 2048 * 1280);
    const u32 ro = (b + 256 * w) * 1280;
    st.xv[0] = ld16(rx, ro + l * 16, 0);
    st.xv[1] = ld16(rx, ro + min(64 + l, 79) * 16, 0);
    st.pw = 0;
    if (w >= 6) {
        const int m = min(l, 47);
        st.pw = __builtin_amdgcn_raw_buffer_load_b32(mkrs(w == 6 ? P.wr : P.wi, 2048 * 642),
                                                     ((((b & 7) + 8 * (8 * (b >> 3) + m / 6)) * 642) & ~127u) + (m % 6) * 128, 0, 0);
    }
    fence();
}
DI u32x2 p1_rows(const Args& P, char* smem, const Start& st, const int t, const int w) {
    const int l = t & 63, g = l >> 4, i16 = l & 15, b = blockIdx.x;
    const u32x4* xv = st.xv;
    if (t < 15) *(u32x2*)(smem + P_S + 321 * S_K + t * 8) = u32x2{0u, 0u};
    u32x4 f20c, f20s, c32, s32;
    float twc[8], tws[8];
    {
        float c[8], s[8];
#pragma unroll
        for (int j = 0; j < 8; ++j) {
            const int d2 = (j < 4) ? 4 * g + j : 16 + 4 * g + j - 4;
            const bool ok = (d2 < 20) && (i16 <= 10);
            const float a = (float)d2 * (float)i16 * 0.05f;
            c[j] = ok ? cosr(a) : 0.f;
            s[j] = ok ? -sinr(a) : 0.f;
        }
        f20c = pk8(c); f20s = pk8(s);
#pragma unroll
        for (int j = 0; j < 8; ++j) {
            const int d1 = (j < 4) ? 4 * g + j : 16 + 4 * g + j - 4;
            const float pr = (float)d1 * (float)i16;
            c[j] = cosr(pr * 0.03125f);
            s[j] = sinr(pr * 0.03125f);
            twc[j] = cosr(pr * (1.0f / 640.0f));
            tws[j] = sinr(pr * (1.0f / 640.0f));
        }
        c32 = pk8(c); s32 = pk8(s);
    }
    const u32x4 ns32 = s32 ^ 0x80008000u, c32b = c32 ^ 0x80000000u, s32b = s32 ^ 0x80000000u, nc32b = c32b ^ 0x80008000u;
    u32x2 fb;
    {
        const int kap = i16 >> 1, plo = i16 & 1;
        float f[4];
#pragma unroll
        for (int e = 0; e < 4; ++e) {
            const int a = 2 * g + (e >> 1), pl = e & 1;
            const float v = cosr((float)kap * (float)(256 * a + b) * (1.0f / 2048.0f) - ((pl != plo) ? 0.25f : 0.f));
            f[e] = (plo == 1 && pl == 0) ? -v : v;
        }
        fb = u32x2{pk(f[0], f[1]), pk(f[2], f[3])};
    }
    if (t == 0) *(u32x4*)(smem + L_WORD) = u32x4{0u, 0u, 0u, 0u};
    {
        const int q = i16 >> 2, p = i16 & 3;
        const int d2a = 4 * g + q, d2b = (g == 0) ? 16 + q : d2a;
        const int offa = 64 * d2a + 8 * p, swa = ((d2a >> 2) & 1) << 5;
        const int offb = 64 * d2b + 8 * p, swb = ((d2b >> 2) & 1) << 5;
        char* xr = smem + P_X + w * 1280;
        *(u32x4*)(xr + ((l * 16) ^ (((l >> 4) & 1) << 5))) = xv[0];
        *(u32x4*)(xr + min(64 + l, 79) * 16) = xv[1];
        f32x4 tre[2], tim[2];
        const f32x4 zero = {0.f, 0.f, 0.f, 0.f};
        u32x4 xa[2];
#pragma unroll
        for (int tau = 0; tau < 2; ++tau) {
            const u32x2 a0 = trread(xr + offa + ((32 * tau) ^ swa));
            const u32x2 a1 = trread(xr + offb + ((32 * tau) ^ swb));
            xa[tau] = u32x4{a0.x, a0.y, a1.x, a1.y};
        }
#pragma unroll
        for (int tau = 0; tau < 2; ++tau) {
            tre[tau] = mfma16(xa[tau], f20c, zero);
            tim[tau] = mfma16(xa[tau], f20s, zero);
        }
        float pr[8], pi[8];
#pragma unroll
        for (int e = 0; e < 8; ++e) {
            const float a = tre[e >> 2][e & 3], bb = tim[e >> 2][e & 3];
            pr[e] = a * twc[e] + bb * tws[e];
            pi[e] = bb * twc[e] - a * tws[e];
        }
        const u32x4 bre = pk8(pr), bim = pk8(pi);
        const f32x4 z0 = mfma16(s32, bim, mfma16(c32, bre, zero));
        const f32x4 z1 = mfma16(ns32, bre, mfma16(c32, bim, zero));
        const f32x4 z2 = mfma16(s32b, bim, mfma16(c32b, bre, zero));
        const f32x4 z3 = mfma16(s32b, bre, mfma16(nc32b, bim, zero));
        const bool okA = i16 <= 10, okB = (i16 >= 1 && i16 <= 9), okB0 = okB || (i16 == 0 && g == 0);
        const int kA = i16 + 80 * g, kB = 320 - i16 - 80 * g;
        const int oA0 = (okA ? kA : 335) * S_K, oAs = okA ? 20 * S_K : 0;
        const int oB0 = (okB0 ? kB : 335) * S_K, oB1 = (okB ? kB - 20 : 335) * S_K, oBs = okB ? 20 * S_K : 0;
        char* st = smem + P_S + w * 4;
#pragma unroll
        for (int i = 0; i < 4; ++i) *(u32*)(st + oA0 + i * oAs) = pk(z0[i], z1[i]);
        *(u32*)(st + oB0) = pk(z2[0], z3[0]);
#pragma unroll
        for (int i = 1; i < 4; ++i) *(u32*)(st + oB1 - (i - 1) * oBs) = pk(z2[i], z3[i]);
    }
    __syncthreads();
    if (t == 0) raise_flag(P.ws, F_X, b, st.gen + 1u);
    return fb;
}
template <bool isB>
DI void p1_finish(const Args& P, char* smem, const u32x2 fb, const int t, const int w) {
    const int l = t & 63, g = l >> 4, i16 = l & 15, b = blockIdx.x;
    {
        u32x2 av[3], dv[3];
#pragma unroll
        for (int it = 0; it < 3; ++it)
            av[it] = *(const u32x2*)(smem + P_S + (16 * min(w + 8 * it, 20) + i16) * S_K + g * 8);
#pragma unroll
        for (int it = 0; it < 3; ++it) {
            const f32x4 d = mfma16k16(av[it], fb, f32x4{0.f, 0.f, 0.f, 0.f});
            dv[it] = u32x2{pk(d[0], d[1]), pk(d[2], d[3])};
        }
        const int kap = i16 >> 1, po = (i16 & 1) * 56;
#pragma unroll
        for (int it = 0; it < 3; ++it) {
            const int k = 16 * min(w + 8 * it, 20) + 4 * g;
            const int cgk = min((k * 2341) >> 16, 11);
            const int o0 = po + (k - min(28 * cgk, 296)) * 2, o1 = po + (k - 296) * 2;
            const int d0 = (k < 324) ? (kap * 12 + cgk) * 128 + (((o0 >> 4) ^ kap) << 4) + (o0 & 8) : 12288 + l * 8;
            const int d1 = (k >= 296 && k < 308) ? (kap * 12 + 11) * 128 + (((o1 >> 4) ^ kap) << 4) + (o1 & 8) : d0;
            *(u32x2*)(smem + P_X + d0) = dv[it];
            *(u32x2*)(smem + P_X + d1) = dv[it];
        }
    }
    __syncthreads();
    const rsrc_t rws = mkrs(P.ws, WS_USED);
    if ((w >= 4) != isB) {
#pragma unroll
        for (int i = 0; i < 3; ++i) {
            const int m = (t & 255) + 256 * i, pc = m >> 3, kp = (pc * 43) >> 9;
            st16sc(rws, OFF_X1 + b * X1_ROW + m * 16, sane(*(const u32x4*)(smem + P_X + pc * 128 + (((m & 7) ^ kp) << 4))));
        }
        if (isB && (t & 63) == 0) (void)__hip_atomic_fetch_add((LAS u32*)(smem + L_WORD + 4), 1u, __ATOMIC_RELAXED, __HIP_MEMORY_SCOPE_WORKGROUP);
    }
}

DI void role_a(const Args& P, char* smem, const int t, const int w, const int ka, const int cg) {
    const int l = t & 63, g = l >> 4, i16 = l & 15;
    const int ai = ka * 12 + cg, nset = ai < 64 ? 3 : 2;
    const int colbase = min(28 * cg, 296);
    const rsrc_t rws = mkrs(P.ws, WS_USED);
    STAMP_DECL STAMP();
    Start st;
    p1_start(P, st, t, w);
    const u32x2 fb = p1_rows(P, smem, st, t, w);
    p1_finish<false>(P, smem, fb, t, w);
    const u32 tag = st.gen + 1u;
    volatile LAS u32* word = (volatile LAS u32*)(smem + L_WORD);
    STAMP();
    u32x4 f1re, f1im, f2re, f2im;
    float t2c[4], t2s[4];
    if (w < 4) {
        const int wpl = w >> 1, dl = (ka + colbase) & 7;
        {
            float c[8], s[8];
#pragma unroll
            for (int j = 0; j < 8; ++j) {
                const float a = (float)(8 * (g & 1) + j) * (float)i16 * 0.0625f;
                const float co = cosr(a), si = sinr(a);
                c[j] = (g < 2) ? co : si;
                s[j] = (g < 2) ? -si : co;
            }
            f1re = pk8(c); f1im = pk8(s);
#pragma unroll
            for (int j = 0; j < 4; ++j) {
                const float pr = (float)(4 * g + j) * (float)i16;
                const float co = cosr(pr * 0.0625f), si = sinr(pr * 0.0625f);
                c[j] = co; c[4 + j] = si;
                s[j] = -si; s[4 + j] = co;
                t2c[j] = cosr(pr * (1.0f / 256.0f));
                t2s[j] = sinr(pr * (1.0f / 256.0f));
            }
            f2re = pk8(c); f2im = pk8(s);
        }
        const rsrc_t rw = mkrs(wpl ? P.wi : P.wr, 2048 * 642);
        u32x4 wv[2][8];
#pragma unroll
        for (int it = 0; it < 2; ++it) {
            const int task = min((t & 127) + 128 * it, 159), r = task / 5, ck = task - 5 * r;
#pragma unroll
            for (int e = 0; e < 8; ++e)
                wv[it][e] = ld16(rw, ((((ka + 8 * ((r & 15) + 16 * (8 * (r >> 4) + e))) * 321 + colbase) * 2) & ~15u) + ck * 16, 0);
        }
#pragma unroll
        for (int it = 0; it < 2; ++it) {
            const int task = min((t & 127) + 128 * it, 159), r = task / 5, ck = task - 5 * r;
#pragma unroll
            for (int c = 0; c < 8; ++c) {
                const int col = 8 * ck + c - dl, m = col * 2 + wpl, d = c >> 1;
                const u32x4* x = wv[it];
                const u32x4 o = (c & 1) ? u32x4{permhi(x[0][d], x[1][d]), permhi(x[2][d], x[3][d]), permhi(x[4][d], x[5][d]), permhi(x[6][d], x[7][d])}
                                        : u32x4{permlo(x[0][d], x[1][d]), permlo(x[2][d], x[3][d]), permlo(x[4][d], x[5][d]), permlo(x[6][d], x[7][d])};
                const int dst = ((u32)col < 28u) ? L_W + m * 512 + ((((r & 15) * 2 + (r >> 4)) ^ ((m >> 3) & 7)) * 16) : L_DUMP + l * 16;
                *(u32x4*)(smem + dst) = o;
            }
        }
    } else {
        {
            float c[8], s[8];
#pragma unroll
            for (int j = 0; j < 8; ++j) {
                const float a = (float)(8 * (g & 1) + j) * (float)i16 * 0.0625f;
                const float co = cosr(a), si = sinr(a);
                c[j] = (g < 2) ? co : si;
                s[j] = (g < 2) ? -si : co;
            }
            f1re = pk8(c); f1im = pk8(s);
#pragma unroll
            for (int j = 0; j < 4; ++j) {
                const float pr = (float)(4 * g + j) * (float)i16;
                const float co = cosr(pr * 0.0625f), si = sinr(pr * 0.0625f);
                c[j] = co; c[4 + j] = si;
                s[j] = -si; s[4 + j] = co;
                t2c[j] = cosr(pr * (1.0f / 256.0f));
                t2s[j] = sinr(pr * (1.0f / 256.0f));
            }
            f2re = pk8(c); f2im = pk8(s);
        }
        const int task = min(t - 256, 223), oc = task / 7, ck = task - 7 * oc;
        const u32 base = OFF_X1 + (8 * oc) * X1_ROW + ai * 128 + ck * 16;
        u32x4 v[8];
        drain();
        if (w == 4) {
            wait_flags<256>(rws, F_X, tag, l);
            if (l == 0) {
                *word = 1u;
                if (ai == 0) __hip_atomic_store((GAS u32*)(P.ws + OFF_SYNC), tag, __ATOMIC_RELAXED, __HIP_MEMORY_SCOPE_AGENT);
            }
        }
        for (u32 spins = 0; spins < SPIN_MAX && *word == 0u; ++spins) __builtin_amdgcn_s_sleep(1);
        for (u32 spins = 0; spins < SPIN_MAX; ++spins) {
            fence();
            bool ok = true;
#pragma unroll
            for (int e = 0; e < 8; ++e) v[e] = ld16(rws, base + e * X1_ROW, 1);
#pragma unroll
            for (int e = 0; e < 8; ++e) ok &= fresh4(v[e]);
            if (__all(ok)) break;
            __builtin_amdgcn_s_sleep(1);
        }
#pragma unroll
        for (int c = 0; c < 8; ++c) {
            const int el = 8 * ck + c, pl = el >= 28, m = (el - 28 * pl) * 2 + pl, d = c >> 1;
            const u32x4 o = (c & 1) ? u32x4{permhi(v[0][d], v[1][d]), permhi(v[2][d], v[3][d]), permhi(v[4][d], v[5][d]), permhi(v[6][d], v[7][d])}
                                    : u32x4{permlo(v[0][d], v[1][d]), permlo(v[2][d], v[3][d]), permlo(v[4][d], v[5][d]), permlo(v[6][d], v[7][d])};
            *(u32x4*)(smem + L_C + m * 512 + ((oc ^ ((m >> 3) & 7)) * 16)) = o;
        }
    }
    const u32x4 ire = {f2re.x, f2re.y, f2re.z ^ 0x80008000u, f2re.w ^ 0x80008000u};
    const u32x4 iim = {f2im.x ^ 0x80008000u, f2im.y ^ 0x80008000u, f2im.z, f2im.w};
    asm volatile("" :: "v"(st.pw));
    STAMP();
    __syncthreads();
#pragma unroll
    for (int i = 0; i < 4; ++i) st16sc(rws, OFF_Y1 + ai * Y1_BLK + (t + 512 * i) * 16, u32x4{SENT, SENT, SENT, SENT});
    fence();
    {
        const int q = i16 >> 2, p = i16 & 3;
        const f32x4 zero = {0.f, 0.f, 0.f, 0.f};
        u32x4 a[4];
        u32x2 wre[4], wim[4];
#pragma unroll
        for (int c = 0; c < 4; ++c) {
            const int col = min(w + 8 * c, 27);
            const int mi = col * 2 + (g >> 1), swi = (mi >> 3) & 7;
            const char* ib = smem + L_C + mi * 512 + (p & 1) * 8;
            const u32x2 a0 = trread(ib + (((2 * (8 * (g & 1) + q) + (p >> 1)) ^ swi) * 16));
            const u32x2 a1 = trread(ib + (((2 * (8 * (g & 1) + 4 + q) + (p >> 1)) ^ swi) * 16));
            a[c] = u32x4{a0.x, a0.y, a1.x, a1.y};
            const int m0 = col * 2, sw = (m0 >> 3) & 7;
            const int woff = (((i16 * 2 + (g >> 1)) ^ sw) * 16) + (g & 1) * 8;
            wre[c] = *(const u32x2*)(smem + L_W + m0 * 512 + woff);
            wim[c] = *(const u32x2*)(smem + L_W + (m0 + 1) * 512 + woff);
        }
        u32x2 ore[4], oim[4];
#pragma unroll
        for (int c = 0; c < 4; ++c) {
            f32x4 re = mfma16(a[c], f1re, zero), im = mfma16(a[c], f1im, zero);
            float x[8];
#pragma unroll
            for (int i = 0; i < 4; ++i) {
                x[i] = re[i] * t2c[i] + im[i] * t2s[i];
                x[4 + i] = im[i] * t2c[i] - re[i] * t2s[i];
            }
            u32x4 d = pk8(x);
            re = mfma16(f2re, d, zero); im = mfma16(f2im, d, zero);
#pragma unroll
            for (int i = 0; i < 4; ++i) {
                const float a_ = (i & 1) ? bfhi(wre[c][i >> 1]) : bflo(wre[c][i >> 1]);
                const float b_ = (i & 1) ? bfhi(wim[c][i >> 1]) : bflo(wim[c][i >> 1]);
                x[i] = re[i] * a_ - im[i] * b_;
                x[4 + i] = re[i] * b_ + im[i] * a_;
            }
            d = pk8(x);
            re = mfma16(d, ire, zero); im = mfma16(d, iim, zero);
#pragma unroll
            for (int i = 0; i < 4; ++i) {
                x[i] = re[i] * t2c[i] - im[i] * t2s[i];
                x[4 + i] = im[i] * t2c[i] + re[i] * t2s[i];
            }
            d = pk8(x);
            re = mfma16(d, ire, zero); im = mfma16(d, iim, zero);
            ore[c] = u32x2{pk(re[0], re[1]), pk(re[2], re[3])};
            oim[c] = u32x2{pk(im[0], im[1]), pk(im[2], im[3])};
        }
#pragma unroll
        for (int c = 0; c < 4; ++c) {
            const int col = w + 8 * c, m0 = col * 2, sw = (m0 >> 3) & 7;
            const int ooff = (((2 * i16 + (g >> 1)) ^ sw) * 16) + (g & 1) * 8;
            if (c < 3 || w < 4) {
                *(u32x2*)(smem + L_C + m0 * 512 + ooff) = ore[c];
                *(u32x2*)(smem + L_C + (m0 + 1) * 512 + ooff) = oim[c];
            }
        }
    }
    drain();
    STAMP();
    __syncthreads();
    if (t == 0) raise_flag(P.ws, F_Y, ai, tag);
    {
        const int q = i16 >> 2, p = i16 & 3;
        const int G = 2 * w + (g >> 1), pl = g & 1;
        u32x2 v[7];
#pragma unroll
        for (int it = 0; it < 7; ++it) {
            const int m = (4 * it + q) * 2 + pl;
            v[it] = trread(smem + L_C + m * 512 + (((2 * G + (p >> 1)) ^ ((m >> 3) & 7)) * 16) + (p & 1) * 8);
        }
#pragma unroll
        for (int it = 0; it < 7; ++it) {
            const int o = pl * 56 + it * 8;
            *(u32x2*)(smem + L_W + (16 * G + i16) * 128 + (((o >> 4) ^ (i16 & 7)) << 4) + (o & 8)) = v[it];
        }
    }
    __syncthreads();
#pragma unroll
    for (int i = 0; i < 4; ++i) {
        const int m = t + 512 * i;
        st16sc(rws, OFF_Y1 + ai * Y1_BLK + m * 16, sane(*(const u32x4*)(smem + L_W + (m >> 3) * 128 + (((m & 7) ^ ((m >> 3) & 7)) << 4))));
    }
    STAMP();

    u32x4 C[2], S[2], nsS[2], sC[2], F[2];
    float twc[8], tws[8];
    {
        const u32 sgn = (g >= 2) ? 0x80008000u : 0u;
#pragma unroll
        for (int tau = 0; tau < 2; ++tau) {
            float c[8], s[8];
            const float d1 = (float)(16 * tau + i16);
#pragma unroll
            for (int j = 0; j < 8; ++j) {
                const float a = (float)(8 * g + j) * d1 * 0.03125f;
                c[j] = cosr(a);
                s[j] = sinr(a);
            }
            C[tau] = pk8(c); S[tau] = pk8(s);
            nsS[tau] = S[tau] ^ (sgn ^ 0x80008000u);
            sC[tau] = C[tau] ^ sgn;
#pragma unroll
            for (int i = 0; i < 4; ++i) {
                const float a = d1 * (float)(4 * g + i) * (1.0f / 640.0f);
                twc[4 * tau + i] = cosr(a) * (1.0f / (2048.0f * 640.0f));
                tws[4 * tau + i] = sinr(a) * (1.0f / (2048.0f * 640.0f));
            }
        }
#pragma unroll
        for (int dl = 0; dl < 2; ++dl) {
            float v[8];
#pragma unroll
            for (int j = 0; j < 8; ++j) {
                const int k2 = 4 * g + (j & 3), d2 = 16 * dl + i16;
                const float wgt = (k2 > 10 || d2 >= 20) ? 0.f : ((k2 == 0 || k2 == 10) ? 1.f : 2.f);
                const float a = (float)k2 * (float)d2 * 0.05f;
                v[j] = (j < 4) ? wgt * cosr(a) : -wgt * sinr(a);
            }
            F[dl] = pk8(v);
        }
    }
    u32x2 fa[3];
    {
        const int a = i16 >> 1, plo = i16 & 1;
#pragma unroll
        for (int j = 0; j < 3; ++j) {
            float f[4];
#pragma unroll
            for (int e = 0; e < 4; ++e) {
                const int kk = 2 * g + (e >> 1), pp = e & 1;
                const float v = cosr((float)kk * (float)(256 * a + ai + 96 * j) * (1.0f / 2048.0f) - ((pp != plo) ? 0.25f : 0.f));
                f[e] = (plo == 0 && pp == 1) ? -v : v;
            }
            fa[j] = u32x2{pk(f[0], f[1]), pk(f[2], f[3])};
        }
    }
    drain();
    if (w == 7) {
        wait_flags<96>(rws, F_Y, tag, l);
        if (l == 0) *word = 2u;
    }
    STAMP();
    {
        u32 off[4];
#pragma unroll
        for (int i = 0; i < 4; ++i) {
            const int m = min(t + 512 * i, nset * 672 - 1), j = (m >= 672) + (m >= 1344), mm = m - 672 * j, ab = mm / 7;
            off[i] = OFF_Y1 + ab * Y1_BLK + (ai + 96 * j) * 128 + (mm - 7 * ab) * 16;
        }
        for (u32 spins = 0; spins < SPIN_MAX && *word != 2u; ++spins) __builtin_amdgcn_s_sleep(1);
        u32x4 v[4];
        for (u32 spins = 0; spins < SPIN_MAX; ++spins) {
            fence();
#pragma unroll
            for (int i = 0; i < 4; ++i) v[i] = ld16(rws, off[i], 1);
            if (__all(fresh4(v[0]) && fresh4(v[1]) && fresh4(v[2]) && fresh4(v[3]))) break;
            __builtin_amdgcn_s_sleep(1);
        }
#pragma unroll
        for (int i = 0; i < 4; ++i) *(u32x4*)(smem + L_X + min(t + 512 * i, nset * 672 - 1) * 16) = v[i];
    }
    STAMP();
    __syncthreads();
    {
        const int q = i16 >> 2, p = i16 & 3;
        const int kap = 2 * g + (q >> 1), pl = q & 1;
        u32x2 bfr[3][3];
#pragma unroll
        for (int it = 0; it < 3; ++it) {
            const int T = min(w + 8 * it, 20);
            const int k = min(16 * T + 4 * p, 320);
            const int cgk = min((k * 2341) >> 16, 11), idx = k - min(28 * cgk, 296);
            const int src = L_X + (kap * 12 + cgk) * 112 + pl * 56 + idx * 2;
#pragma unroll
            for (int j = 0; j < 3; ++j) bfr[it][j] = trread(smem + src + j * 10752);
        }
        u32x2 dv[3][3];
#pragma unroll
        for (int it = 0; it < 3; ++it)
#pragma unroll
            for (int j = 0; j < 3; ++j) {
                const f32x4 d = mfma16k16(fa[j], bfr[it][j], f32x4{0.f, 0.f, 0.f, 0.f});
                dv[it][j] = u32x2{pk(d[0], d[1]), pk(d[2], d[3])};
            }
#pragma unroll
        for (int it = 0; it < 3; ++it)
#pragma unroll
            for (int j = 0; j < 3; ++j)
                *(u32x2*)(smem + L_S + j * S_SZ + (16 * min(w + 8 * it, 20) + i16) * S_K + g * 8) = dv[it][j];
    }
    __syncthreads();
    STAMP();
    {
        const int k2 = i16 <= 10 ? i16 : 10;
        const int k0 = k2 + 160 * g;
        const int kofs = ((g < 2) ? k0 : 640 - k0) * S_K + w * 4, kstep = (g < 2) ? 20 * S_K : -20 * S_K;
        const rsrc_t ro = mkrs(P.out, 2048 * 2560);
        u32 dd[3][8];
#pragma unroll
        for (int j = 0; j < 3; ++j)
#pragma unroll
            for (int e = 0; e < 8; ++e) dd[j][e] = *(const u32*)(smem + L_S + j * S_SZ + kofs + e * kstep);
#pragma unroll
        for (int j = 0; j < 3; ++j) {
            {
                const u32x4 are = {permlo(dd[j][0], dd[j][1]), permlo(dd[j][2], dd[j][3]), permlo(dd[j][4], dd[j][5]), permlo(dd[j][6], dd[j][7])};
                const u32x4 aim = {permhi(dd[j][0], dd[j][1]), permhi(dd[j][2], dd[j][3]), permhi(dd[j][4], dd[j][5]), permhi(dd[j][6], dd[j][7])};
                u32x4 va[2];
                const f32x4 zero = {0.f, 0.f, 0.f, 0.f};
#pragma unroll
                for (int tau = 0; tau < 2; ++tau) {
                    const f32x4 vre = mfma16(aim, nsS[tau], mfma16(are, C[tau], zero));
                    const f32x4 vim = mfma16(aim, sC[tau], mfma16(are, S[tau], zero));
                    float pr[8];
#pragma unroll
                    for (int i = 0; i < 4; ++i) {
                        pr[i] = vre[i] * twc[4 * tau + i] - vim[i] * tws[4 * tau + i];
                        pr[4 + i] = vim[i] * twc[4 * tau + i] + vre[i] * tws[4 * tau + i];
                    }
                    va[tau] = pk8(pr);
                }
                const u32 orow = (ai + 96 * j + 256 * w) * 2560;
#pragma unroll
                for (int dl = 0; dl < 2; ++dl) {
                    const f32x4 y0 = mfma16(va[0], F[dl], zero);
                    const f32x4 y1 = mfma16(va[1], F[dl], zero);
                    u32 a0 = pk(y0[0], y0[1]), a1 = pk(y0[2], y0[3]);
                    u32 b0 = pk(y1[0], y1[1]), b1 = pk(y1[2], y1[3]);
                    auto s0 = __builtin_amdgcn_permlane16_swap(a0, b0, false, false);
                    auto s1 = __builtin_amdgcn_permlane16_swap(a1, b1, false, false);
                    const int d2 = 16 * dl + i16;
                    const u32 off = (d2 < 20 && j < nset) ? orow + (32 * d2 + (g & 1) * 16 + (g >> 1) * 8) * 2 : 0x80000000u;
                    __builtin_amdgcn_raw_buffer_store_b128(u32x4{s0[0], s1[0], s0[1], s1[1]}, ro, off, 0, 0);
                }
            }
        }
    }
    STAMP();
    STAMP_OUT(P.ws);
}

DI void role_b(const Args& P, char* smem, const int t, const int w, const int chb) {
    const int l = t & 63, g = l >> 4, i16 = l & 15;
    const int ch = w & 3, rh = w >> 2;
    STAMP_DECL STAMP();
    Start st;
    p1_start(P, st, t, w);
    u32 pv[12], ldt = 0, pu = 0;
    const rsrc_t ru = mkrs(P.u, 2048 * 1280);
    if (w < 4) {
        const u32 nb = 640 * 128 * 2;
        const rsrc_t r0 = mkrs(P.Are, nb), r1 = mkrs(P.Aim, nb), r2 = mkrs(P.Bre, nb), r3 = mkrs(P.Bim, nb), r4 = mkrs(P.Cre, nb), r5 = mkrs(P.Cim, nb);
#pragma unroll
        for (int h = 0; h < 2; ++h) {
            const u32 off = ((4 * chb + w) * 128 + 64 * h + l) * 2;
            pv[6 * h + 0] = ld2(r0, off); pv[6 * h + 1] = ld2(r1, off); pv[6 * h + 2] = ld2(r2, off);
            pv[6 * h + 3] = ld2(r3, off); pv[6 * h + 4] = ld2(r4, off); pv[6 * h + 5] = ld2(r5, off);
        }
        ldt = ld2(mkrs(P.logdt, 1280), (4 * chb + w) * 2);
    }
    const u32 dks = ld2(mkrs(P.dskip, 1280), (4 * chb + ch) * 2);
    if (w < 5) {
        const int xcd = blockIdx.x & 7, jx = blockIdx.x >> 3;
        const int b0 = (160 * xcd) & ~127, nl = (160 * xcd + 160 - b0 + 127) >> 7;
        const int m = min(t, 103 * 3 - 1), r = m / 3;
        pu = __builtin_amdgcn_raw_buffer_load_b32(ru, min(103 * jx + r, 2047) * 1280 + b0 + min(m - 3 * r, nl - 1) * 128, 0, 0);
    }
    fence();
    const u32x2 fb = p1_rows(P, smem, st, t, w);
    p1_finish<true>(P, smem, fb, t, w);
    STAMP();
    if (w >= 4) {
        const int tt = t - 256;
        for (u32 spins = 0; spins < SPIN_MAX && *(volatile LAS u32*)(smem + L_WORD + 4) < 4u; ++spins) __builtin_amdgcn_s_sleep(2);
        fence();
        u32x2 gv[8];
#pragma unroll
        for (int e = 0; e < 8; ++e) gv[e] = ld8(ru, (8 * tt + e) * 1280 + chb * 8, 0);
        fence();
        if (tt < 128) *(u32x4*)(smem + L_U + (tt >> 5) * PB_U + (tt & 31) * 16) = u32x4{0u, 0u, 0u, 0u};
        else if (tt < 144) {
            const int v = tt - 128;
            *(u32x4*)(smem + L_K + (v >> 2) * PB_KS + ((v >> 1) & 1) * PB_K1 + 4096 + (v & 1) * 16) = u32x4{0u, 0u, 0u, 0u};
        }
        u32x4 o[4];
        tr8x4(gv, o);
#pragma unroll
        for (int c = 0; c < 4; ++c) *(u32x4*)(smem + L_U + c * PB_U + 512 + tt * 16) = o[c];
    } else {
        char* sc = smem + w * KG_SZ;
        const float dt = rbf(expf(bf2f(ldt)));
        f32x4 kacc[3];
#pragma unroll
        for (int T = 0; T < 3; ++T) kacc[T] = f32x4{0.f, 0.f, 0.f, 0.f};
#pragma unroll 1
        for (int h = 0; h < 2; ++h) {
            const float lre = dt * bf2f(h ? pv[6] : pv[0]), lim = dt * bf2f(h ? pv[7] : pv[1]) * 0.15915494309189535f;
            const cf c = cmul(cf{bf2f(h ? pv[10] : pv[4]), bf2f(h ? pv[11] : pv[5])}, cf{dt * bf2f(h ? pv[8] : pv[2]), dt * bf2f(h ? pv[9] : pv[3])});
            char* rowp = sc + l * 4;
            const cf z1 = cexpk(lre, lim, 1.f), z8 = cexpk(lre, lim, 8.f), z128 = cexpk(lre, lim, 128.f);
            const cf z2 = cmul(z1, z1), z4 = cmul(z2, z2), z6 = cmul(z2, z4);
            const cf z8c = {z8.x, -z8.y};
            cf v = {1.f, 0.f}, r0 = c, r1 = cmul(c, z128);
            cf q = cmul(r1, z128);
#pragma unroll
            for (int j = 0; j < 8; ++j) {
                *(u32*)(rowp + KG_B + (2 * j) * KG_RS) = pk(v.x, v.y);
                v = cmul(v, z8c);
                *(u32*)(rowp + KG_B + (2 * j + 1) * KG_RS) = pk(v.x, v.y);
                v = cmul(v, z8c);
                *(u32*)(rowp + j * KG_RS) = pkc(r0);
                *(u32*)(rowp + (8 + j) * KG_RS) = pkc(r1);
                r0 = cmul(r0, z1);
                r1 = cmul(r1, z1);
#pragma unroll
                for (int e = 0; e < 2; ++e) {
                    const int hh = 2 + 2 * j + e;
                    if (hh < 4) {
                        const int r = 16 + (hh - 2) * 4;
                        *(u32*)(rowp + r * KG_RS) = pkc(q);
                        *(u32*)(rowp + (r + 1) * KG_RS) = pkc(cmul(q, z2));
                        *(u32*)(rowp + (r + 2) * KG_RS) = pkc(cmul(q, z4));
                        *(u32*)(rowp + (r + 3) * KG_RS) = pkc(cmul(q, z6));
                    } else if (hh < 8) {
                        const int r = 24 + (hh - 4) * 2;
                        *(u32*)(rowp + r * KG_RS) = pkc(q);
                        *(u32*)(rowp + (r + 1) * KG_RS) = pkc(cmul(q, z4));
                    } else if (hh <= 16) {
                        *(u32*)(rowp + (32 + hh - 8) * KG_RS) = pkc(q);
                    }
                    if (hh < 16) q = cmul(q, z128);
                }
            }
            u32x4 bf[4], af[3][4];
#pragma unroll
            for (int ks = 0; ks < 4; ++ks) {
                bf[ks] = *(const u32x4*)(sc + KG_B + i16 * KG_RS + g * 16 + ks * 64);
#pragma unroll
                for (int T = 0; T < 3; ++T) af[T][ks] = *(const u32x4*)(sc + (16 * T + i16) * KG_RS + g * 16 + ks * 64);
            }
#pragma unroll
            for (int T = 0; T < 3; ++T)
#pragma unroll
                for (int ks = 0; ks < 4; ++ks) kacc[T] = mfma16(af[T][ks], bf[ks], kacc[T]);
        }
        char* tbl = smem + L_T + w * KG_T;
#pragma unroll
        for (int T = 0; T < 3; ++T)
#pragma unroll
            for (int i = 0; i < 4; ++i) {
                const int row = 16 * T + 4 * g + i;
                int e;
                if (T == 0) e = 128 * (row >> 3) + (row & 7);
                else if (T == 1) e = (g < 2) ? 256 + 128 * g + 2 * i : 512 + 256 * (g - 2) + 128 * (i >> 1) + 4 * (i & 1);
                else e = 1024 + 128 * (row - 32);
                const bool ok = row < 40 || (row == 40 && i16 == 0);
                *(u16*)(tbl + (ok ? (e + 8 * i16) * 2 : 4110)) = (u16)(pk(kacc[T][i], kacc[T][i]) & 0xffffu);
            }
        u32 v[4][8], vm[4];
#pragma unroll
        for (int it = 0; it < 4; ++it) {
            const int tt = l + 64 * it, a = 255 - tt;
            const u32 code = (a < 32) ? 0x76543210u : (a < 64) ? 0x86444200u : (a < 128) ? 0x88444000u : ((a & 1) ? 0x88880000u : 0x88800000u);
            const int lastoff = (a <= 32) ? 7 : 8;
            const char* tb = tbl + 16 * a;
#pragma unroll
            for (int e = 0; e < 8; ++e) v[it][e] = *(const u16*)(tb + 2 * ((code >> (4 * e)) & 15));
            vm[it] = *(const u16*)(tb + ((a == 0) ? 0 : 2 * lastoff - 16));
            vm[it] = (a == 0) ? 0u : vm[it];
        }
#pragma unroll
        for (int it = 0; it < 4; ++it) {
            char* kb = smem + L_K + w * PB_KS + (l + 64 * it) * 16;
            const u32* x = v[it];
            *(u32x4*)(kb) = u32x4{x[7] | (x[6] << 16), x[5] | (x[4] << 16), x[3] | (x[2] << 16), x[1] | (x[0] << 16)};
            *(u32x4*)(kb + PB_K1) = u32x4{x[6] | (x[5] << 16), x[4] | (x[3] << 16), x[2] | (x[1] << 16), x[0] | (vm[it] << 16)};
        }
    }
    asm volatile("" :: "v"(st.pw ^ pu));
    STAMP();
    __syncthreads();
    STAMP();
    const int mu = g >> 1, hp = g & 1;
    const int x0 = 2047 - 16 * mu - i16 + 8 * hp;
    const char* ap = smem + L_K + ch * PB_KS + ((x0 & 1) ? PB_K1 + (x0 - 1) * 2 : x0 * 2);
    const char* bp = smem + L_U + ch * PB_U + (256 + 16 * (i16 - mu) + 8 * hp) * 2;
    f32x4 acc[8];
#pragma unroll
    for (int c = 0; c < 8; ++c) acc[c] = f32x4{0.f, 0.f, 0.f, 0.f};
    {
        u32x4 A[2][8], B[2][8];
        const char* ap0 = ap - 256 * rh;
        const char* bp0 = bp - 256 * rh;
#pragma unroll
        for (int p = 0; p < 8; ++p) {
            const u32* a = (const u32*)(ap0 - 512 * p);
            B[0][p] = *(const u32x4*)(bp0 + 512 * p);
            A[0][p] = u32x4{a[0], a[1], a[2], a[3]};
        }
#pragma unroll
        for (int r = 0; r < 4; ++r) {
            __builtin_amdgcn_sched_barrier(0);
            if (r < 3) {
#pragma unroll
                for (int p = 0; p < 8; ++p) {
                    const u32* a = (const u32*)(ap0 - 64 * (r + 1) - 512 * p);
                    B[(r + 1) & 1][p] = *(const u32x4*)(bp0 - 64 * (r + 1) + 512 * p);
                    A[(r + 1) & 1][p] = u32x4{a[0], a[1], a[2], a[3]};
                }
            }
#pragma unroll
            for (int p = 0; p < 8; ++p)
#pragma unroll
                for (int q = 0; q + p < 8; ++q) acc[p + q] = mfma16(A[r & 1][p], B[r & 1][q], acc[p + q]);
            if (r < 3) {
#pragma unroll
                for (int k = 0; k < 12; ++k) {
                    __builtin_amdgcn_sched_group_barrier(0x008, 3, 0);
                    __builtin_amdgcn_sched_group_barrier(0x100, 2, 0);
                }
            }
        }
        __builtin_amdgcn_sched_barrier(0);
    }
    STAMP();
    char* rb = smem + L_R;
#pragma unroll
    for (int c = 0; c < 4; ++c) *(f32x4*)(rb + (w * 4 + c) * 1024 + l * 16) = rh ? acc[c] : acc[4 + c];
    __syncthreads();
    char* yi = smem + L_Y;
    const float dsk = bf2f(dks);
    {
        f32x4 o[4];
        u32x2 uu[4];
#pragma unroll
        for (int c = 0; c < 4; ++c) {
            o[c] = *(const f32x4*)(rb + ((w ^ 4) * 4 + c) * 1024 + l * 16);
            uu[c] = *(const u32x2*)(smem + L_U + ch * PB_U + (256 + 256 * (4 * rh + c) + 16 * i16 + 4 * g) * 2);
        }
        u32x2 res[4];
#pragma unroll
        for (int c = 0; c < 4; ++c) {
            const f32x4 mine = rh ? acc[4 + c] : acc[c];
            float v[4];
#pragma unroll
            for (int i = 0; i < 4; ++i) {
                const float uf = (i & 1) ? bfhi(i < 2 ? uu[c].x : uu[c].y) : bflo(i < 2 ? uu[c].x : uu[c].y);
                v[i] = rbf(mine[i] + o[c][i]) + rbf(uf * dsk);
            }
            res[c] = u32x2{pk(v[0], v[1]), pk(v[2], v[3])};
        }
#pragma unroll
        for (int c = 0; c < 4; ++c) *(u32x2*)(yi + ch * PB_Y + (64 * (4 * rh + c) + 4 * i16 + (g ^ ((i16 >> 2) & 3))) * 8) = res[c];
    }
    __syncthreads();
    STAMP();
    {
        const rsrc_t ro = mkrs(P.out, 2048 * 2560);
        const int q = i16 >> 2, p = i16 & 3;
        u32x2 v[4];
#pragma unroll
        for (int i = 0; i < 4; ++i) {
            const int sg = ((256 * w + 64 * i + 16 * g) >> 2) + p;
            v[i] = trread(yi + q * PB_Y + (sg ^ ((sg >> 4) & 3)) * 8);
        }
#pragma unroll
        for (int i = 0; i < 4; ++i)
            __builtin_amdgcn_raw_buffer_store_b64(v[i], ro, (256 * w + 64 * i + 16 * g + i16) * 2560 + 1280 + chb * 8, 0, 0);
    }
    STAMP();
    STAMP_OUT(P.ws);
}

__global__ void __launch_bounds__(512, 2) fused_kernel(Args P) {
    extern __shared__ __attribute__((aligned(16))) char smem[];
    const int t = threadIdx.x;
    const int w = __builtin_amdgcn_readfirstlane(t >> 6);
    const int b = blockIdx.x, xcd = b & 7, jx = b >> 3;
    if (jx < 20) role_b(P, smem, t, w, 20 * xcd + jx);
    else role_a(P, smem, t, w, xcd, jx - 20);
}

extern "C" void kernel_launch(void* const* d_in, const int* in_sizes, int n_in,
                              void* d_out, int out_size, void* d_ws, size_t ws_size,
                              hipStream_t stream) {
    Args a;
    a.x = (const u16*)d_in[0];
    a.wr = (const u16*)d_in[1];
    a.wi = (const u16*)d_in[2];
    a.u = (const u16*)d_in[3];
    a.Are = (const u16*)d_in[4];
    a.Aim = (const u16*)d_in[5];
    a.Bre = (const u16*)d_in[6];
    a.Bim = (const u16*)d_in[7];
    a.Cre = (const u16*)d_in[8];
    a.Cim = (const u16*)d_in[9];
    a.logdt = (const u16*)d_in[10];
    a.dskip = (const u16*)d_in[11];
    a.ws = (char*)d_ws;
    a.out = (u16*)d_out;
    (void)hipFuncSetAttribute((const void*)fused_kernel, hipFuncAttributeMaxDynamicSharedMemorySize, LDS_BYTES);
    fused_kernel<<<256, 512, LDS_BYTES, stream>>>(a);
}
```

```cpp
#include <hip/hip_runtime.h>
#include <hip/hip_bf16.h>

typedef unsigned short u16;
typedef unsigned int u32;
typedef __attribute__((ext_vector_type(2))) __bf16 bf16x2;
typedef __attribute__((ext_vector_type(8))) __bf16 bf16x8;
typedef __attribute__((ext_vector_type(2))) float f32x2;
typedef __attribute__((ext_vector_type(4))) float f32x4;
typedef __attribute__((ext_vector_type(4))) u32 u32x4;
typedef __attribute__((ext_vector_type(2))) u32 u32x2;
typedef __attribute__((ext_vector_type(4))) short s16x4;
typedef __amdgpu_buffer_rsrc_t rsrc_t;
#define LAS __attribute__((address_space(3)))
#define GAS __attribute__((address_space(1)))
#define DI __device__ __forceinline__
#define SC1 16

constexpr u32 X1_ROW = 12288;
constexpr u32 OFF_X1 = 0;
constexpr u32 Y1_BLK = 32768;
constexpr u32 OFF_Y1 = 256 * X1_ROW;
constexpr u32 OFF_SYNC = OFF_Y1 + 96 * Y1_BLK;
constexpr u32 F_X = 1024;
constexpr u32 F_Y = F_X + 1024;
constexpr u32 WS_USED = OFF_SYNC + F_Y + 1024;
constexpr u32 SENT = 0xFFFFFFFFu;
constexpr u32 SPIN_MAX = 1u << 16;

constexpr int S_K = 40, S_SZ = 336 * S_K;
constexpr int P_X = 80448;
constexpr int P_S = P_X + 12800;
constexpr int P_C = 32768;
constexpr int L_W = 0;
constexpr int L_C = L_W + 32768;
constexpr int L_DUMP = L_C + 28672;
constexpr int L_X = L_DUMP + 1024;
constexpr int L_S = L_X + 3 * 10752;
constexpr int L_P3C = L_S + 3 * S_SZ;
constexpr int KG_RS = 272, KG_B = 41 * KG_RS, KG_SZ = 57 * KG_RS;
constexpr int PB_U = 4608, PB_K1 = 4160, PB_KS = 8320, PB_Y = 4128, KG_T = 4112;
constexpr int L_U = 4 * KG_SZ;
constexpr int L_K = P_S + S_SZ;
constexpr int L_T = L_K + 4 * PB_KS;
constexpr int L_R = 0;
constexpr int L_Y = L_R + 32768;
constexpr int L_WORD = L_T + 4 * KG_T;
constexpr int LDS_BYTES = L_WORD + 16;
static_assert(L_P3C + 11776 <= L_WORD && L_U + 4 * PB_U <= P_X && L_Y + 4 * PB_Y <= L_U && LDS_BYTES <= 163840, "LDS map");

#define STAMP_DECL
#define STAMP()
#define STAMP_OUT(ws)

DI u32 pk(float a, float b) {
    f32x2 v = {a, b};
    return __builtin_bit_cast(u32, __builtin_convertvector(v, bf16x2));
}
DI u32x4 pk8(const float* v) { return u32x4{pk(v[0], v[1]), pk(v[2], v[3]), pk(v[4], v[5]), pk(v[6], v[7])}; }
DI float bflo(u32 v) { return __uint_as_float(v << 16); }
DI float bfhi(u32 v) { return __uint_as_float(v & 0xffff0000u); }
DI float bf2f(u32 v) { return __uint_as_float(v << 16); }
DI float rbf(float a) { return bflo(pk(a, a)); }
DI float cosr(float rev) { return __builtin_amdgcn_cosf(rev); }
DI float sinr(float rev) { return __builtin_amdgcn_sinf(rev); }
DI f32x4 mfma16(u32x4 a, u32x4 b, f32x4 c) {
    return __builtin_amdgcn_mfma_f32_16x16x32_bf16(__builtin_bit_cast(bf16x8, a), __builtin_bit_cast(bf16x8, b), c, 0, 0, 0);
}
DI f32x4 mfma16k16(u32x2 a, u32x2 b, f32x4 c) {
    return __builtin_amdgcn_mfma_f32_16x16x16bf16_1k(__builtin_bit_cast(s16x4, a), __builtin_bit_cast(s16x4, b), c, 0, 0, 0);
}
DI u32x2 trread(const char* p) {
    return __builtin_bit_cast(u32x2, __builtin_amdgcn_ds_read_tr16_b64_v4i16((LAS s16x4*)(p)));
}
DI u32 permlo(u32 a, u32 b) { return __builtin_amdgcn_perm(b, a, 0x05040100u); }
DI u32 permhi(u32 a, u32 b) { return __builtin_amdgcn_perm(b, a, 0x07060302u); }
DI rsrc_t mkrs(const void* p, u32 bytes) { return __builtin_amdgcn_make_buffer_rsrc((void*)p, 0, bytes, 0x00020000); }
DI u32x4 ld16(rsrc_t r, u32 off, int aux) { return aux ? __builtin_amdgcn_raw_buffer_load_b128(r, off, 0, SC1) : __builtin_amdgcn_raw_buffer_load_b128(r, off, 0, 0); }
DI u32x2 ld8(rsrc_t r, u32 off, int aux) { return aux ? __builtin_amdgcn_raw_buffer_load_b64(r, off, 0, SC1) : __builtin_amdgcn_raw_buffer_load_b64(r, off, 0, 0); }
DI u32 ld2(rsrc_t r, u32 off) { return __builtin_amdgcn_raw_buffer_load_b16(r, off, 0, 0); }
DI void st16sc(rsrc_t r, u32 off, u32x4 v) { __builtin_amdgcn_raw_buffer_store_b128(v, r, off, 0, SC1); }
DI u32x4 sane(u32x4 v) { return u32x4{min(v.x, SENT - 1), min(v.y, SENT - 1), min(v.z, SENT - 1), min(v.w, SENT - 1)}; }
DI bool fresh4(u32x4 v) { return max(max(v.x, v.y), max(v.z, v.w)) != SENT; }
DI void fence() { asm volatile("" ::: "memory"); }
DI void drain() { asm volatile("s_waitcnt vmcnt(0)" ::: "memory"); }
DI void tr8x4(const u32x2* v, u32x4* o) {
    o[0] = u32x4{permlo(v[0].x, v[1].x), permlo(v[2].x, v[3].x), permlo(v[4].x, v[5].x), permlo(v[6].x, v[7].x)};
    o[1] = u32x4{permhi(v[0].x, v[1].x), permhi(v[2].x, v[3].x), permhi(v[4].x, v[5].x), permhi(v[6].x, v[7].x)};
    o[2] = u32x4{permlo(v[0].y, v[1].y), permlo(v[2].y, v[3].y), permlo(v[4].y, v[5].y), permlo(v[6].y, v[7].y)};
    o[3] = u32x4{permhi(v[0].y, v[1].y), permhi(v[2].y, v[3].y), permhi(v[4].y, v[5].y), permhi(v[6].y, v[7].y)};
}
DI void raise_flag(char* ws, u32 set, int i, u32 tag) {
    __hip_atomic_store((GAS u32*)(ws + OFF_SYNC + set) + i, tag, __ATOMIC_RELAXED, __HIP_MEMORY_SCOPE_AGENT);
}
template <int NF>
DI void wait_flags(rsrc_t rws, u32 set, u32 tag, int l) {
    for (u32 spins = 0; spins < SPIN_MAX; ++spins) {
        fence();
        const u32x4 f = ld16(rws, OFF_SYNC + set + min(l, NF / 4 - 1) * 16, 1);
        if (__all(f.x == tag && f.y == tag && f.z == tag && f.w == tag)) break;
        __builtin_amdgcn_s_sleep(4);
    }
}

template <int N> struct ic { static constexpr int value = N; };
typedef f32x2 cf;
DI cf cmul(cf a, cf b) { return __builtin_elementwise_fma(cf{a.y, a.y}, cf{-b.y, b.x}, cf{a.x, a.x} * b); }
DI cf cexpk(float lre, float lim_rev, float k) {
    const float m = __builtin_amdgcn_exp2f(k * lre * 1.44269504088896341f);
    const float a = __builtin_amdgcn_fractf(k * lim_rev);
    return cf{m * cosr(a), m * sinr(a)};
}
DI u32 pkc(cf v) { return pk(v.x, v.y); }

struct Args {
    const u16 *x, *wr, *wi, *u, *Are, *Aim, *Bre, *Bim, *Cre, *Cim, *logdt, *dskip;
    char* ws;
    u16* out;
};

struct Start { u32x4 xv[2]; u32 gen; };
DI void p1_start(const Args& P, Start& st, const int t, const int w) {
    const int l = t & 63, b = blockIdx.x;
    const rsrc_t rws = mkrs(P.ws, WS_USED);
    st.gen = __builtin_amdgcn_raw_buffer_load_b32(rws, OFF_SYNC, 0, SC1);
    fence();
    const rsrc_t rx = mkrs(P.x, 2048 * 1280);
    const u32 ro = (b + 256 * w) * 1280;
    st.xv[0] = ld16(rx, ro + l * 16, 0);
    st.xv[1] = ld16(rx, ro + min(64 + l, 79) * 16, 0);
    fence();
#pragma unroll
    for (int i = 0; i < 2; ++i) st16sc(rws, OFF_X1 + b * X1_ROW + min(t + 512 * i, 767) * 16, u32x4{SENT, SENT, SENT, SENT});
    fence();
}
DI u32 warm_w(const Args& P, const int t, const int w) {
    const int l = t & 63, b = blockIdx.x;
    u32 pw = 0;
    if (w >= 6) {
        const int m = min(l, 47);
        pw = __builtin_amdgcn_raw_buffer_load_b32(mkrs(w == 6 ? P.wr : P.wi, 2048 * 642),
                                                  ((((b & 7) + 8 * (8 * (b >> 3) + m / 6)) * 642) & ~127u) + (m % 6) * 128, 0, 0);
    }
    return pw;
}
DI void p1_consts(char* smem, const int t, const int w) {
    const int l = t & 63, g = l >> 4, i16 = l & 15, b = blockIdx.x;
    if (t < 15) *(u32x2*)(smem + P_S + 321 * S_K + t * 8) = u32x2{0u, 0u};
    {
        float v[8];
        if (w < 4) {
#pragma unroll
            for (int j = 0; j < 8; ++j) {
                const int d = (j < 4) ? 4 * g + j : 16 + 4 * g + j - 4;
                const float c = cosr((float)d * (float)i16 * (w < 2 ? 0.05f : 0.03125f) - ((w & 1) ? 0.25f : 0.f));
                v[j] = (w >= 2) ? c : ((d < 20 && i16 <= 10) ? ((w & 1) ? -c : c) : 0.f);
            }
            *(u32x4*)(smem + P_C + w * 1024 + l * 16) = pk8(v);
        } else {
#pragma unroll
            for (int i = 0; i < 4; ++i)
                v[i] = cosr((float)(16 * (w & 1) + 4 * g + i) * (float)i16 * (1.0f / 640.0f) - ((w >= 6) ? 0.25f : 0.f));
            *(f32x4*)(smem + P_C + w * 1024 + l * 16) = f32x4{v[0], v[1], v[2], v[3]};
            if (w == 4) {
                const int kap = i16 >> 1, plo = i16 & 1;
#pragma unroll
                for (int e = 0; e < 4; ++e) {
                    const int a = 2 * g + (e >> 1), pl = e & 1;
                    const float c = cosr((float)kap * (float)(256 * a + b) * (1.0f / 2048.0f) - ((pl != plo) ? 0.25f : 0.f));
                    v[e] = (plo == 1 && pl == 0) ? -c : c;
                }
                *(u32x2*)(smem + P_C + 8192 + l * 8) = u32x2{pk(v[0], v[1]), pk(v[2], v[3])};
            }
        }
    }
    if (t == 0) *(u32x4*)(smem + L_WORD) = u32x4{0u, 0u, 0u, 0u};
    __syncthreads();
}
DI u32x2 p1_rows(const Args& P, char* smem, const Start& st, const int t, const int w) {
    const int l = t & 63, g = l >> 4, i16 = l & 15, b = blockIdx.x;
    const u32x4* xv = st.xv;
    const u32x4 f20c = *(const u32x4*)(smem + P_C + l * 16), f20s = *(const u32x4*)(smem + P_C + 1024 + l * 16);
    const u32x4 c32 = *(const u32x4*)(smem + P_C + 2048 + l * 16), s32 = *(const u32x4*)(smem + P_C + 3072 + l * 16);
    float twc[8], tws[8];
#pragma unroll
    for (int i = 0; i < 2; ++i) {
        const f32x4 a = *(const f32x4*)(smem + P_C + (4 + i) * 1024 + l * 16), bq = *(const f32x4*)(smem + P_C + (6 + i) * 1024 + l * 16);
#pragma unroll
        for (int e = 0; e < 4; ++e) { twc[4 * i + e] = a[e]; tws[4 * i + e] = bq[e]; }
    }
    const u32x2 fb = *(const u32x2*)(smem + P_C + 8192 + l * 8);
    const u32x4 ns32 = s32 ^ 0x80008000u, c32b = c32 ^ 0x80000000u, s32b = s32 ^ 0x80000000u, nc32b = c32b ^ 0x80008000u;
    {
        const int q = i16 >> 2, p = i16 & 3;
        const int d2a = 4 * g + q, d2b = (g == 0) ? 16 + q : d2a;
        const int offa = 64 * d2a + 8 * p, swa = ((d2a >> 2) & 1) << 5;
        const int offb = 64 * d2b + 8 * p, swb = ((d2b >> 2) & 1) << 5;
        char* xr = smem + P_X + w * 1280;
        *(u32x4*)(xr + ((l * 16) ^ (((l >> 4) & 1) << 5))) = xv[0];
        *(u32x4*)(xr + min(64 + l, 79) * 16) = xv[1];
        f32x4 tre[2], tim[2];
        const f32x4 zero = {0.f, 0.f, 0.f, 0.f};
        u32x4 xa[2];
#pragma unroll
        for (int tau = 0; tau < 2; ++tau) {
            const u32x2 a0 = trread(xr + offa + ((32 * tau) ^ swa));
            const u32x2 a1 = trread(xr + offb + ((32 * tau) ^ swb));
            xa[tau] = u32x4{a0.x, a0.y, a1.x, a1.y};
        }
#pragma unroll
        for (int tau = 0; tau < 2; ++tau) {
            tre[tau] = mfma16(xa[tau], f20c, zero);
            tim[tau] = mfma16(xa[tau], f20s, zero);
        }
        float pr[8], pi[8];
#pragma unroll
        for (int e = 0; e < 8; ++e) {
            const float a = tre[e >> 2][e & 3], bb = tim[e >> 2][e & 3];
            pr[e] = a * twc[e] + bb * tws[e];
            pi[e] = bb * twc[e] - a * tws[e];
        }
        const u32x4 bre = pk8(pr), bim = pk8(pi);
        const f32x4 z0 = mfma16(s32, bim, mfma16(c32, bre, zero));
        const f32x4 z1 = mfma16(ns32, bre, mfma16(c32, bim, zero));
        const f32x4 z2 = mfma16(s32b, bim, mfma16(c32b, bre, zero));
        const f32x4 z3 = mfma16(s32b, bre, mfma16(nc32b, bim, zero));
        const bool okA = i16 <= 10, okB = (i16 >= 1 && i16 <= 9), okB0 = okB || (i16 == 0 && g == 0);
        const int kA = i16 + 80 * g, kB = 320 - i16 - 80 * g;
        const int oA0 = (okA ? kA : 335) * S_K, oAs = okA ? 20 * S_K : 0;
        const int oB0 = (okB0 ? kB : 335) * S_K, oB1 = (okB ? kB - 20 : 335) * S_K, oBs = okB ? 20 * S_K : 0;
        char* st = smem + P_S + w * 4;
#pragma unroll
        for (int i = 0; i < 4; ++i) *(u32*)(st + oA0 + i * oAs) = pk(z0[i], z1[i]);
        *(u32*)(st + oB0) = pk(z2[0], z3[0]);
#pragma unroll
        for (int i = 1; i < 4; ++i) *(u32*)(st + oB1 - (i - 1) * oBs) = pk(z2[i], z3[i]);
    }
    drain();
    __syncthreads();
    if (t == 0) raise_flag(P.ws, F_X, b, st.gen + 1u);
    return fb;
}
DI void p1_finish(const Args& P, char* smem, const u32x2 fb, const int t, const int w) {
    const int l = t & 63, g = l >> 4, i16 = l & 15, b = blockIdx.x;
    {
        u32x2 av[3], dv[3];
#pragma unroll
        for (int it = 0; it < 3; ++it)
            av[it] = *(const u32x2*)(smem + P_S + (16 * min(w + 8 * it, 20) + i16) * S_K + g * 8);
#pragma unroll
        for (int it = 0; it < 3; ++it) {
            const f32x4 d = mfma16k16(av[it], fb, f32x4{0.f, 0.f, 0.f, 0.f});
            dv[it] = u32x2{pk(d[0], d[1]), pk(d[2], d[3])};
        }
        const int kap = i16 >> 1, po = (i16 & 1) * 56;
#pragma unroll
        for (int it = 0; it < 3; ++it) {
            const int k = 16 * min(w + 8 * it, 20) + 4 * g;
            const int cgk = min((k * 2341) >> 16, 11);
            const int o0 = po + (k - min(28 * cgk, 296)) * 2, o1 = po + (k - 296) * 2;
            const int d0 = (k < 324) ? (kap * 12 + cgk) * 128 + (((o0 >> 4) ^ kap) << 4) + (o0 & 8) : 12288 + l * 8;
            const int d1 = (k >= 296 && k < 308) ? (kap * 12 + 11) * 128 + (((o1 >> 4) ^ kap) << 4) + (o1 & 8) : d0;
            *(u32x2*)(smem + P_X + d0) = dv[it];
            *(u32x2*)(smem + P_X + d1) = dv[it];
        }
    }
    __syncthreads();
    const rsrc_t rws = mkrs(P.ws, WS_USED);
    if (w >= 4) {
#pragma unroll
        for (int i = 0; i < 3; ++i) {
            const int m = (t & 255) + 256 * i, pc = m >> 3, kp = (pc * 43) >> 9;
            st16sc(rws, OFF_X1 + b * X1_ROW + m * 16, sane(*(const u32x4*)(smem + P_X + pc * 128 + (((m & 7) ^ kp) << 4))));
        }
    }
}

DI void p3_consts(char* tb, const int w, const int l, const int ai) {
    const int g = l >> 4, i16 = l & 15;
    float v[8];
    if (w < 4) {
        const float d1 = (float)(16 * (w & 1) + i16);
#pragma unroll
        for (int j = 0; j < 8; ++j) v[j] = cosr((float)(8 * g + j) * d1 * 0.03125f - ((w >= 2) ? 0.25f : 0.f));
        *(u32x4*)(tb + w * 1024 + l * 16) = pk8(v);
    } else {
        const int tau = w & 1;
        const float d1 = (float)(16 * tau + i16);
#pragma unroll
        for (int i = 0; i < 4; ++i)
            v[i] = cosr(d1 * (float)(4 * g + i) * (1.0f / 640.0f) - ((w >= 6) ? 0.25f : 0.f)) * (1.0f / (2048.0f * 640.0f));
        *(f32x4*)(tb + w * 1024 + l * 16) = f32x4{v[0], v[1], v[2], v[3]};
        if (w < 6) {
#pragma unroll
            for (int j = 0; j < 8; ++j) {
                const int k2 = 4 * g + (j & 3), d2 = 16 * tau + i16;
                const float wgt = (k2 > 10 || d2 >= 20) ? 0.f : ((k2 == 0 || k2 == 10) ? 1.f : 2.f);
                const float c = cosr((float)k2 * (float)d2 * 0.05f - ((j < 4) ? 0.f : 0.25f));
                v[j] = (j < 4) ? wgt * c : -wgt * c;
            }
            *(u32x4*)(tb + (8 + tau) * 1024 + l * 16) = pk8(v);
        }
    }
    if (w < 3) {
        const int a = i16 >> 1, plo = i16 & 1;
#pragma unroll
        for (int e = 0; e < 4; ++e) {
            const int kk = 2 * g + (e >> 1), pp = e & 1;
            const float c = cosr((float)kk * (float)(256 * a + ai + 96 * w) * (1.0f / 2048.0f) - ((pp != plo) ? 0.25f : 0.f));
            v[e] = (plo == 0 && pp == 1) ? -c : c;
        }
        *(u32x2*)(tb + 10240 + w * 512 + l * 8) = u32x2{pk(v[0], v[1]), pk(v[2], v[3])};
    }
}

DI void role_a(const Args& P, char* smem, const int t, const int w, const int ka, const int cg) {
    const int l = t & 63, g = l >> 4, i16 = l & 15;
    const int ai = ka * 12 + cg, nset = ai < 64 ? 3 : 2;
    const int colbase = min(28 * cg, 296);
    const rsrc_t rws = mkrs(P.ws, WS_USED);
    STAMP_DECL STAMP();
    Start st;
    p1_start(P, st, t, w);
    p1_consts(smem, t, w);
    const u32x2 fb = p1_rows(P, smem, st, t, w);
    const u32 pw = warm_w(P, t, w);
    fence();
    p1_finish(P, smem, fb, t, w);
    const u32 tag = st.gen + 1u;
    volatile LAS u32* word = (volatile LAS u32*)(smem + L_WORD);
    STAMP();
    u32x4 f1re, f1im, f2re, f2im;
    float t2c[4], t2s[4];
    if (w < 4) {
        const int wpl = w >> 1, dl = (ka + colbase) & 7;
        {
            float c[8], s[8];
#pragma unroll
            for (int j = 0; j < 8; ++j) {
                const float a = (float)(8 * (g & 1) + j) * (float)i16 * 0.0625f;
                const float co = cosr(a), si = sinr(a);
                c[j] = (g < 2) ? co : si;
                s[j] = (g < 2) ? -si : co;
            }
            f1re = pk8(c); f1im = pk8(s);
#pragma unroll
            for (int j = 0; j < 4; ++j) {
                const float pr = (float)(4 * g + j) * (float)i16;
                const float co = cosr(pr * 0.0625f), si = sinr(pr * 0.0625f);
                c[j] = co; c[4 + j] = si;
                s[j] = -si; s[4 + j] = co;
                t2c[j] = cosr(pr * (1.0f / 256.0f));
                t2s[j] = sinr(pr * (1.0f / 256.0f));
            }
            f2re = pk8(c); f2im = pk8(s);
        }
        const rsrc_t rw = mkrs(wpl ? P.wi : P.wr, 2048 * 642);
        u32x4 wv[2][8];
#pragma unroll
        for (int it = 0; it < 2; ++it) {
            const int task = min((t & 127) + 128 * it, 159), r = task / 5, ck = task - 5 * r;
#pragma unroll
            for (int e = 0; e < 8; ++e)
                wv[it][e] = ld16(rw, ((((ka + 8 * ((r & 15) + 16 * (8 * (r >> 4) + e))) * 321 + colbase) * 2) & ~15u) + ck * 16, 0);
        }
        fence();
        p3_consts(smem + L_P3C, w, l, ai);
#pragma unroll
        for (int it = 0; it < 2; ++it) {
            const int task = min((t & 127) + 128 * it, 159), r = task / 5, ck = task - 5 * r;
#pragma unroll
            for (int c = 0; c < 8; ++c) {
                const int col = 8 * ck + c - dl, m = col * 2 + wpl, d = c >> 1;
                const u32x4* x = wv[it];
                const u32x4 o = (c & 1) ? u32x4{permhi(x[0][d], x[1][d]), permhi(x[2][d], x[3][d]), permhi(x[4][d], x[5][d]), permhi(x[6][d], x[7][d])}
                                        : u32x4{permlo(x[0][d], x[1][d]), permlo(x[2][d], x[3][d]), permlo(x[4][d], x[5][d]), permlo(x[6][d], x[7][d])};
                const int dst = ((u32)col < 28u) ? L_W + m * 512 + ((((r & 15) * 2 + (r >> 4)) ^ ((m >> 3) & 7)) * 16) : L_DUMP + l * 16;
                *(u32x4*)(smem + dst) = o;
            }
        }
    } else {
        {
            float c[8], s[8];
#pragma unroll
            for (int j = 0; j < 8; ++j) {
                const float a = (float)(8 * (g & 1) + j) * (float)i16 * 0.0625f;
                const float co = cosr(a), si = sinr(a);
                c[j] = (g < 2) ? co : si;
                s[j] = (g < 2) ? -si : co;
            }
            f1re = pk8(c); f1im = pk8(s);
#pragma unroll
            for (int j = 0; j < 4; ++j) {
                const float pr = (float)(4 * g + j) * (float)i16;
                const float co = cosr(pr * 0.0625f), si = sinr(pr * 0.0625f);
                c[j] = co; c[4 + j] = si;
                s[j] = -si; s[4 + j] = co;
                t2c[j] = cosr(pr * (1.0f / 256.0f));
                t2s[j] = sinr(pr * (1.0f / 256.0f));
            }
            f2re = pk8(c); f2im = pk8(s);
        }
        const int task = min(t - 256, 223), oc = task / 7, ck = task - 7 * oc;
        const u32 base = OFF_X1 + (8 * oc) * X1_ROW + ai * 128 + ck * 16;
        u32x4 v[8];
        p3_consts(smem + L_P3C, w, l, ai);
        drain();
        if (w == 4) {
            wait_flags<256>(rws, F_X, tag, l);
            if (l == 0) {
                *word = 1u;
                if (ai == 0) __hip_atomic_store((GAS u32*)(P.ws + OFF_SYNC), tag, __ATOMIC_RELAXED, __HIP_MEMORY_SCOPE_AGENT);
            }
        }
        for (u32 spins = 0; spins < SPIN_MAX && *word == 0u; ++spins) __builtin_amdgcn_s_sleep(1);
        for (u32 spins = 0; spins < SPIN_MAX; ++spins) {
            fence();
            bool ok = true;
#pragma unroll
            for (int e = 0; e < 8; ++e) v[e] = ld16(rws, base + e * X1_ROW, 1);
#pragma unroll
            for (int e = 0; e < 8; ++e) ok &= fresh4(v[e]);
            if (__all(ok)) break;
            __builtin_amdgcn_s_sleep(1);
        }
#pragma unroll
        for (int c = 0; c < 8; ++c) {
            const int el = 8 * ck + c, pl = el >= 28, m = (el - 28 * pl) * 2 + pl, d = c >> 1;
            const u32x4 o = (c & 1) ? u32x4{permhi(v[0][d], v[1][d]), permhi(v[2][d], v[3][d]), permhi(v[4][d], v[5][d]), permhi(v[6][d], v[7][d])}
                                    : u32x4{permlo(v[0][d], v[1][d]), permlo(v[2][d], v[3][d]), permlo(v[4][d], v[5][d]), permlo(v[6][d], v[7][d])};
            *(u32x4*)(smem + L_C + m * 512 + ((oc ^ ((m >> 3) & 7)) * 16)) = o;
        }
    }
    const u32x4 ire = {f2re.x, f2re.y, f2re.z ^ 0x80008000u, f2re.w ^ 0x80008000u};
    const u32x4 iim = {f2im.x ^ 0x80008000u, f2im.y ^ 0x80008000u, f2im.z, f2im.w};
    asm volatile("" :: "v"(pw));
    STAMP();
    __syncthreads();
#pragma unroll
    for (int i = 0; i < 4; ++i) st16sc(rws, OFF_Y1 + ai * Y1_BLK + (t + 512 * i) * 16, u32x4{SENT, SENT, SENT, SENT});
    fence();
    {
        const int q = i16 >> 2, p = i16 & 3;
        const f32x4 zero = {0.f, 0.f, 0.f, 0.f};
        auto fetch = [&](int col, u32x4& a, u32x2& wre, u32x2& wim) {
            const int mi = col * 2 + (g >> 1), swi = (mi >> 3) & 7;
            const char* ib = smem + L_C + mi * 512 + (p & 1) * 8;
            const u32x2 a0 = trread(ib + (((2 * (8 * (g & 1) + q) + (p >> 1)) ^ swi) * 16));
            const u32x2 a1 = trread(ib + (((2 * (8 * (g & 1) + 4 + q) + (p >> 1)) ^ swi) * 16));
            a = u32x4{a0.x, a0.y, a1.x, a1.y};
            const int m0 = col * 2, sw = (m0 >> 3) & 7;
            const int woff = (((i16 * 2 + (g >> 1)) ^ sw) * 16) + (g & 1) * 8;
            wre = *(const u32x2*)(smem + L_W + m0 * 512 + woff);
            wim = *(const u32x2*)(smem + L_W + (m0 + 1) * 512 + woff);
        };
        auto columns = [&](auto nc, const u32x4* a, const u32x2* wre, const u32x2* wim, u32x2* ore, u32x2* oim) {
            constexpr int NC = decltype(nc)::value;
            f32x4 re[NC], im[NC];
            u32x4 d[NC];
#pragma unroll
            for (int c = 0; c < NC; ++c) { re[c] = mfma16(a[c], f1re, zero); im[c] = mfma16(a[c], f1im, zero); }
#pragma unroll
            for (int c = 0; c < NC; ++c) {
                float x[8];
#pragma unroll
                for (int i = 0; i < 4; ++i) {
                    x[i] = re[c][i] * t2c[i] + im[c][i] * t2s[i];
                    x[4 + i] = im[c][i] * t2c[i] - re[c][i] * t2s[i];
                }
                d[c] = pk8(x);
            }
#pragma unroll
            for (int c = 0; c < NC; ++c) { re[c] = mfma16(f2re, d[c], zero); im[c] = mfma16(f2im, d[c], zero); }
#pragma unroll
            for (int c = 0; c < NC; ++c) {
                float x[8];
#pragma unroll
                for (int i = 0; i < 4; ++i) {
                    const float a_ = (i & 1) ? bfhi(wre[c][i >> 1]) : bflo(wre[c][i >> 1]);
                    const float b_ = (i & 1) ? bfhi(wim[c][i >> 1]) : bflo(wim[c][i >> 1]);
                    x[i] = re[c][i] * a_ - im[c][i] * b_;
                    x[4 + i] = re[c][i] * b_ + im[c][i] * a_;
                }
                d[c] = pk8(x);
            }
#pragma unroll
            for (int c = 0; c < NC; ++c) { re[c] = mfma16(d[c], ire, zero); im[c] = mfma16(d[c], iim, zero); }
#pragma unroll
            for (int c = 0; c < NC; ++c) {
                float x[8];
#pragma unroll
                for (int i = 0; i < 4; ++i) {
                    x[i] = re[c][i] * t2c[i] - im[c][i] * t2s[i];
                    x[4 + i] = im[c][i] * t2c[i] + re[c][i] * t2s[i];
                }
                d[c] = pk8(x);
            }
#pragma unroll
            for (int c = 0; c < NC; ++c) { re[c] = mfma16(d[c], ire, zero); im[c] = mfma16(d[c], iim, zero); }
#pragma unroll
            for (int c = 0; c < NC; ++c) {
                ore[c] = u32x2{pk(re[c][0], re[c][1]), pk(re[c][2], re[c][3])};
                oim[c] = u32x2{pk(im[c][0], im[c][1]), pk(im[c][2], im[c][3])};
            }
        };
        auto put = [&](int col, const u32x2 ore, const u32x2 oim) {
            const int m0 = col * 2, sw = (m0 >> 3) & 7;
            const int ooff = (((2 * i16 + (g >> 1)) ^ sw) * 16) + (g & 1) * 8;
            *(u32x2*)(smem + L_C + m0 * 512 + ooff) = ore;
            *(u32x2*)(smem + L_C + (m0 + 1) * 512 + ooff) = oim;
        };
        u32x4 a[4];
        u32x2 wre[4], wim[4], ore[4], oim[4];
        if (w < 4) {
#pragma unroll
            for (int c = 0; c < 4; ++c) fetch(w + 8 * c, a[c], wre[c], wim[c]);
            columns(ic<4>{}, a, wre, wim, ore, oim);
#pragma unroll
            for (int c = 0; c < 4; ++c) put(w + 8 * c, ore[c], oim[c]);
        } else {
#pragma unroll
            for (int c = 0; c < 3; ++c) fetch(w + 8 * c, a[c], wre[c], wim[c]);
            columns(ic<3>{}, a, wre, wim, ore, oim);
#pragma unroll
            for (int c = 0; c < 3; ++c) put(w + 8 * c, ore[c], oim[c]);
        }
    }
    drain();
    STAMP();
    __syncthreads();
    if (t == 0) raise_flag(P.ws, F_Y, ai, tag);
    {
        const int q = i16 >> 2, p = i16 & 3;
        const int G = 2 * w + (g >> 1), pl = g & 1;
        u32x2 v[7];
#pragma unroll
        for (int it = 0; it < 7; ++it) {
            const int m = (4 * it + q) * 2 + pl;
            v[it] = trread(smem + L_C + m * 512 + (((2 * G + (p >> 1)) ^ ((m >> 3) & 7)) * 16) + (p & 1) * 8);
        }
#pragma unroll
        for (int it = 0; it < 7; ++it) {
            const int o = pl * 56 + it * 8;
            *(u32x2*)(smem + L_W + (16 * G + i16) * 128 + (((o >> 4) ^ (i16 & 7)) << 4) + (o & 8)) = v[it];
        }
    }
    __syncthreads();
#pragma unroll
    for (int i = 0; i < 4; ++i) {
        const int m = t + 512 * i;
        st16sc(rws, OFF_Y1 + ai * Y1_BLK + m * 16, sane(*(const u32x4*)(smem + L_W + (m >> 3) * 128 + (((m & 7) ^ ((m >> 3) & 7)) << 4))));
    }
    STAMP();

    u32x4 C[2], S[2], nsS[2], sC[2], F[2];
    float twc[8], tws[8];
    u32x2 fa[3];
    {
        const char* tb = smem + L_P3C;
        const u32 sgn = (g >= 2) ? 0x80008000u : 0u;
#pragma unroll
        for (int tau = 0; tau < 2; ++tau) {
            C[tau] = *(const u32x4*)(tb + tau * 1024 + l * 16);
            S[tau] = *(const u32x4*)(tb + (2 + tau) * 1024 + l * 16);
            nsS[tau] = S[tau] ^ (sgn ^ 0x80008000u);
            sC[tau] = C[tau] ^ sgn;
            const f32x4 a = *(const f32x4*)(tb + (4 + tau) * 1024 + l * 16), bq = *(const f32x4*)(tb + (6 + tau) * 1024 + l * 16);
#pragma unroll
            for (int i = 0; i < 4; ++i) { twc[4 * tau + i] = a[i]; tws[4 * tau + i] = bq[i]; }
            F[tau] = *(const u32x4*)(tb + (8 + tau) * 1024 + l * 16);
        }
#pragma unroll
        for (int j = 0; j < 3; ++j) fa[j] = *(const u32x2*)(tb + 10240 + j * 512 + l * 8);
    }
    drain();
    if (w == 7) {
        wait_flags<96>(rws, F_Y, tag, l);
        if (l == 0) *word = 2u;
    }
    STAMP();
    {
        u32 off[4];
#pragma unroll
        for (int i = 0; i < 4; ++i) {
            const int m = min(t + 512 * i, nset * 672 - 1), j = (m >= 672) + (m >= 1344), mm = m - 672 * j, ab = mm / 7;
            off[i] = OFF_Y1 + ab * Y1_BLK + (ai + 96 * j) * 128 + (mm - 7 * ab) * 16;
        }
        for (u32 spins = 0; spins < SPIN_MAX && *word != 2u; ++spins) __builtin_amdgcn_s_sleep(1);
        u32x4 v[4];
        for (u32 spins = 0; spins < SPIN_MAX; ++spins) {
            fence();
#pragma unroll
            for (int i = 0; i < 4; ++i) v[i] = ld16(rws, off[i], 1);
            if (__all(fresh4(v[0]) && fresh4(v[1]) && fresh4(v[2]) && fresh4(v[3]))) break;
            __builtin_amdgcn_s_sleep(1);
        }
#pragma unroll
        for (int i = 0; i < 4; ++i) *(u32x4*)(smem + L_X + min(t + 512 * i, nset * 672 - 1) * 16) = v[i];
    }
    STAMP();
    __syncthreads();
    {
        const int q = i16 >> 2, p = i16 & 3;
        const int kap = 2 * g + (q >> 1), pl = q & 1;
        u32x2 bfr[3][3];
#pragma unroll
        for (int it = 0; it < 3; ++it) {
            const int T = min(w + 8 * it, 20);
            const int k = min(16 * T + 4 * p, 320);
            const int cgk = min((k * 2341) >> 16, 11), idx = k - min(28 * cgk, 296);
            const int src = L_X + (kap * 12 + cgk) * 112 + pl * 56 + idx * 2;
#pragma unroll
            for (int j = 0; j < 3; ++j) bfr[it][j] = trread(smem + src + j * 10752);
        }
        u32x2 dv[3][3];
#pragma unroll
        for (int it = 0; it < 3; ++it)
#pragma unroll
            for (int j = 0; j < 3; ++j) {
                const f32x4 d = mfma16k16(fa[j], bfr[it][j], f32x4{0.f, 0.f, 0.f, 0.f});
                dv[it][j] = u32x2{pk(d[0], d[1]), pk(d[2], d[3])};
            }
#pragma unroll
        for (int it = 0; it < 3; ++it)
#pragma unroll
            for (int j = 0; j < 3; ++j)
                *(u32x2*)(smem + L_S + j * S_SZ + (16 * min(w + 8 * it, 20) + i16) * S_K + g * 8) = dv[it][j];
    }
    __syncthreads();
    STAMP();
    {
        const int k2 = i16 <= 10 ? i16 : 10;
        const int k0 = k2 + 160 * g;
        const int kofs = ((g < 2) ? k0 : 640 - k0) * S_K + w * 4, kstep = (g < 2) ? 20 * S_K : -20 * S_K;
        const rsrc_t ro = mkrs(P.out, 2048 * 2560);
        u32 dd[3][8];
#pragma unroll
        for (int j = 0; j < 3; ++j)
#pragma unroll
            for (int e = 0; e < 8; ++e) dd[j][e] = *(const u32*)(smem + L_S + j * S_SZ + kofs + e * kstep);
        const f32x4 zero = {0.f, 0.f, 0.f, 0.f};
        f32x4 vre[3][2], vim[3][2];
#pragma unroll
        for (int j = 0; j < 3; ++j) {
            const u32x4 are = {permlo(dd[j][0], dd[j][1]), permlo(dd[j][2], dd[j][3]), permlo(dd[j][4], dd[j][5]), permlo(dd[j][6], dd[j][7])};
            const u32x4 aim = {permhi(dd[j][0], dd[j][1]), permhi(dd[j][2], dd[j][3]), permhi(dd[j][4], dd[j][5]), permhi(dd[j][6], dd[j][7])};
#pragma unroll
            for (int tau = 0; tau < 2; ++tau) {
                vre[j][tau] = mfma16(aim, nsS[tau], mfma16(are, C[tau], zero));
                vim[j][tau] = mfma16(aim, sC[tau], mfma16(are, S[tau], zero));
            }
        }
        u32x4 va[3][2];
#pragma unroll
        for (int j = 0; j < 3; ++j)
#pragma unroll
            for (int tau = 0; tau < 2; ++tau) {
                float pr[8];
#pragma unroll
                for (int i = 0; i < 4; ++i) {
                    pr[i] = vre[j][tau][i] * twc[4 * tau + i] - vim[j][tau][i] * tws[4 * tau + i];
                    pr[4 + i] = vim[j][tau][i] * twc[4 * tau + i] + vre[j][tau][i] * tws[4 * tau + i];
                }
                va[j][tau] = pk8(pr);
            }
        f32x4 y[3][2][2];
#pragma unroll
        for (int j = 0; j < 3; ++j)
#pragma unroll
            for (int dl = 0; dl < 2; ++dl) {
                y[j][dl][0] = mfma16(va[j][0], F[dl], zero);
                y[j][dl][1] = mfma16(va[j][1], F[dl], zero);
            }
#pragma unroll
        for (int j = 0; j < 3; ++j) {
            const u32 orow = (ai + 96 * j + 256 * w) * 2560;
#pragma unroll
            for (int dl = 0; dl < 2; ++dl) {
                u32 a0 = pk(y[j][dl][0][0], y[j][dl][0][1]), a1 = pk(y[j][dl][0][2], y[j][dl][0][3]);
                u32 b0 = pk(y[j][dl][1][0], y[j][dl][1][1]), b1 = pk(y[j][dl][1][2], y[j][dl][1][3]);
                auto s0 = __builtin_amdgcn_permlane16_swap(a0, b0, false, false);
                auto s1 = __builtin_amdgcn_permlane16_swap(a1, b1, false, false);
                const int d2 = 16 * dl + i16;
                const u32 off = (d2 < 20 && j < nset) ? orow + (32 * d2 + (g & 1) * 16 + (g >> 1) * 8) * 2 : 0x80000000u;
                __builtin_amdgcn_raw_buffer_store_b128(u32x4{s0[0], s1[0], s0[1], s1[1]}, ro, off, 0, 0);
            }
        }
    }
    STAMP();
    STAMP_OUT(P.ws);
}

DI void role_b(const Args& P, char* smem, const int t, const int w, const int chb) {
    const int l = t & 63, g = l >> 4, i16 = l & 15;
    const int ch = w & 3, rh = w >> 2;
    STAMP_DECL STAMP();
    const rsrc_t rws = mkrs(P.ws, WS_USED);
    Start st;
    p1_start(P, st, t, w);
    p1_consts(smem, t, w);
    const u32x2 fb = p1_rows(P, smem, st, t, w);
    u32 pv[12], ldt = 0, pu = 0;
    const rsrc_t ru = mkrs(P.u, 2048 * 1280);
    if (w < 4) {
        const u32 nb = 640 * 128 * 2;
        const rsrc_t r0 = mkrs(P.Are, nb), r1 = mkrs(P.Aim, nb), r2 = mkrs(P.Bre, nb), r3 = mkrs(P.Bim, nb), r4 = mkrs(P.Cre, nb), r5 = mkrs(P.Cim, nb);
#pragma unroll
        for (int h = 0; h < 2; ++h) {
            const u32 off = ((4 * chb + w) * 128 + 64 * h + l) * 2;
            pv[6 * h + 0] = ld2(r0, off); pv[6 * h + 1] = ld2(r1, off); pv[6 * h + 2] = ld2(r2, off);
            pv[6 * h + 3] = ld2(r3, off); pv[6 * h + 4] = ld2(r4, off); pv[6 * h + 5] = ld2(r5, off);
        }
        ldt = ld2(mkrs(P.logdt, 1280), (4 * chb + w) * 2);
    }
    const u32 dks = ld2(mkrs(P.dskip, 1280), (4 * chb + ch) * 2);
    if (w < 5) {
        const int xcd = blockIdx.x & 7, jx = blockIdx.x >> 3;
        const int b0 = (160 * xcd) & ~127, nl = (160 * xcd + 160 - b0 + 127) >> 7;
        const int m = min(t, 103 * 3 - 1), r = m / 3;
        pu = __builtin_amdgcn_raw_buffer_load_b32(ru, min(103 * jx + r, 2047) * 1280 + b0 + min(m - 3 * r, nl - 1) * 128, 0, 0);
    }
    const u32 pw = warm_w(P, t, w);
    fence();
    p1_finish(P, smem, fb, t, w);
    STAMP();
    if (w >= 4) {
        const int tt = t - 256;
        fence();
        u32x2 gv[8];
#pragma unroll
        for (int e = 0; e < 8; ++e) gv[e] = ld8(ru, (8 * tt + e) * 1280 + chb * 8, 0);
        fence();
        if (tt < 128) *(u32x4*)(smem + L_U + (tt >> 5) * PB_U + (tt & 31) * 16) = u32x4{0u, 0u, 0u, 0u};
        else if (tt < 144) {
            const int v = tt - 128;
            *(u32x4*)(smem + L_K + (v >> 2) * PB_KS + ((v >> 1) & 1) * PB_K1 + 4096 + (v & 1) * 16) = u32x4{0u, 0u, 0u, 0u};
        }
        u32x4 o[4];
        tr8x4(gv, o);
#pragma unroll
        for (int c = 0; c < 4; ++c) *(u32x4*)(smem + L_U + c * PB_U + 512 + tt * 16) = o[c];
    } else {
        char* sc = smem + w * KG_SZ;
        const float dt = rbf(expf(bf2f(ldt)));
        f32x4 kacc[3];
#pragma unroll
        for (int T = 0; T < 3; ++T) kacc[T] = f32x4{0.f, 0.f, 0.f, 0.f};
#pragma unroll 1
        for (int h = 0; h < 2; ++h) {
            const float lre = dt * bf2f(h ? pv[6] : pv[0]), lim = dt * bf2f(h ? pv[7] : pv[1]) * 0.15915494309189535f;
            const cf c = cmul(cf{bf2f(h ? pv[10] : pv[4]), bf2f(h ? pv[11] : pv[5])}, cf{dt * bf2f(h ? pv[8] : pv[2]), dt * bf2f(h ? pv[9] : pv[3])});
            char* rowp = sc + l * 4;
            const cf z1 = cexpk(lre, lim, 1.f), z8 = cexpk(lre, lim, 8.f), z128 = cexpk(lre, lim, 128.f);
            const cf z2 = cmul(z1, z1), z4 = cmul(z2, z2), z6 = cmul(z2, z4);
            const cf z8c = {z8.x, -z8.y};
            cf v = {1.f, 0.f}, r0 = c, r1 = cmul(c, z128);
            cf q = cmul(r1, z128);
#pragma unroll
            for (int j = 0; j < 8; ++j) {
                *(u32*)(rowp + KG_B + (2 * j) * KG_RS) = pk(v.x, v.y);
                v = cmul(v, z8c);
                *(u32*)(rowp + KG_B + (2 * j + 1) * KG_RS) = pk(v.x, v.y);
                v = cmul(v, z8c);
                *(u32*)(rowp + j * KG_RS) = pkc(r0);
                *(u32*)(rowp + (8 + j) * KG_RS) = pkc(r1);
                r0 = cmul(r0, z1);
                r1 = cmul(r1, z1);
#pragma unroll
                for (int e = 0; e < 2; ++e) {
                    const int hh = 2 + 2 * j + e;
                    if (hh < 4) {
                        const int r = 16 + (hh - 2) * 4;
                        *(u32*)(rowp + r * KG_RS) = pkc(q);
                        *(u32*)(rowp + (r + 1) * KG_RS) = pkc(cmul(q, z2));
                        *(u32*)(rowp + (r + 2) * KG_RS) = pkc(cmul(q, z4));
                        *(u32*)(rowp + (r + 3) * KG_RS) = pkc(cmul(q, z6));
                    } else if (hh < 8) {
                        const int r = 24 + (hh - 4) * 2;
                        *(u32*)(rowp + r * KG_RS) = pkc(q);
                        *(u32*)(rowp + (r + 1) * KG_RS) = pkc(cmul(q, z4));
                    } else if (hh <= 16) {
                        *(u32*)(rowp + (32 + hh - 8) * KG_RS) = pkc(q);
                    }
                    if (hh < 16) q = cmul(q, z128);
                }
            }
            u32x4 bf[4], af[3][4];
#pragma unroll
            for (int ks = 0; ks < 4; ++ks) {
                bf[ks] = *(const u32x4*)(sc + KG_B + i16 * KG_RS + g * 16 + ks * 64);
#pragma unroll
                for (int T = 0; T < 3; ++T) af[T][ks] = *(const u32x4*)(sc + (16 * T + i16) * KG_RS + g * 16 + ks * 64);
            }
#pragma unroll
            for (int T = 0; T < 3; ++T)
#pragma unroll
                for (int ks = 0; ks < 4; ++ks) kacc[T] = mfma16(af[T][ks], bf[ks], kacc[T]);
        }
        char* tbl = smem + L_T + w * KG_T;
#pragma unroll
        for (int T = 0; T < 3; ++T)
#pragma unroll
            for (int i = 0; i < 4; ++i) {
                const int row = 16 * T + 4 * g + i;
                int e;
                if (T == 0) e = 128 * (row >> 3) + (row & 7);
                else if (T == 1) e = (g < 2) ? 256 + 128 * g + 2 * i : 512 + 256 * (g - 2) + 128 * (i >> 1) + 4 * (i & 1);
                else e = 1024 + 128 * (row - 32);
                const bool ok = row < 40 || (row == 40 && i16 == 0);
                *(u16*)(tbl + (ok ? (e + 8 * i16) * 2 : 4110)) = (u16)(pk(kacc[T][i], kacc[T][i]) & 0xffffu);
            }
        u32 v[4][8], vm[4];
#pragma unroll
        for (int it = 0; it < 4; ++it) {
            const int tt = l + 64 * it, a = 255 - tt;
            const u32 code = (a < 32) ? 0x76543210u : (a < 64) ? 0x86444200u : (a < 128) ? 0x88444000u : ((a & 1) ? 0x88880000u : 0x88800000u);
            const int lastoff = (a <= 32) ? 7 : 8;
            const char* tb = tbl + 16 * a;
#pragma unroll
            for (int e = 0; e < 8; ++e) v[it][e] = *(const u16*)(tb + 2 * ((code >> (4 * e)) & 15));
            vm[it] = *(const u16*)(tb + ((a == 0) ? 0 : 2 * lastoff - 16));
            vm[it] = (a == 0) ? 0u : vm[it];
        }
#pragma unroll
        for (int it = 0; it < 4; ++it) {
            char* kb = smem + L_K + w * PB_KS + (l + 64 * it) * 16;
            const u32* x = v[it];
            *(u32x4*)(kb) = u32x4{x[7] | (x[6] << 16), x[5] | (x[4] << 16), x[3] | (x[2] << 16), x[1] | (x[0] << 16)};
            *(u32x4*)(kb + PB_K1) = u32x4{x[6] | (x[5] << 16), x[4] | (x[3] << 16), x[2] | (x[1] << 16), x[0] | (vm[it] << 16)};
        }
    }
    asm volatile("" :: "v"(pw ^ pu));
    STAMP();
    __syncthreads();
    STAMP();
    const int mu = g >> 1, hp = g & 1;
    const int x0 = 2047 - 16 * mu - i16 + 8 * hp;
    const char* ap = smem + L_K + ch * PB_KS + ((x0 & 1) ? PB_K1 + (x0 - 1) * 2 : x0 * 2);
    const char* bp = smem + L_U + ch * PB_U + (256 + 16 * (i16 - mu) + 8 * hp) * 2;
    f32x4 acc[8];
#pragma unroll
    for (int c = 0; c < 8; ++c) acc[c] = f32x4{0.f, 0.f, 0.f, 0.f};
    {
        u32x4 A[2][8], B[2][8];
        const char* ap0 = ap - 256 * rh;
        const char* bp0 = bp - 256 * rh;
#pragma unroll
        for (int p = 0; p < 8; ++p) {
            const u32* a = (const u32*)(ap0 - 512 * p);
            B[0][p] = *(const u32x4*)(bp0 + 512 * p);
            A[0][p] = u32x4{a[0], a[1], a[2], a[3]};
        }
#pragma unroll
        for (int r = 0; r < 4; ++r) {
            __builtin_amdgcn_sched_barrier(0);
            if (r < 3) {
#pragma unroll
                for (int p = 0; p < 8; ++p) {
                    const u32* a = (const u32*)(ap0 - 64 * (r + 1) - 512 * p);
                    B[(r + 1) & 1][p] = *(const u32x4*)(bp0 - 64 * (r + 1) + 512 * p);
                    A[(r + 1) & 1][p] = u32x4{a[0], a[1], a[2], a[3]};
                }
            }
#pragma unroll
            for (int p = 0; p < 8; ++p)
#pragma unroll
                for (int q = 0; q + p < 8; ++q) acc[p + q] = mfma16(A[r & 1][p], B[r & 1][q], acc[p + q]);
            if (r < 3) {
#pragma unroll
                for (int k = 0; k < 12; ++k) {
                    __builtin_amdgcn_sched_group_barrier(0x008, 3, 0);
                    __builtin_amdgcn_sched_group_barrier(0x100, 2, 0);
                }
            }
        }
        __builtin_amdgcn_sched_barrier(0);
    }
    STAMP();
    char* rb = smem + L_R;
#pragma unroll
    for (int c = 0; c < 4; ++c) *(f32x4*)(rb + (w * 4 + c) * 1024 + l * 16) = rh ? acc[c] : acc[4 + c];
    __syncthreads();
    char* yi = smem + L_Y;
    const float dsk = bf2f(dks);
    {
        f32x4 o[4];
        u32x2 uu[4];
#pragma unroll
        for (int c = 0; c < 4; ++c) {
            o[c] = *(const f32x4*)(rb + ((w ^ 4) * 4 + c) * 1024 + l * 16);
            uu[c] = *(const u32x2*)(smem + L_U + ch * PB_U + (256 + 256 * (4 * rh + c) + 16 * i16 + 4 * g) * 2);
        }
        u32x2 res[4];
#pragma unroll
        for (int c = 0; c < 4; ++c) {
            const f32x4 mine = rh ? acc[4 + c] : acc[c];
            float v[4];
#pragma unroll
            for (int i = 0; i < 4; ++i) {
                const float uf = (i & 1) ? bfhi(i < 2 ? uu[c].x : uu[c].y) : bflo(i < 2 ? uu[c].x : uu[c].y);
                v[i] = rbf(mine[i] + o[c][i]) + rbf(uf * dsk);
            }
            res[c] = u32x2{pk(v[0], v[1]), pk(v[2], v[3])};
        }
#pragma unroll
        for (int c = 0; c < 4; ++c) *(u32x2*)(yi + ch * PB_Y + (64 * (4 * rh + c) + 4 * i16 + (g ^ ((i16 >> 2) & 3))) * 8) = res[c];
    }
    __syncthreads();
    STAMP();
    {
        const rsrc_t ro = mkrs(P.out, 2048 * 2560);
        const int q = i16 >> 2, p = i16 & 3;
        u32x2 v[4];
#pragma unroll
        for (int i = 0; i < 4; ++i) {
            const int sg = ((256 * w + 64 * i + 16 * g) >> 2) + p;
            v[i] = trread(yi + q * PB_Y + (sg ^ ((sg >> 4) & 3)) * 8);
        }
#pragma unroll
        for (int i = 0; i < 4; ++i)
            __builtin_amdgcn_raw_buffer_store_b64(v[i], ro, (256 * w + 64 * i + 16 * g + i16) * 2560 + 1280 + chb * 8, 0, 0);
    }
    STAMP();
    STAMP_OUT(P.ws);
}

__global__ void __launch_bounds__(512, 2) fused_kernel(Args P) {
    extern __shared__ __attribute__((aligned(16))) char smem[];
    const int t = threadIdx.x;
    const int w = __builtin_amdgcn_readfirstlane(t >> 6);
    const int b = blockIdx.x, xcd = b & 7, jx = b >> 3;
    if (jx < 20) role_b(P, smem, t, w, 20 * xcd + jx);
    else role_a(P, smem, t, w, xcd, jx - 20);
}

extern "C" void kernel_launch(void* const* d_in, const int* in_sizes, int n_in,
                              void* d_out, int out_size, void* d_ws, size_t ws_size,
                              hipStream_t stream) {
    Args a;
    a.x = (const u16*)d_in[0];
    a.wr = (const u16*)d_in[1];
    a.wi = (const u16*)d_in[2];
    a.u = (const u16*)d_in[3];
    a.Are = (const u16*)d_in[4];
    a.Aim = (const u16*)d_in[5];
    a.Bre = (const u16*)d_in[6];
    a.Bim = (const u16*)d_in[7];
    a.Cre = (const u16*)d_in[8];
    a.Cim = (const u16*)d_in[9];
    a.logdt = (const u16*)d_in[10];
    a.dskip = (const u16*)d_in[11];
    a.ws = (char*)d_ws;
    a.out = (u16*)d_out;
    (void)hipFuncSetAttribute((const void*)fused_kernel, hipFuncAttributeMaxDynamicSharedMemorySize, LDS_BYTES);
    fused_kernel<<<256, 512, LDS_BYTES, stream>>>(a);
}
```

```cpp
#include <hip/hip_runtime.h>
#include <hip/hip_bf16.h>

typedef unsigned short u16;
typedef unsigned int u32;
typedef __attribute__((ext_vector_type(2))) __bf16 bf16x2;
typedef __attribute__((ext_vector_type(8))) __bf16 bf16x8;
typedef __attribute__((ext_vector_type(2))) float f32x2;
typedef __attribute__((ext_vector_type(4))) float f32x4;
typedef __attribute__((ext_vector_type(4))) u32 u32x4;
typedef __attribute__((ext_vector_type(2))) u32 u32x2;
typedef __attribute__((ext_vector_type(4))) short s16x4;
typedef __amdgpu_buffer_rsrc_t rsrc_t;
#define LAS __attribute__((address_space(3)))
#define GAS __attribute__((address_space(1)))
#define DI __device__ __forceinline__
#define SC1 16

constexpr u32 X1_ROW = 12288;
constexpr u32 OFF_X1 = 0;
constexpr u32 Y1_BLK = 32768;
constexpr u32 OFF_Y1 = 256 * X1_ROW;
constexpr u32 OFF_SYNC = OFF_Y1 + 96 * Y1_BLK;
constexpr u32 F_X = 1024;
constexpr u32 F_Y = F_X + 1024;
constexpr u32 WS_USED = OFF_SYNC + F_Y + 1024;
constexpr u32 SENT = 0xFFFFFFFFu;
constexpr u32 SPIN_MAX = 1u << 16;

constexpr int S_K = 40, S_SZ = 336 * S_K;
constexpr int P_X = 80448;
constexpr int P_S = P_X + 12800;
constexpr int P_C = 32768;
constexpr int L_W = 0;
constexpr int L_C = L_W + 32768;
constexpr int L_DUMP = L_C + 28672;
constexpr int L_X = L_DUMP + 1024;
constexpr int L_S = L_X + 3 * 10752;
constexpr int L_P3C = L_S + 3 * S_SZ;
constexpr int KG_RS = 272, KG_B = 41 * KG_RS, KG_SZ = 57 * KG_RS;
constexpr int PB_U = 4608, PB_K1 = 4160, PB_KS = 8320, PB_Y = 4128, KG_T = 4112;
constexpr int L_U = 4 * KG_SZ;
constexpr int L_K = P_S + S_SZ;
constexpr int L_T = L_K + 4 * PB_KS;
constexpr int L_R = 0;
constexpr int L_Y = L_R + 32768;
constexpr int L_WORD = L_T + 4 * KG_T;
constexpr int LDS_BYTES = L_WORD + 16;
static_assert(L_P3C + 11776 <= L_WORD && L_U + 4 * PB_U <= P_X && L_Y + 4 * PB_Y <= L_U && LDS_BYTES <= 163840, "LDS map");

DI u32 pk(float a, float b) {
    f32x2 v = {a, b};
    return __builtin_bit_cast(u32, __builtin_convertvector(v, bf16x2));
}
DI u32x4 pk8(const float* v) { return u32x4{pk(v[0], v[1]), pk(v[2], v[3]), pk(v[4], v[5]), pk(v[6], v[7])}; }
DI float bflo(u32 v) { return __uint_as_float(v << 16); }
DI float bfhi(u32 v) { return __uint_as_float(v & 0xffff0000u); }
DI float bf2f(u32 v) { return __uint_as_float(v << 16); }
DI float rbf(float a) { return bflo(pk(a, a)); }
DI float cosr(float rev) { return __builtin_amdgcn_cosf(rev); }
DI float sinr(float rev) { return __builtin_amdgcn_sinf(rev); }
DI f32x4 mfma16(u32x4 a, u32x4 b, f32x4 c) {
    return __builtin_amdgcn_mfma_f32_16x16x32_bf16(__builtin_bit_cast(bf16x8, a), __builtin_bit_cast(bf16x8, b), c, 0, 0, 0);
}
DI f32x4 mfma16k16(u32x2 a, u32x2 b, f32x4 c) {
    return __builtin_amdgcn_mfma_f32_16x16x16bf16_1k(__builtin_bit_cast(s16x4, a), __builtin_bit_cast(s16x4, b), c, 0, 0, 0);
}
DI u32x2 trread(const char* p) {
    return __builtin_bit_cast(u32x2, __builtin_amdgcn_ds_read_tr16_b64_v4i16((LAS s16x4*)(p)));
}
DI u32 permlo(u32 a, u32 b) { return __builtin_amdgcn_perm(b, a, 0x05040100u); }
DI u32 permhi(u32 a, u32 b) { return __builtin_amdgcn_perm(b, a, 0x07060302u); }
DI rsrc_t mkrs(const void* p, u32 bytes) { return __builtin_amdgcn_make_buffer_rsrc((void*)p, 0, bytes, 0x00020000); }
DI u32x4 ld16(rsrc_t r, u32 off, int aux) { return aux ? __builtin_amdgcn_raw_buffer_load_b128(r, off, 0, SC1) : __builtin_amdgcn_raw_buffer_load_b128(r, off, 0, 0); }
DI u32x2 ld8(rsrc_t r, u32 off, int aux) { return aux ? __builtin_amdgcn_raw_buffer_load_b64(r, off, 0, SC1) : __builtin_amdgcn_raw_buffer_load_b64(r, off, 0, 0); }
DI u32 ld2(rsrc_t r, u32 off) { return __builtin_amdgcn_raw_buffer_load_b16(r, off, 0, 0); }
DI void st16sc(rsrc_t r, u32 off, u32x4 v) { __builtin_amdgcn_raw_buffer_store_b128(v, r, off, 0, SC1); }
DI u32x4 sane(u32x4 v) { return u32x4{min(v.x, SENT - 1), min(v.y, SENT - 1), min(v.z, SENT - 1), min(v.w, SENT - 1)}; }
DI bool fresh4(u32x4 v) { return max(max(v.x, v.y), max(v.z, v.w)) != SENT; }
DI void fence() { asm volatile("" ::: "memory"); }
DI void drain() { asm volatile("s_waitcnt vmcnt(0)" ::: "memory"); }
DI void tr8x4(const u32x2* v, u32x4* o) {
    o[0] = u32x4{permlo(v[0].x, v[1].x), permlo(v[2].x, v[3].x), permlo(v[4].x, v[5].x), permlo(v[6].x, v[7].x)};
    o[1] = u32x4{permhi(v[0].x, v[1].x), permhi(v[2].x, v[3].x), permhi(v[4].x, v[5].x), permhi(v[6].x, v[7].x)};
    o[2] = u32x4{permlo(v[0].y, v[1].y), permlo(v[2].y, v[3].y), permlo(v[4].y, v[5].y), permlo(v[6].y, v[7].y)};
    o[3] = u32x4{permhi(v[0].y, v[1].y), permhi(v[2].y, v[3].y), permhi(v[4].y, v[5].y), permhi(v[6].y, v[7].y)};
}
DI void raise_flag(char* ws, u32 set, int i, u32 tag) {
    __hip_atomic_store((GAS u32*)(ws + OFF_SYNC + set) + i, tag, __ATOMIC_RELAXED, __HIP_MEMORY_SCOPE_AGENT);
}
template <int NF>
DI void wait_flags(rsrc_t rws, u32 set, u32 tag, int l) {
    for (u32 spins = 0; spins < SPIN_MAX; ++spins) {
        fence();
        const u32x4 f = ld16(rws, OFF_SYNC + set + min(l, NF / 4 - 1) * 16, 1);
        if (__all(f.x == tag && f.y == tag && f.z == tag && f.w == tag)) break;
        __builtin_amdgcn_s_sleep(4);
    }
}

template <int N> struct ic { static constexpr int value = N; };
typedef f32x2 cf;
DI cf cmul(cf a, cf b) { return __builtin_elementwise_fma(cf{a.y, a.y}, cf{-b.y, b.x}, cf{a.x, a.x} * b); }
DI cf cexpk(float lre, float lim_rev, float k) {
    const float m = __builtin_amdgcn_exp2f(k * lre * 1.44269504088896341f);
    const float a = __builtin_amdgcn_fractf(k * lim_rev);
    return cf{m * cosr(a), m * sinr(a)};
}
DI u32 pkc(cf v) { return pk(v.x, v.y); }

struct Args {
    const u16 *x, *wr, *wi, *u, *Are, *Aim, *Bre, *Bim, *Cre, *Cim, *logdt, *dskip;
    char* ws;
    u16* out;
};

struct Start { u32x4 xv[2]; u32 gen; };
DI void p1_start(const Args& P, Start& st, const int t, const int w) {
    const int l = t & 63, b = blockIdx.x;
    const rsrc_t rws = mkrs(P.ws, WS_USED);
    st.gen = __builtin_amdgcn_raw_buffer_load_b32(rws, OFF_SYNC, 0, SC1);
    fence();
    const rsrc_t rx = mkrs(P.x, 2048 * 1280);
    const u32 ro = (b + 256 * w) * 1280;
    st.xv[0] = ld16(rx, ro + l * 16, 0);
    st.xv[1] = ld16(rx, ro + min(64 + l, 79) * 16, 0);
    fence();
#pragma unroll
    for (int i = 0; i < 2; ++i) st16sc(rws, OFF_X1 + b * X1_ROW + min(t + 512 * i, 767) * 16, u32x4{SENT, SENT, SENT, SENT});
    fence();
}
DI u32 warm_w(const Args& P, const int t, const int w) {
    const int l = t & 63, b = blockIdx.x;
    u32 pw = 0;
    if (w >= 6) {
        const int m = min(l, 47);
        pw = __builtin_amdgcn_raw_buffer_load_b32(mkrs(w == 6 ? P.wr : P.wi, 2048 * 642),
                                                  ((((b & 7) + 8 * (8 * (b >> 3) + m / 6)) * 642) & ~127u) + (m % 6) * 128, 0, 0);
    }
    return pw;
}
DI void p1_consts(char* smem, const int t, const int w) {
    const int l = t & 63, g = l >> 4, i16 = l & 15, b = blockIdx.x;
    if (t < 15) *(u32x2*)(smem + P_S + 321 * S_K + t * 8) = u32x2{0u, 0u};
    {
        float v[8];
        if (w < 4) {
#pragma unroll
            for (int j = 0; j < 8; ++j) {
                const int d = (j < 4) ? 4 * g + j : 16 + 4 * g + j - 4;
                const float c = cosr((float)d * (float)i16 * (w < 2 ? 0.05f : 0.03125f) - ((w & 1) ? 0.25f : 0.f));
                v[j] = (w >= 2) ? c : ((d < 20 && i16 <= 10) ? ((w & 1) ? -c : c) : 0.f);
            }
            *(u32x4*)(smem + P_C + w * 1024 + l * 16) = pk8(v);
        } else {
#pragma unroll
            for (int i = 0; i < 4; ++i)
                v[i] = cosr((float)(16 * (w & 1) + 4 * g + i) * (float)i16 * (1.0f / 640.0f) - ((w >= 6) ? 0.25f : 0.f));
            *(f32x4*)(smem + P_C + w * 1024 + l * 16) = f32x4{v[0], v[1], v[2], v[3]};
            if (w == 4) {
                const int kap = i16 >> 1, plo = i16 & 1;
#pragma unroll
                for (int e = 0; e < 4; ++e) {
                    const int a = 2 * g + (e >> 1), pl = e & 1;
                    const float c = cosr((float)kap * (float)(256 * a + b) * (1.0f / 2048.0f) - ((pl != plo) ? 0.25f : 0.f));
                    v[e] = (plo == 1 && pl == 0) ? -c : c;
                }
                *(u32x2*)(smem + P_C + 8192 + l * 8) = u32x2{pk(v[0], v[1]), pk(v[2], v[3])};
            }
        }
    }
    if (t == 0) *(u32x4*)(smem + L_WORD) = u32x4{0u, 0u, 0u, 0u};
    __syncthreads();
}
DI u32x2 p1_rows(const Args& P, char* smem, const Start& st, const int t, const int w) {
    const int l = t & 63, g = l >> 4, i16 = l & 15, b = blockIdx.x;
    const u32x4* xv = st.xv;
    const u32x4 f20c = *(const u32x4*)(smem + P_C + l * 16), f20s = *(const u32x4*)(smem + P_C + 1024 + l * 16);
    const u32x4 c32 = *(const u32x4*)(smem + P_C + 2048 + l * 16), s32 = *(const u32x4*)(smem + P_C + 3072 + l * 16);
    float twc[8], tws[8];
#pragma unroll
    for (int i = 0; i < 2; ++i) {
        const f32x4 a = *(const f32x4*)(smem + P_C + (4 + i) * 1024 + l * 16), bq = *(const f32x4*)(smem + P_C + (6 + i) * 1024 + l * 16);
#pragma unroll
        for (int e = 0; e < 4; ++e) { twc[4 * i + e] = a[e]; tws[4 * i + e] = bq[e]; }
    }
    const u32x2 fb = *(const u32x2*)(smem + P_C + 8192 + l * 8);
    const u32x4 ns32 = s32 ^ 0x80008000u, c32b = c32 ^ 0x80000000u, s32b = s32 ^ 0x80000000u, nc32b = c32b ^ 0x80008000u;
    {
        const int q = i16 >> 2, p = i16 & 3;
        const int d2a = 4 * g + q, d2b = (g == 0) ? 16 + q : d2a;
        const int offa = 64 * d2a + 8 * p, swa = ((d2a >> 2) & 1) << 5;
        const int offb = 64 * d2b + 8 * p, swb = ((d2b >> 2) & 1) << 5;
        char* xr = smem + P_X + w * 1280;
        *(u32x4*)(xr + ((l * 16) ^ (((l >> 4) & 1) << 5))) = xv[0];
        *(u32x4*)(xr + min(64 + l, 79) * 16) = xv[1];
        f32x4 tre[2], tim[2];
        const f32x4 zero = {0.f, 0.f, 0.f, 0.f};
        u32x4 xa[2];
#pragma unroll
        for (int tau = 0; tau < 2; ++tau) {
            const u32x2 a0 = trread(xr + offa + ((32 * tau) ^ swa));
            const u32x2 a1 = trread(xr + offb + ((32 * tau) ^ swb));
            xa[tau] = u32x4{a0.x, a0.y, a1.x, a1.y};
        }
#pragma unroll
        for (int tau = 0; tau < 2; ++tau) {
            tre[tau] = mfma16(xa[tau], f20c, zero);
            tim[tau] = mfma16(xa[tau], f20s, zero);
        }
        float pr[8], pi[8];
#pragma unroll
        for (int e = 0; e < 8; ++e) {
            const float a = tre[e >> 2][e & 3], bb = tim[e >> 2][e & 3];
            pr[e] = a * twc[e] + bb * tws[e];
            pi[e] = bb * twc[e] - a * tws[e];
        }
        const u32x4 bre = pk8(pr), bim = pk8(pi);
        const f32x4 z0 = mfma16(s32, bim, mfma16(c32, bre, zero));
        const f32x4 z1 = mfma16(ns32, bre, mfma16(c32, bim, zero));
        const f32x4 z2 = mfma16(s32b, bim, mfma16(c32b, bre, zero));
        const f32x4 z3 = mfma16(s32b, bre, mfma16(nc32b, bim, zero));
        const bool okA = i16 <= 10, okB = (i16 >= 1 && i16 <= 9), okB0 = okB || (i16 == 0 && g == 0);
        const int kA = i16 + 80 * g, kB = 320 - i16 - 80 * g;
        const int oA0 = (okA ? kA : 335) * S_K, oAs = okA ? 20 * S_K : 0;
        const int oB0 = (okB0 ? kB : 335) * S_K, oB1 = (okB ? kB - 20 : 335) * S_K, oBs = okB ? 20 * S_K : 0;
        char* st = smem + P_S + w * 4;
#pragma unroll
        for (int i = 0; i < 4; ++i) *(u32*)(st + oA0 + i * oAs) = pk(z0[i], z1[i]);
        *(u32*)(st + oB0) = pk(z2[0], z3[0]);
#pragma unroll
        for (int i = 1; i < 4; ++i) *(u32*)(st + oB1 - (i - 1) * oBs) = pk(z2[i], z3[i]);
    }
    drain();
    __syncthreads();
    if (t == 0) raise_flag(P.ws, F_X, b, st.gen + 1u);
    return fb;
}
DI void p1_finish(const Args& P, char* smem, const u32x2 fb, const int t, const int w) {
    const int l = t & 63, g = l >> 4, i16 = l & 15, b = blockIdx.x;
    {
        u32x2 av[3], dv[3];
#pragma unroll
        for (int it = 0; it < 3; ++it)
            av[it] = *(const u32x2*)(smem + P_S + (16 * min(w + 8 * it, 20) + i16) * S_K + g * 8);
#pragma unroll
        for (int it = 0; it < 3; ++it) {
            const f32x4 d = mfma16k16(av[it], fb, f32x4{0.f, 0.f, 0.f, 0.f});
            dv[it] = u32x2{pk(d[0], d[1]), pk(d[2], d[3])};
        }
        const int kap = i16 >> 1, po = (i16 & 1) * 56;
#pragma unroll
        for (int it = 0; it < 3; ++it) {
            const int k = 16 * min(w + 8 * it, 20) + 4 * g;
            const int cgk = min((k * 2341) >> 16, 11);
            const int o0 = po + (k - min(28 * cgk, 296)) * 2, o1 = po + (k - 296) * 2;
            const int d0 = (k < 324) ? (kap * 12 + cgk) * 128 + (((o0 >> 4) ^ kap) << 4) + (o0 & 8) : 12288 + l * 8;
            const int d1 = (k >= 296 && k < 308) ? (kap * 12 + 11) * 128 + (((o1 >> 4) ^ kap) << 4) + (o1 & 8) : d0;
            *(u32x2*)(smem + P_X + d0) = dv[it];
            *(u32x2*)(smem + P_X + d1) = dv[it];
        }
    }
    __syncthreads();
    const rsrc_t rws = mkrs(P.ws, WS_USED);
    if (w >= 4) {
#pragma unroll
        for (int i = 0; i < 3; ++i) {
            const int m = (t & 255) + 256 * i, pc = m >> 3, kp = (pc * 43) >> 9;
            st16sc(rws, OFF_X1 + b * X1_ROW + m * 16, sane(*(const u32x4*)(smem + P_X + pc * 128 + (((m & 7) ^ kp) << 4))));
        }
    }
}

DI void p3_consts(char* tb, const int w, const int l, const int ai) {
    const int g = l >> 4, i16 = l & 15;
    float v[8];
    if (w < 4) {
        const float d1 = (float)(16 * (w & 1) + i16);
#pragma unroll
        for (int j = 0; j < 8; ++j) v[j] = cosr((float)(8 * g + j) * d1 * 0.03125f - ((w >= 2) ? 0.25f : 0.f));
        *(u32x4*)(tb + w * 1024 + l * 16) = pk8(v);
    } else {
        const int tau = w & 1;
        const float d1 = (float)(16 * tau + i16);
#pragma unroll
        for (int i = 0; i < 4; ++i)
            v[i] = cosr(d1 * (float)(4 * g + i) * (1.0f / 640.0f) - ((w >= 6) ? 0.25f : 0.f)) * (1.0f / (2048.0f * 640.0f));
        *(f32x4*)(tb + w * 1024 + l * 16) = f32x4{v[0], v[1], v[2], v[3]};
        if (w < 6) {
#pragma unroll
            for (int j = 0; j < 8; ++j) {
                const int k2 = 4 * g + (j & 3), d2 = 16 * tau + i16;
                const float wgt = (k2 > 10 || d2 >= 20) ? 0.f : ((k2 == 0 || k2 == 10) ? 1.f : 2.f);
                const float c = cosr((float)k2 * (float)d2 * 0.05f - ((j < 4) ? 0.f : 0.25f));
                v[j] = (j < 4) ? wgt * c : -wgt * c;
            }
            *(u32x4*)(tb + (8 + tau) * 1024 + l * 16) = pk8(v);
        }
    }
    if (w < 3) {
        const int a = i16 >> 1, plo = i16 & 1;
#pragma unroll
        for (int e = 0; e < 4; ++e) {
            const int kk = 2 * g + (e >> 1), pp = e & 1;
            const float c = cosr((float)kk * (float)(256 * a + ai + 96 * w) * (1.0f / 2048.0f) - ((pp != plo) ? 0.25f : 0.f));
            v[e] = (plo == 0 && pp == 1) ? -c : c;
        }
        *(u32x2*)(tb + 10240 + w * 512 + l * 8) = u32x2{pk(v[0], v[1]), pk(v[2], v[3])};
    }
}

DI void role_a(const Args& P, char* smem, const int t, const int w, const int ka, const int cg) {
    const int l = t & 63, g = l >> 4, i16 = l & 15;
    const int ai = ka * 12 + cg, nset = ai < 64 ? 3 : 2;
    const int colbase = min(28 * cg, 296);
    const rsrc_t rws = mkrs(P.ws, WS_USED);
    Start st;
    p1_start(P, st, t, w);
    p1_consts(smem, t, w);
    const u32x2 fb = p1_rows(P, smem, st, t, w);
    const u32 pw = warm_w(P, t, w);
    fence();
    p1_finish(P, smem, fb, t, w);
    const u32 tag = st.gen + 1u;
    volatile LAS u32* word = (volatile LAS u32*)(smem + L_WORD);
    u32x4 f1re, f1im, f2re, f2im;
    float t2c[4], t2s[4];
    if (w < 4) {
        const int wpl = w >> 1, dl = (ka + colbase) & 7;
        {
            float c[8], s[8];
#pragma unroll
            for (int j = 0; j < 8; ++j) {
                const float a = (float)(8 * (g & 1) + j) * (float)i16 * 0.0625f;
                const float co = cosr(a), si = sinr(a);
                c[j] = (g < 2) ? co : si;
                s[j] = (g < 2) ? -si : co;
            }
            f1re = pk8(c); f1im = pk8(s);
#pragma unroll
            for (int j = 0; j < 4; ++j) {
                const float pr = (float)(4 * g + j) * (float)i16;
                const float co = cosr(pr * 0.0625f), si = sinr(pr * 0.0625f);
                c[j] = co; c[4 + j] = si;
                s[j] = -si; s[4 + j] = co;
                t2c[j] = cosr(pr * (1.0f / 256.0f));
                t2s[j] = sinr(pr * (1.0f / 256.0f));
            }
            f2re = pk8(c); f2im = pk8(s);
        }
        const rsrc_t rw = mkrs(wpl ? P.wi : P.wr, 2048 * 642);
        u32x4 wv[2][8];
#pragma unroll
        for (int it = 0; it < 2; ++it) {
            const int task = min((t & 127) + 128 * it, 159), r = task / 5, ck = task - 5 * r;
#pragma unroll
            for (int e = 0; e < 8; ++e)
                wv[it][e] = ld16(rw, ((((ka + 8 * ((r & 15) + 16 * (8 * (r >> 4) + e))) * 321 + colbase) * 2) & ~15u) + ck * 16, 0);
        }
        fence();
        p3_consts(smem + L_P3C, w, l, ai);
#pragma unroll
        for (int it = 0; it < 2; ++it) {
            const int task = min((t & 127) + 128 * it, 159), r = task / 5, ck = task - 5 * r;
#pragma unroll
            for (int c = 0; c < 8; ++c) {
                const int col = 8 * ck + c - dl, m = col * 2 + wpl, d = c >> 1;
                const u32x4* x = wv[it];
                const u32x4 o = (c & 1) ? u32x4{permhi(x[0][d], x[1][d]), permhi(x[2][d], x[3][d]), permhi(x[4][d], x[5][d]), permhi(x[6][d], x[7][d])}
                                        : u32x4{permlo(x[0][d], x[1][d]), permlo(x[2][d], x[3][d]), permlo(x[4][d], x[5][d]), permlo(x[6][d], x[7][d])};
                const int dst = ((u32)col < 28u) ? L_W + m * 512 + ((((r & 15) * 2 + (r >> 4)) ^ ((m >> 3) & 7)) * 16) : L_DUMP + l * 16;
                *(u32x4*)(smem + dst) = o;
            }
        }
    } else {
        {
            float c[8], s[8];
#pragma unroll
            for (int j = 0; j < 8; ++j) {
                const float a = (float)(8 * (g & 1) + j) * (float)i16 * 0.0625f;
                const float co = cosr(a), si = sinr(a);
                c[j] = (g < 2) ? co : si;
                s[j] = (g < 2) ? -si : co;
            }
            f1re = pk8(c); f1im = pk8(s);
#pragma unroll
            for (int j = 0; j < 4; ++j) {
                const float pr = (float)(4 * g + j) * (float)i16;
                const float co = cosr(pr * 0.0625f), si = sinr(pr * 0.0625f);
                c[j] = co; c[4 + j] = si;
                s[j] = -si; s[4 + j] = co;
                t2c[j] = cosr(pr * (1.0f / 256.0f));
                t2s[j] = sinr(pr * (1.0f / 256.0f));
            }
            f2re = pk8(c); f2im = pk8(s);
        }
        const int task = min(t - 256, 223), oc = task / 7, ck = task - 7 * oc;
        const u32 base = OFF_X1 + (8 * oc) * X1_ROW + ai * 128 + ck * 16;
        u32x4 v[8];
        p3_consts(smem + L_P3C, w, l, ai);
        drain();
        if (w == 4) {
            wait_flags<256>(rws, F_X, tag, l);
            if (l == 0) {
                *word = 1u;
                if (ai == 0) __hip_atomic_store((GAS u32*)(P.ws + OFF_SYNC), tag, __ATOMIC_RELAXED, __HIP_MEMORY_SCOPE_AGENT);
            }
        }
        for (u32 spins = 0; spins < SPIN_MAX && *word == 0u; ++spins) __builtin_amdgcn_s_sleep(1);
        for (u32 spins = 0; spins < SPIN_MAX; ++spins) {
            fence();
            bool ok = true;
#pragma unroll
            for (int e = 0; e < 8; ++e) v[e] = ld16(rws, base + e * X1_ROW, 1);
#pragma unroll
            for (int e = 0; e < 8; ++e) ok &= fresh4(v[e]);
            if (__all(ok)) break;
            __builtin_amdgcn_s_sleep(1);
        }
#pragma unroll
        for (int c = 0; c < 8; ++c) {
            const int el = 8 * ck + c, pl = el >= 28, m = (el - 28 * pl) * 2 + pl, d = c >> 1;
            const u32x4 o = (c & 1) ? u32x4{permhi(v[0][d], v[1][d]), permhi(v[2][d], v[3][d]), permhi(v[4][d], v[5][d]), permhi(v[6][d], v[7][d])}
                                    : u32x4{permlo(v[0][d], v[1][d]), permlo(v[2][d], v[3][d]), permlo(v[4][d], v[5][d]), permlo(v[6][d], v[7][d])};
            *(u32x4*)(smem + L_C + m * 512 + ((oc ^ ((m >> 3) & 7)) * 16)) = o;
        }
    }
    const u32x4 ire = {f2re.x, f2re.y, f2re.z ^ 0x80008000u, f2re.w ^ 0x80008000u};
    const u32x4 iim = {f2im.x ^ 0x80008000u, f2im.y ^ 0x80008000u, f2im.z, f2im.w};
    asm volatile("" :: "v"(pw));
    __syncthreads();
#pragma unroll
    for (int i = 0; i < 4; ++i) st16sc(rws, OFF_Y1 + ai * Y1_BLK + (t + 512 * i) * 16, u32x4{SENT, SENT, SENT, SENT});
    fence();
    {
        const int q = i16 >> 2, p = i16 & 3;
        const f32x4 zero = {0.f, 0.f, 0.f, 0.f};
        auto fetch = [&](int col, u32x4& a, u32x2& wre, u32x2& wim) {
            const int mi = col * 2 + (g >> 1), swi = (mi >> 3) & 7;
            const char* ib = smem + L_C + mi * 512 + (p & 1) * 8;
            const u32x2 a0 = trread(ib + (((2 * (8 * (g & 1) + q) + (p >> 1)) ^ swi) * 16));
            const u32x2 a1 = trread(ib + (((2 * (8 * (g & 1) + 4 + q) + (p >> 1)) ^ swi) * 16));
            a = u32x4{a0.x, a0.y, a1.x, a1.y};
            const int m0 = col * 2, sw = (m0 >> 3) & 7;
            const int woff = (((i16 * 2 + (g >> 1)) ^ sw) * 16) + (g & 1) * 8;
            wre = *(const u32x2*)(smem + L_W + m0 * 512 + woff);
            wim = *(const u32x2*)(smem + L_W + (m0 + 1) * 512 + woff);
        };
        auto columns = [&](auto nc, const u32x4* a, const u32x2* wre, const u32x2* wim, u32x2* ore, u32x2* oim) {
            constexpr int NC = decltype(nc)::value;
            f32x4 re[NC], im[NC];
            u32x4 d[NC];
#pragma unroll
            for (int c = 0; c < NC; ++c) { re[c] = mfma16(a[c], f1re, zero); im[c] = mfma16(a[c], f1im, zero); }
            __builtin_amdgcn_sched_barrier(0);
#pragma unroll
            for (int c = 0; c < NC; ++c) {
                float x[8];
#pragma unroll
                for (int i = 0; i < 4; ++i) {
                    x[i] = re[c][i] * t2c[i] + im[c][i] * t2s[i];
                    x[4 + i] = im[c][i] * t2c[i] - re[c][i] * t2s[i];
                }
                d[c] = pk8(x);
            }
            __builtin_amdgcn_sched_barrier(0);
#pragma unroll
            for (int c = 0; c < NC; ++c) { re[c] = mfma16(f2re, d[c], zero); im[c] = mfma16(f2im, d[c], zero); }
            __builtin_amdgcn_sched_barrier(0);
#pragma unroll
            for (int c = 0; c < NC; ++c) {
                float x[8];
#pragma unroll
                for (int i = 0; i < 4; ++i) {
                    const float a_ = (i & 1) ? bfhi(wre[c][i >> 1]) : bflo(wre[c][i >> 1]);
                    const float b_ = (i & 1) ? bfhi(wim[c][i >> 1]) : bflo(wim[c][i >> 1]);
                    x[i] = re[c][i] * a_ - im[c][i] * b_;
                    x[4 + i] = re[c][i] * b_ + im[c][i] * a_;
                }
                d[c] = pk8(x);
            }
            __builtin_amdgcn_sched_barrier(0);
#pragma unroll
            for (int c = 0; c < NC; ++c) { re[c] = mfma16(d[c], ire, zero); im[c] = mfma16(d[c], iim, zero); }
            __builtin_amdgcn_sched_barrier(0);
#pragma unroll
            for (int c = 0; c < NC; ++c) {
                float x[8];
#pragma unroll
                for (int i = 0; i < 4; ++i) {
                    x[i] = re[c][i] * t2c[i] - im[c][i] * t2s[i];
                    x[4 + i] = im[c][i] * t2c[i] + re[c][i] * t2s[i];
                }
                d[c] = pk8(x);
            }
            __builtin_amdgcn_sched_barrier(0);
#pragma unroll
            for (int c = 0; c < NC; ++c) { re[c] = mfma16(d[c], ire, zero); im[c] = mfma16(d[c], iim, zero); }
            __builtin_amdgcn_sched_barrier(0);
#pragma unroll
            for (int c = 0; c < NC; ++c) {
                ore[c] = u32x2{pk(re[c][0], re[c][1]), pk(re[c][2], re[c][3])};
                oim[c] = u32x2{pk(im[c][0], im[c][1]), pk(im[c][2], im[c][3])};
            }
        };
        auto put = [&](int col, const u32x2 ore, const u32x2 oim) {
            const int m0 = col * 2, sw = (m0 >> 3) & 7;
            const int ooff = (((2 * i16 + (g >> 1)) ^ sw) * 16) + (g & 1) * 8;
            *(u32x2*)(smem + L_C + m0 * 512 + ooff) = ore;
            *(u32x2*)(smem + L_C + (m0 + 1) * 512 + ooff) = oim;
        };
        u32x4 a[4];
        u32x2 wre[4], wim[4], ore[4], oim[4];
        if (w < 4) {
#pragma unroll
            for (int c = 0; c < 4; ++c) fetch(w + 8 * c, a[c], wre[c], wim[c]);
            columns(ic<4>{}, a, wre, wim, ore, oim);
#pragma unroll
            for (int c = 0; c < 4; ++c) put(w + 8 * c, ore[c], oim[c]);
        } else {
#pragma unroll
            for (int c = 0; c < 3; ++c) fetch(w + 8 * c, a[c], wre[c], wim[c]);
            columns(ic<3>{}, a, wre, wim, ore, oim);
#pragma unroll
            for (int c = 0; c < 3; ++c) put(w + 8 * c, ore[c], oim[c]);
        }
    }
    drain();
    __syncthreads();
    if (t == 0) raise_flag(P.ws, F_Y, ai, tag);
    {
        const int q = i16 >> 2, p = i16 & 3;
        const int G = 2 * w + (g >> 1), pl = g & 1;
        u32x2 v[7];
#pragma unroll
        for (int it = 0; it < 7; ++it) {
            const int m = (4 * it + q) * 2 + pl;
            v[it] = trread(smem + L_C + m * 512 + (((2 * G + (p >> 1)) ^ ((m >> 3) & 7)) * 16) + (p & 1) * 8);
        }
#pragma unroll
        for (int it = 0; it < 7; ++it) {
            const int o = pl * 56 + it * 8;
            *(u32x2*)(smem + L_W + (16 * G + i16) * 128 + (((o >> 4) ^ (i16 & 7)) << 4) + (o & 8)) = v[it];
        }
    }
    __syncthreads();
#pragma unroll
    for (int i = 0; i < 4; ++i) {
        const int m = t + 512 * i;
        st16sc(rws, OFF_Y1 + ai * Y1_BLK + m * 16, sane(*(const u32x4*)(smem + L_W + (m >> 3) * 128 + (((m & 7) ^ ((m >> 3) & 7)) << 4))));
    }

    u32x4 C[2], S[2], nsS[2], sC[2], F[2];
    float twc[8], tws[8];
    u32x2 fa[3];
    {
        const char* tb = smem + L_P3C;
        const u32 sgn = (g >= 2) ? 0x80008000u : 0u;
#pragma unroll
        for (int tau = 0; tau < 2; ++tau) {
            C[tau] = *(const u32x4*)(tb + tau * 1024 + l * 16);
            S[tau] = *(const u32x4*)(tb + (2 + tau) * 1024 + l * 16);
            nsS[tau] = S[tau] ^ (sgn ^ 0x80008000u);
            sC[tau] = C[tau] ^ sgn;
            const f32x4 a = *(const f32x4*)(tb + (4 + tau) * 1024 + l * 16), bq = *(const f32x4*)(tb + (6 + tau) * 1024 + l * 16);
#pragma unroll
            for (int i = 0; i < 4; ++i) { twc[4 * tau + i] = a[i]; tws[4 * tau + i] = bq[i]; }
            F[tau] = *(const u32x4*)(tb + (8 + tau) * 1024 + l * 16);
        }
#pragma unroll
        for (int j = 0; j < 3; ++j) fa[j] = *(const u32x2*)(tb + 10240 + j * 512 + l * 8);
    }
    drain();
    if (w == 7) {
        wait_flags<96>(rws, F_Y, tag, l);
        if (l == 0) *word = 2u;
    }
    {
        u32 off[4];
#pragma unroll
        for (int i = 0; i < 4; ++i) {
            const int m = min(t + 512 * i, nset * 672 - 1), j = (m >= 672) + (m >= 1344), mm = m - 672 * j, ab = mm / 7;
            off[i] = OFF_Y1 + ab * Y1_BLK + (ai + 96 * j) * 128 + (mm - 7 * ab) * 16;
        }
        for (u32 spins = 0; spins < SPIN_MAX && *word != 2u; ++spins) __builtin_amdgcn_s_sleep(1);
        u32x4 v[4];
        for (u32 spins = 0; spins < SPIN_MAX; ++spins) {
            fence();
#pragma unroll
            for (int i = 0; i < 4; ++i) v[i] = ld16(rws, off[i], 1);
            if (__all(fresh4(v[0]) && fresh4(v[1]) && fresh4(v[2]) && fresh4(v[3]))) break;
            __builtin_amdgcn_s_sleep(1);
        }
#pragma unroll
        for (int i = 0; i < 4; ++i) *(u32x4*)(smem + L_X + min(t + 512 * i, nset * 672 - 1) * 16) = v[i];
    }
    __syncthreads();
    {
        const int q = i16 >> 2, p = i16 & 3;
        const int kap = 2 * g + (q >> 1), pl = q & 1;
        u32x2 bfr[3][3];
#pragma unroll
        for (int it = 0; it < 3; ++it) {
            const int T = min(w + 8 * it, 20);
            const int k = min(16 * T + 4 * p, 320);
            const int cgk = min((k * 2341) >> 16, 11), idx = k - min(28 * cgk, 296);
            const int src = L_X + (kap * 12 + cgk) * 112 + pl * 56 + idx * 2;
#pragma unroll
            for (int j = 0; j < 3; ++j) bfr[it][j] = trread(smem + src + j * 10752);
        }
        u32x2 dv[3][3];
#pragma unroll
        for (int it = 0; it < 3; ++it)
#pragma unroll
            for (int j = 0; j < 3; ++j) {
                const f32x4 d = mfma16k16(fa[j], bfr[it][j], f32x4{0.f, 0.f, 0.f, 0.f});
                dv[it][j] = u32x2{pk(d[0], d[1]), pk(d[2], d[3])};
            }
#pragma unroll
        for (int it = 0; it < 3; ++it)
#pragma unroll
            for (int j = 0; j < 3; ++j)
                *(u32x2*)(smem + L_S + j * S_SZ + (16 * min(w + 8 * it, 20) + i16) * S_K + g * 8) = dv[it][j];
    }
    __syncthreads();
    {
        const int k2 = i16 <= 10 ? i16 : 10;
        const int k0 = k2 + 160 * g;
        const int kofs = ((g < 2) ? k0 : 640 - k0) * S_K + w * 4, kstep = (g < 2) ? 20 * S_K : -20 * S_K;
        const rsrc_t ro = mkrs(P.out, 2048 * 2560);
        u32 dd[3][8];
#pragma unroll
        for (int j = 0; j < 3; ++j)
#pragma unroll
            for (int e = 0; e < 8; ++e) dd[j][e] = *(const u32*)(smem + L_S + j * S_SZ + kofs + e * kstep);
        const f32x4 zero = {0.f, 0.f, 0.f, 0.f};
        f32x4 vre[3][2], vim[3][2];
#pragma unroll
        for (int j = 0; j < 3; ++j) {
            const u32x4 are = {permlo(dd[j][0], dd[j][1]), permlo(dd[j][2], dd[j][3]), permlo(dd[j][4], dd[j][5]), permlo(dd[j][6], dd[j][7])};
            const u32x4 aim = {permhi(dd[j][0], dd[j][1]), permhi(dd[j][2], dd[j][3]), permhi(dd[j][4], dd[j][5]), permhi(dd[j][6], dd[j][7])};
#pragma unroll
            for (int tau = 0; tau < 2; ++tau) {
                vre[j][tau] = mfma16(aim, nsS[tau], mfma16(are, C[tau], zero));
                vim[j][tau] = mfma16(aim, sC[tau], mfma16(are, S[tau], zero));
            }
        }
        __builtin_amdgcn_sched_barrier(0);
        u32x4 va[3][2];
#pragma unroll
        for (int j = 0; j < 3; ++j)
#pragma unroll
            for (int tau = 0; tau < 2; ++tau) {
                float pr[8];
#pragma unroll
                for (int i = 0; i < 4; ++i) {
                    pr[i] = vre[j][tau][i] * twc[4 * tau + i] - vim[j][tau][i] * tws[4 * tau + i];
                    pr[4 + i] = vim[j][tau][i] * twc[4 * tau + i] + vre[j][tau][i] * tws[4 * tau + i];
                }
                va[j][tau] = pk8(pr);
            }
        __builtin_amdgcn_sched_barrier(0);
        f32x4 y[3][2][2];
#pragma unroll
        for (int j = 0; j < 3; ++j)
#pragma unroll
            for (int dl = 0; dl < 2; ++dl) {
                y[j][dl][0] = mfma16(va[j][0], F[dl], zero);
                y[j][dl][1] = mfma16(va[j][1], F[dl], zero);
            }
        __builtin_amdgcn_sched_barrier(0);
#pragma unroll
        for (int j = 0; j < 3; ++j) {
            const u32 orow = (ai + 96 * j + 256 * w) * 2560;
#pragma unroll
            for (int dl = 0; dl < 2; ++dl) {
                u32 a0 = pk(y[j][dl][0][0], y[j][dl][0][1]), a1 = pk(y[j][dl][0][2], y[j][dl][0][3]);
                u32 b0 = pk(y[j][dl][1][0], y[j][dl][1][1]), b1 = pk(y[j][dl][1][2], y[j][dl][1][3]);
                auto s0 = __builtin_amdgcn_permlane16_swap(a0, b0, false, false);
                auto s1 = __builtin_amdgcn_permlane16_swap(a1, b1, false, false);
                const int d2 = 16 * dl + i16;
                const u32 off = (d2 < 20 && j < nset) ? orow + (32 * d2 + (g & 1) * 16 + (g >> 1) * 8) * 2 : 0x80000000u;
                __builtin_amdgcn_raw_buffer_store_b128(u32x4{s0[0], s1[0], s0[1], s1[1]}, ro, off, 0, 0);
            }
        }
    }
}

DI void role_b(const Args& P, char* smem, const int t, const int w, const int chb) {
    const int l = t & 63, g = l >> 4, i16 = l & 15;
    const int ch = w & 3, rh = w >> 2;
    const rsrc_t rws = mkrs(P.ws, WS_USED);
    Start st;
    p1_start(P, st, t, w);
    p1_consts(smem, t, w);
    const u32x2 fb = p1_rows(P, smem, st, t, w);
    u32 pv[12], ldt = 0, pu = 0;
    const rsrc_t ru = mkrs(P.u, 2048 * 1280);
    if (w < 4) {
        const u32 nb = 640 * 128 * 2;
        const rsrc_t r0 = mkrs(P.Are, nb), r1 = mkrs(P.Aim, nb), r2 = mkrs(P.Bre, nb), r3 = mkrs(P.Bim, nb), r4 = mkrs(P.Cre, nb), r5 = mkrs(P.Cim, nb);
#pragma unroll
        for (int h = 0; h < 2; ++h) {
            const u32 off = ((4 * chb + w) * 128 + 64 * h + l) * 2;
            pv[6 * h + 0] = ld2(r0, off); pv[6 * h + 1] = ld2(r1, off); pv[6 * h + 2] = ld2(r2, off);
            pv[6 * h + 3] = ld2(r3, off); pv[6 * h + 4] = ld2(r4, off); pv[6 * h + 5] = ld2(r5, off);
        }
        ldt = ld2(mkrs(P.logdt, 1280), (4 * chb + w) * 2);
    }
    const u32 dks = ld2(mkrs(P.dskip, 1280), (4 * chb + ch) * 2);
    if (w < 5) {
        const int xcd = blockIdx.x & 7, jx = blockIdx.x >> 3;
        const int b0 = (160 * xcd) & ~127, nl = (160 * xcd + 160 - b0 + 127) >> 7;
        const int m = min(t, 103 * 3 - 1), r = m / 3;
        pu = __builtin_amdgcn_raw_buffer_load_b32(ru, min(103 * jx + r, 2047) * 1280 + b0 + min(m - 3 * r, nl - 1) * 128, 0, 0);
    }
    const u32 pw = warm_w(P, t, w);
    fence();
    p1_finish(P, smem, fb, t, w);
    if (w >= 4) {
        const int tt = t - 256;
        fence();
        u32x2 gv[8];
#pragma unroll
        for (int e = 0; e < 8; ++e) gv[e] = ld8(ru, (8 * tt + e) * 1280 + chb * 8, 0);
        fence();
        if (tt < 128) *(u32x4*)(smem + L_U + (tt >> 5) * PB_U + (tt & 31) * 16) = u32x4{0u, 0u, 0u, 0u};
        else if (tt < 144) {
            const int v = tt - 128;
            *(u32x4*)(smem + L_K + (v >> 2) * PB_KS + ((v >> 1) & 1) * PB_K1 + 4096 + (v & 1) * 16) = u32x4{0u, 0u, 0u, 0u};
        }
        u32x4 o[4];
        tr8x4(gv, o);
#pragma unroll
        for (int c = 0; c < 4; ++c) *(u32x4*)(smem + L_U + c * PB_U + 512 + tt * 16) = o[c];
    } else {
        char* sc = smem + w * KG_SZ;
        const float dt = rbf(expf(bf2f(ldt)));
        f32x4 kacc[3];
#pragma unroll
        for (int T = 0; T < 3; ++T) kacc[T] = f32x4{0.f, 0.f, 0.f, 0.f};
#pragma unroll 1
        for (int h = 0; h < 2; ++h) {
            const float lre = dt * bf2f(h ? pv[6] : pv[0]), lim = dt * bf2f(h ? pv[7] : pv[1]) * 0.15915494309189535f;
            const cf c = cmul(cf{bf2f(h ? pv[10] : pv[4]), bf2f(h ? pv[11] : pv[5])}, cf{dt * bf2f(h ? pv[8] : pv[2]), dt * bf2f(h ? pv[9] : pv[3])});
            char* rowp = sc + l * 4;
            const cf z1 = cexpk(lre, lim, 1.f), z8 = cexpk(lre, lim, 8.f), z128 = cexpk(lre, lim, 128.f);
            const cf z2 = cmul(z1, z1), z4 = cmul(z2, z2), z6 = cmul(z2, z4);
            const cf z8c = {z8.x, -z8.y};
            cf v = {1.f, 0.f}, r0 = c, r1 = cmul(c, z128);
            cf q = cmul(r1, z128);
#pragma unroll
            for (int j = 0; j < 8; ++j) {
                *(u32*)(rowp + KG_B + (2 * j) * KG_RS) = pk(v.x, v.y);
                v = cmul(v, z8c);
                *(u32*)(rowp + KG_B + (2 * j + 1) * KG_RS) = pk(v.x, v.y);
                v = cmul(v, z8c);
                *(u32*)(rowp + j * KG_RS) = pkc(r0);
                *(u32*)(rowp + (8 + j) * KG_RS) = pkc(r1);
                r0 = cmul(r0, z1);
                r1 = cmul(r1, z1);
#pragma unroll
                for (int e = 0; e < 2; ++e) {
                    const int hh = 2 + 2 * j + e;
                    if (hh < 4) {
                        const int r = 16 + (hh - 2) * 4;
                        *(u32*)(rowp + r * KG_RS) = pkc(q);
                        *(u32*)(rowp + (r + 1) * KG_RS) = pkc(cmul(q, z2));
                        *(u32*)(rowp + (r + 2) * KG_RS) = pkc(cmul(q, z4));
                        *(u32*)(rowp + (r + 3) * KG_RS) = pkc(cmul(q, z6));
                    } else if (hh < 8) {
                        const int r = 24 + (hh - 4) * 2;
                        *(u32*)(rowp + r * KG_RS) = pkc(q);
                        *(u32*)(rowp + (r + 1) * KG_RS) = pkc(cmul(q, z4));
                    } else if (hh <= 16) {
                        *(u32*)(rowp + (32 + hh - 8) * KG_RS) = pkc(q);
                    }
                    if (hh < 16) q = cmul(q, z128);
                }
            }
            u32x4 bf[4], af[3][4];
#pragma unroll
            for (int ks = 0; ks < 4; ++ks) {
                bf[ks] = *(const u32x4*)(sc + KG_B + i16 * KG_RS + g * 16 + ks * 64);
#pragma unroll
                for (int T = 0; T < 3; ++T) af[T][ks] = *(const u32x4*)(sc + (16 * T + i16) * KG_RS + g * 16 + ks * 64);
            }
#pragma unroll
            for (int T = 0; T < 3; ++T)
#pragma unroll
                for (int ks = 0; ks < 4; ++ks) kacc[T] = mfma16(af[T][ks], bf[ks], kacc[T]);
        }
        char* tbl = smem + L_T + w * KG_T;
#pragma unroll
        for (int T = 0; T < 3; ++T)
#pragma unroll
            for (int i = 0; i < 4; ++i) {
                const int row = 16 * T + 4 * g + i;
                int e;
                if (T == 0) e = 128 * (row >> 3) + (row & 7);
                else if (T == 1) e = (g < 2) ? 256 + 128 * g + 2 * i : 512 + 256 * (g - 2) + 128 * (i >> 1) + 4 * (i & 1);
                else e = 1024 + 128 * (row - 32);
                const bool ok = row < 40 || (row == 40 && i16 == 0);
                *(u16*)(tbl + (ok ? (e + 8 * i16) * 2 : 4110)) = (u16)(pk(kacc[T][i], kacc[T][i]) & 0xffffu);
            }
        u32 v[4][8], vm[4];
#pragma unroll
        for (int it = 0; it < 4; ++it) {
            const int tt = l + 64 * it, a = 255 - tt;
            const u32 code = (a < 32) ? 0x76543210u : (a < 64) ? 0x86444200u : (a < 128) ? 0x88444000u : ((a & 1) ? 0x88880000u : 0x88800000u);
            const int lastoff = (a <= 32) ? 7 : 8;
            const char* tb = tbl + 16 * a;
#pragma unroll
            for (int e = 0; e < 8; ++e) v[it][e] = *(const u16*)(tb + 2 * ((code >> (4 * e)) & 15));
            vm[it] = *(const u16*)(tb + ((a == 0) ? 0 : 2 * lastoff - 16));
            vm[it] = (a == 0) ? 0u : vm[it];
        }
#pragma unroll
        for (int it = 0; it < 4; ++it) {
            char* kb = smem + L_K + w * PB_KS + (l + 64 * it) * 16;
            const u32* x = v[it];
            *(u32x4*)(kb) = u32x4{x[7] | (x[6] << 16), x[5] | (x[4] << 16), x[3] | (x[2] << 16), x[1] | (x[0] << 16)};
            *(u32x4*)(kb + PB_K1) = u32x4{x[6] | (x[5] << 16), x[4] | (x[3] << 16), x[2] | (x[1] << 16), x[0] | (vm[it] << 16)};
        }
    }
    asm volatile("" :: "v"(pw ^ pu));
    __syncthreads();
    const int mu = g >> 1, hp = g & 1;
    const int x0 = 2047 - 16 * mu - i16 + 8 * hp;
    const char* ap = smem + L_K + ch * PB_KS + ((x0 & 1) ? PB_K1 + (x0 - 1) * 2 : x0 * 2);
    const char* bp = smem + L_U + ch * PB_U + (256 + 16 * (i16 - mu) + 8 * hp) * 2;
    f32x4 acc[8];
#pragma unroll
    for (int c = 0; c < 8; ++c) acc[c] = f32x4{0.f, 0.f, 0.f, 0.f};
    {
        u32x4 A[2][8], B[2][8];
        const char* ap0 = ap - 256 * rh;
        const char* bp0 = bp - 256 * rh;
#pragma unroll
        for (int p = 0; p < 8; ++p) {
            const u32* a = (const u32*)(ap0 - 512 * p);
            B[0][p] = *(const u32x4*)(bp0 + 512 * p);
            A[0][p] = u32x4{a[0], a[1], a[2], a[3]};
        }
#pragma unroll
        for (int r = 0; r < 4; ++r) {
            __builtin_amdgcn_sched_barrier(0);
            if (r < 3) {
#pragma unroll
                for (int p = 0; p < 8; ++p) {
                    const u32* a = (const u32*)(ap0 - 64 * (r + 1) - 512 * p);
                    B[(r + 1) & 1][p] = *(const u32x4*)(bp0 - 64 * (r + 1) + 512 * p);
                    A[(r + 1) & 1][p] = u32x4{a[0], a[1], a[2], a[3]};
                }
            }
#pragma unroll
            for (int p = 0; p < 8; ++p)
#pragma unroll
                for (int q = 0; q + p < 8; ++q) acc[p + q] = mfma16(A[r & 1][p], B[r & 1][q], acc[p + q]);
            if (r < 3) {
#pragma unroll
                for (int k = 0; k < 12; ++k) {
                    __builtin_amdgcn_sched_group_barrier(0x008, 3, 0);
                    __builtin_amdgcn_sched_group_barrier(0x100, 2, 0);
                }
            }
        }
        __builtin_amdgcn_sched_barrier(0);
    }
    char* rb = smem + L_R;
#pragma unroll
    for (int c = 0; c < 4; ++c) *(f32x4*)(rb + (w * 4 + c) * 1024 + l * 16) = rh ? acc[c] : acc[4 + c];
    __syncthreads();
    char* yi = smem + L_Y;
    const float dsk = bf2f(dks);
    {
        f32x4 o[4];
        u32x2 uu[4];
#pragma unroll
        for (int c = 0; c < 4; ++c) {
            o[c] = *(const f32x4*)(rb + ((w ^ 4) * 4 + c) * 1024 + l * 16);
            uu[c] = *(const u32x2*)(smem + L_U + ch * PB_U + (256 + 256 * (4 * rh + c) + 16 * i16 + 4 * g) * 2);
        }
        u32x2 res[4];
#pragma unroll
        for (int c = 0; c < 4; ++c) {
            const f32x4 mine = rh ? acc[4 + c] : acc[c];
            float v[4];
#pragma unroll
            for (int i = 0; i < 4; ++i) {
                const float uf = (i & 1) ? bfhi(i < 2 ? uu[c].x : uu[c].y) : bflo(i < 2 ? uu[c].x : uu[c].y);
                v[i] = rbf(mine[i] + o[c][i]) + rbf(uf * dsk);
            }
            res[c] = u32x2{pk(v[0], v[1]), pk(v[2], v[3])};
        }
#pragma unroll
        for (int c = 0; c < 4; ++c) *(u32x2*)(yi + ch * PB_Y + (64 * (4 * rh + c) + 4 * i16 + (g ^ ((i16 >> 2) & 3))) * 8) = res[c];
    }
    __syncthreads();
    {
        const rsrc_t ro = mkrs(P.out, 2048 * 2560);
        const int q = i16 >> 2, p = i16 & 3;
        u32x2 v[4];
#pragma unroll
        for (int i = 0; i < 4; ++i) {
            const int sg = ((256 * w + 64 * i + 16 * g) >> 2) + p;
            v[i] = trread(yi + q * PB_Y + (sg ^ ((sg >> 4) & 3)) * 8);
        }
#pragma unroll
        for (int i = 0; i < 4; ++i)
            __builtin_amdgcn_raw_buffer_store_b64(v[i], ro, (256 * w + 64 * i + 16 * g + i16) * 2560 + 1280 + chb * 8, 0, 0);
    }
}

__global__ void __launch_bounds__(512, 2) fused_kernel(Args P) {
    extern __shared__ __attribute__((aligned(16))) char smem[];
    const int t = threadIdx.x;
    const int w = __builtin_amdgcn_readfirstlane(t >> 6);
    const int b = blockIdx.x, xcd = b & 7, jx = b >> 3;
    if (jx < 20) role_b(P, smem, t, w, 20 * xcd + jx);
    else role_a(P, smem, t, w, xcd, jx - 20);
}

extern "C" void kernel_launch(void* const* d_in, const int* in_sizes, int n_in,
                              void* d_out, int out_size, void* d_ws, size_t ws_size,
                              hipStream_t stream) {
    Args a;
    a.x = (const u16*)d_in[0];
    a.wr = (const u16*)d_in[1];
    a.wi = (const u16*)d_in[2];
    a.u = (const u16*)d_in[3];
    a.Are = (const u16*)d_in[4];
    a.Aim = (const u16*)d_in[5];
    a.Bre = (const u16*)d_in[6];
    a.Bim = (const u16*)d_in[7];
    a.Cre = (const u16*)d_in[8];
    a.Cim = (const u16*)d_in[9];
    a.logdt = (const u16*)d_in[10];
    a.dskip = (const u16*)d_in[11];
    a.ws = (char*)d_ws;
    a.out = (u16*)d_out;
    (void)hipFuncSetAttribute((const void*)fused_kernel, hipFuncAttributeMaxDynamicSharedMemorySize, LDS_BYTES);
    fused_kernel<<<256, 512, LDS_BYTES, stream>>>(a);
}
```

```cpp
#include <hip/hip_runtime.h>
#include <hip/hip_bf16.h>

typedef unsigned short u16;
typedef unsigned int u32;
typedef __attribute__((ext_vector_type(2))) __bf16 bf16x2;
typedef __attribute__((ext_vector_type(8))) __bf16 bf16x8;
typedef __attribute__((ext_vector_type(2))) float f32x2;
typedef __attribute__((ext_vector_type(4))) float f32x4;
typedef __attribute__((ext_vector_type(4))) u32 u32x4;
typedef __attribute__((ext_vector_type(2))) u32 u32x2;
typedef __attribute__((ext_vector_type(4))) short s16x4;
typedef __amdgpu_buffer_rsrc_t rsrc_t;
#define LAS __attribute__((address_space(3)))
#define GAS __attribute__((address_space(1)))
#define DI __device__ __forceinline__
#define SC1 16

constexpr u32 X1_ROW = 12288;
constexpr u32 OFF_X1 = 0;
constexpr u32 Y1_BLK = 32768;
constexpr u32 OFF_Y1 = 256 * X1_ROW;
constexpr u32 OFF_SYNC = OFF_Y1 + 96 * Y1_BLK;
constexpr u32 F_X = 1024;
constexpr u32 F_Y = F_X + 1024;
constexpr u32 WS_USED = OFF_SYNC + F_Y + 1024;
constexpr u32 SENT = 0xFFFFFFFFu;
constexpr u32 SPIN_MAX = 1u << 16;
constexpr int PROBE_ID = 4, PROBE_WAVE = 4, AMP = 1;

constexpr int S_K = 40, S_SZ = 336 * S_K;
constexpr int P_X = 80448;
constexpr int P_S = P_X + 12800;
constexpr int P_C = 32768;
constexpr int L_W = 0;
constexpr int L_C = L_W + 32768;
constexpr int L_DUMP = L_C + 28672;
constexpr int L_X = L_DUMP + 1024;
constexpr int L_S = L_X + 3 * 10752;
constexpr int L_P3C = L_S + 3 * S_SZ;
constexpr int KG_RS = 272, KG_B = 41 * KG_RS, KG_SZ = 57 * KG_RS;
constexpr int PB_U = 4608, PB_K1 = 4160, PB_KS = 8320, PB_Y = 4128, KG_T = 4112;
constexpr int L_U = 4 * KG_SZ;
constexpr int L_K = P_S + S_SZ;
constexpr int L_T = L_K + 4 * PB_KS;
constexpr int L_R = 0;
constexpr int L_Y = L_R + 32768;
constexpr int L_WORD = L_T + 4 * KG_T;
constexpr int LDS_BYTES = L_WORD + 16;
static_assert(L_P3C + 11776 <= L_WORD && L_U + 4 * PB_U <= P_X && L_Y + 4 * PB_Y <= L_U && LDS_BYTES <= 163840, "LDS map");

#define STAMP_DECL unsigned long long pr0_ = __builtin_amdgcn_s_memrealtime(), pr1_ = pr0_; int prn_ = 0;
#define STAMP() do { __builtin_amdgcn_sched_barrier(0); if (prn_ == PROBE_ID) pr1_ = __builtin_amdgcn_s_memrealtime(); ++prn_; __builtin_amdgcn_sched_barrier(0); } while (0)
#define STAMP_OUT(ws) do { if (PROBE_ROLE_OK && w == PROBE_WAVE) { const unsigned long long n_ = __builtin_amdgcn_s_memrealtime(); \
    while (__builtin_amdgcn_s_memrealtime() - n_ < (pr1_ - pr0_) * AMP) __builtin_amdgcn_s_sleep(1); } } while (0)

DI u32 pk(float a, float b) {
    f32x2 v = {a, b};
    return __builtin_bit_cast(u32, __builtin_convertvector(v, bf16x2));
}
DI u32x4 pk8(const float* v) { return u32x4{pk(v[0], v[1]), pk(v[2], v[3]), pk(v[4], v[5]), pk(v[6], v[7])}; }
DI float bflo(u32 v) { return __uint_as_float(v << 16); }
DI float bfhi(u32 v) { return __uint_as_float(v & 0xffff0000u); }
DI float bf2f(u32 v) { return __uint_as_float(v << 16); }
DI float rbf(float a) { return bflo(pk(a, a)); }
DI float cosr(float rev) { return __builtin_amdgcn_cosf(rev); }
DI float sinr(float rev) { return __builtin_amdgcn_sinf(rev); }
DI f32x4 mfma16(u32x4 a, u32x4 b, f32x4 c) {
    return __builtin_amdgcn_mfma_f32_16x16x32_bf16(__builtin_bit_cast(bf16x8, a), __builtin_bit_cast(bf16x8, b), c, 0, 0, 0);
}
DI f32x4 mfma16k16(u32x2 a, u32x2 b, f32x4 c) {
    return __builtin_amdgcn_mfma_f32_16x16x16bf16_1k(__builtin_bit_cast(s16x4, a), __builtin_bit_cast(s16x4, b), c, 0, 0, 0);
}
DI u32x2 trread(const char* p) {
    return __builtin_bit_cast(u32x2, __builtin_amdgcn_ds_read_tr16_b64_v4i16((LAS s16x4*)(p)));
}
DI u32 permlo(u32 a, u32 b) { return __builtin_amdgcn_perm(b, a, 0x05040100u); }
DI u32 permhi(u32 a, u32 b) { return __builtin_amdgcn_perm(b, a, 0x07060302u); }
DI rsrc_t mkrs(const void* p, u32 bytes) { return __builtin_amdgcn_make_buffer_rsrc((void*)p, 0, bytes, 0x00020000); }
DI u32x4 ld16(rsrc_t r, u32 off, int aux) { return aux ? __builtin_amdgcn_raw_buffer_load_b128(r, off, 0, SC1) : __builtin_amdgcn_raw_buffer_load_b128(r, off, 0, 0); }
DI u32x2 ld8(rsrc_t r, u32 off, int aux) { return aux ? __builtin_amdgcn_raw_buffer_load_b64(r, off, 0, SC1) : __builtin_amdgcn_raw_buffer_load_b64(r, off, 0, 0); }
DI u32 ld2(rsrc_t r, u32 off) { return __builtin_amdgcn_raw_buffer_load_b16(r, off, 0, 0); }
DI void st16sc(rsrc_t r, u32 off, u32x4 v) { __builtin_amdgcn_raw_buffer_store_b128(v, r, off, 0, SC1); }
DI u32x4 sane(u32x4 v) { return u32x4{min(v.x, SENT - 1), min(v.y, SENT - 1), min(v.z, SENT - 1), min(v.w, SENT - 1)}; }
DI bool fresh4(u32x4 v) { return max(max(v.x, v.y), max(v.z, v.w)) != SENT; }
DI void fence() { asm volatile("" ::: "memory"); }
DI void drain() { asm volatile("s_waitcnt vmcnt(0)" ::: "memory"); }
DI void tr8x4(const u32x2* v, u32x4* o) {
    o[0] = u32x4{permlo(v[0].x, v[1].x), permlo(v[2].x, v[3].x), permlo(v[4].x, v[5].x), permlo(v[6].x, v[7].x)};
    o[1] = u32x4{permhi(v[0].x, v[1].x), permhi(v[2].x, v[3].x), permhi(v[4].x, v[5].x), permhi(v[6].x, v[7].x)};
    o[2] = u32x4{permlo(v[0].y, v[1].y), permlo(v[2].y, v[3].y), permlo(v[4].y, v[5].y), permlo(v[6].y, v[7].y)};
    o[3] = u32x4{permhi(v[0].y, v[1].y), permhi(v[2].y, v[3].y), permhi(v[4].y, v[5].y), permhi(v[6].y, v[7].y)};
}
DI void raise_flag(char* ws, u32 set, int i, u32 tag) {
    __hip_atomic_store((GAS u32*)(ws + OFF_SYNC + set) + i, tag, __ATOMIC_RELAXED, __HIP_MEMORY_SCOPE_AGENT);
}
template <int NF>
DI void wait_flags(rsrc_t rws, u32 set, u32 tag, int l) {
    for (u32 spins = 0; spins < SPIN_MAX; ++spins) {
        fence();
        const u32x4 f = ld16(rws, OFF_SYNC + set + min(l, NF / 4 - 1) * 16, 1);
        if (__all(f.x == tag && f.y == tag && f.z == tag && f.w == tag)) break;
        __builtin_amdgcn_s_sleep(4);
    }
}

template <int N> struct ic { static constexpr int value = N; };
typedef f32x2 cf;
DI cf cmul(cf a, cf b) { return __builtin_elementwise_fma(cf{a.y, a.y}, cf{-b.y, b.x}, cf{a.x, a.x} * b); }
DI cf cexpk(float lre, float lim_rev, float k) {
    const float m = __builtin_amdgcn_exp2f(k * lre * 1.44269504088896341f);
    const float a = __builtin_amdgcn_fractf(k * lim_rev);
    return cf{m * cosr(a), m * sinr(a)};
}
DI u32 pkc(cf v) { return pk(v.x, v.y); }

struct Args {
    const u16 *x, *wr, *wi, *u, *Are, *Aim, *Bre, *Bim, *Cre, *Cim, *logdt, *dskip;
    char* ws;
    u16* out;
};

struct Start { u32x4 xv[2]; u32 gen; };
DI void p1_start(const Args& P, Start& st, const int t, const int w) {
    const int l = t & 63, b = blockIdx.x;
    const rsrc_t rws = mkrs(P.ws, WS_USED);
    st.gen = __builtin_amdgcn_raw_buffer_load_b32(rws, OFF_SYNC, 0, SC1);
    fence();
    const rsrc_t rx = mkrs(P.x, 2048 * 1280);
    const u32 ro = (b + 256 * w) * 1280;
    st.xv[0] = ld16(rx, ro + l * 16, 0);
    st.xv[1] = ld16(rx, ro + min(64 + l, 79) * 16, 0);
    fence();
#pragma unroll
    for (int i = 0; i < 2; ++i) st16sc(rws, OFF_X1 + b * X1_ROW + min(t + 512 * i, 767) * 16, u32x4{SENT, SENT, SENT, SENT});
    fence();
}
DI u32 warm_w(const Args& P, const int t, const int w) {
    const int l = t & 63, b = blockIdx.x;
    u32 pw = 0;
    if (w >= 6) {
        const int m = min(l, 47);
        pw = __builtin_amdgcn_raw_buffer_load_b32(mkrs(w == 6 ? P.wr : P.wi, 2048 * 642),
                                                  ((((b & 7) + 8 * (8 * (b >> 3) + m / 6)) * 642) & ~127u) + (m % 6) * 128, 0, 0);
    }
    return pw;
}
DI void p1_consts(char* smem, const int t, const int w) {
    const int l = t & 63, g = l >> 4, i16 = l & 15, b = blockIdx.x;
    if (t < 15) *(u32x2*)(smem + P_S + 321 * S_K + t * 8) = u32x2{0u, 0u};
    {
        float v[8];
        if (w < 4) {
#pragma unroll
            for (int j = 0; j < 8; ++j) {
                const int d = (j < 4) ? 4 * g + j : 16 + 4 * g + j - 4;
                const float c = cosr((float)d * (float)i16 * (w < 2 ? 0.05f : 0.03125f) - ((w & 1) ? 0.25f : 0.f));
                v[j] = (w >= 2) ? c : ((d < 20 && i16 <= 10) ? ((w & 1) ? -c : c) : 0.f);
            }
            *(u32x4*)(smem + P_C + w * 1024 + l * 16) = pk8(v);
        } else {
#pragma unroll
            for (int i = 0; i < 4; ++i)
                v[i] = cosr((float)(16 * (w & 1) + 4 * g + i) * (float)i16 * (1.0f / 640.0f) - ((w >= 6) ? 0.25f : 0.f));
            *(f32x4*)(smem + P_C + w * 1024 + l * 16) = f32x4{v[0], v[1], v[2], v[3]};
            if (w == 4) {
                const int kap = i16 >> 1, plo = i16 & 1;
#pragma unroll
                for (int e = 0; e < 4; ++e) {
                    const int a = 2 * g + (e >> 1), pl = e & 1;
                    const float c = cosr((float)kap * (float)(256 * a + b) * (1.0f / 2048.0f) - ((pl != plo) ? 0.25f : 0.f));
                    v[e] = (plo == 1 && pl == 0) ? -c : c;
                }
                *(u32x2*)(smem + P_C + 8192 + l * 8) = u32x2{pk(v[0], v[1]), pk(v[2], v[3])};
            }
        }
    }
    if (t == 0) *(u32x4*)(smem + L_WORD) = u32x4{0u, 0u, 0u, 0u};
    __syncthreads();
}
DI u32x2 p1_rows(const Args& P, char* smem, const Start& st, const int t, const int w) {
    const int l = t & 63, g = l >> 4, i16 = l & 15, b = blockIdx.x;
    const u32x4* xv = st.xv;
    const u32x4 f20c = *(const u32x4*)(smem + P_C + l * 16), f20s = *(const u32x4*)(smem + P_C + 1024 + l * 16);
    const u32x4 c32 = *(const u32x4*)(smem + P_C + 2048 + l * 16), s32 = *(const u32x4*)(smem + P_C + 3072 + l * 16);
    float twc[8], tws[8];
#pragma unroll
    for (int i = 0; i < 2; ++i) {
        const f32x4 a = *(const f32x4*)(smem + P_C + (4 + i) * 1024 + l * 16), bq = *(const f32x4*)(smem + P_C + (6 + i) * 1024 + l * 16);
#pragma unroll
        for (int e = 0; e < 4; ++e) { twc[4 * i + e] = a[e]; tws[4 * i + e] = bq[e]; }
    }
    const u32x2 fb = *(const u32x2*)(smem + P_C + 8192 + l * 8);
    const u32x4 ns32 = s32 ^ 0x80008000u, c32b = c32 ^ 0x80000000u, s32b = s32 ^ 0x80000000u, nc32b = c32b ^ 0x80008000u;
    {
        const int q = i16 >> 2, p = i16 & 3;
        const int d2a = 4 * g + q, d2b = (g == 0) ? 16 + q : d2a;
        const int offa = 64 * d2a + 8 * p, swa = ((d2a >> 2) & 1) << 5;
        const int offb = 64 * d2b + 8 * p, swb = ((d2b >> 2) & 1) << 5;
        char* xr = smem + P_X + w * 1280;
        *(u32x4*)(xr + ((l * 16) ^ (((l >> 4) & 1) << 5))) = xv[0];
        *(u32x4*)(xr + min(64 + l, 79) * 16) = xv[1];
        f32x4 tre[2], tim[2];
        const f32x4 zero = {0.f, 0.f, 0.f, 0.f};
        u32x4 xa[2];
#pragma unroll
        for (int tau = 0; tau < 2; ++tau) {
            const u32x2 a0 = trread(xr + offa + ((32 * tau) ^ swa));
            const u32x2 a1 = trread(xr + offb + ((32 * tau) ^ swb));
            xa[tau] = u32x4{a0.x, a0.y, a1.x, a1.y};
        }
#pragma unroll
        for (int tau = 0; tau < 2; ++tau) {
            tre[tau] = mfma16(xa[tau], f20c, zero);
            tim[tau] = mfma16(xa[tau], f20s, zero);
        }
        float pr[8], pi[8];
#pragma unroll
        for (int e = 0; e < 8; ++e) {
            const float a = tre[e >> 2][e & 3], bb = tim[e >> 2][e & 3];
            pr[e] = a * twc[e] + bb * tws[e];
            pi[e] = bb * twc[e] - a * tws[e];
        }
        const u32x4 bre = pk8(pr), bim = pk8(pi);
        const f32x4 z0 = mfma16(s32, bim, mfma16(c32, bre, zero));
        const f32x4 z1 = mfma16(ns32, bre, mfma16(c32, bim, zero));
        const f32x4 z2 = mfma16(s32b, bim, mfma16(c32b, bre, zero));
        const f32x4 z3 = mfma16(s32b, bre, mfma16(nc32b, bim, zero));
        const bool okA = i16 <= 10, okB = (i16 >= 1 && i16 <= 9), okB0 = okB || (i16 == 0 && g == 0);
        const int kA = i16 + 80 * g, kB = 320 - i16 - 80 * g;
        const int oA0 = (okA ? kA : 335) * S_K, oAs = okA ? 20 * S_K : 0;
        const int oB0 = (okB0 ? kB : 335) * S_K, oB1 = (okB ? kB - 20 : 335) * S_K, oBs = okB ? 20 * S_K : 0;
        char* st = smem + P_S + w * 4;
#pragma unroll
        for (int i = 0; i < 4; ++i) *(u32*)(st + oA0 + i * oAs) = pk(z0[i], z1[i]);
        *(u32*)(st + oB0) = pk(z2[0], z3[0]);
#pragma unroll
        for (int i = 1; i < 4; ++i) *(u32*)(st + oB1 - (i - 1) * oBs) = pk(z2[i], z3[i]);
    }
    drain();
    __syncthreads();
    if (t == 0) raise_flag(P.ws, F_X, b, st.gen + 1u);
    return fb;
}
DI void p1_finish(const Args& P, char* smem, const u32x2 fb, const int t, const int w) {
    const int l = t & 63, g = l >> 4, i16 = l & 15, b = blockIdx.x;
    {
        u32x2 av[3], dv[3];
#pragma unroll
        for (int it = 0; it < 3; ++it)
            av[it] = *(const u32x2*)(smem + P_S + (16 * min(w + 8 * it, 20) + i16) * S_K + g * 8);
#pragma unroll
        for (int it = 0; it < 3; ++it) {
            const f32x4 d = mfma16k16(av[it], fb, f32x4{0.f, 0.f, 0.f, 0.f});
            dv[it] = u32x2{pk(d[0], d[1]), pk(d[2], d[3])};
        }
        const int kap = i16 >> 1, po = (i16 & 1) * 56;
#pragma unroll
        for (int it = 0; it < 3; ++it) {
            const int k = 16 * min(w + 8 * it, 20) + 4 * g;
            const int cgk = min((k * 2341) >> 16, 11);
            const int o0 = po + (k - min(28 * cgk, 296)) * 2, o1 = po + (k - 296) * 2;
            const int d0 = (k < 324) ? (kap * 12 + cgk) * 128 + (((o0 >> 4) ^ kap) << 4) + (o0 & 8) : 12288 + l * 8;
            const int d1 = (k >= 296 && k < 308) ? (kap * 12 + 11) * 128 + (((o1 >> 4) ^ kap) << 4) + (o1 & 8) : d0;
            *(u32x2*)(smem + P_X + d0) = dv[it];
            *(u32x2*)(smem + P_X + d1) = dv[it];
        }
    }
    __syncthreads();
    const rsrc_t rws = mkrs(P.ws, WS_USED);
    if (w >= 4) {
#pragma unroll
        for (int i = 0; i < 3; ++i) {
            const int m = (t & 255) + 256 * i, pc = m >> 3, kp = (pc * 43) >> 9;
            st16sc(rws, OFF_X1 + b * X1_ROW + m * 16, sane(*(const u32x4*)(smem + P_X + pc * 128 + (((m & 7) ^ kp) << 4))));
        }
    }
}

DI void p3_consts(char* tb, const int w, const int l, const int ai) {
    const int g = l >> 4, i16 = l & 15;
    float v[8];
    if (w < 4) {
        const float d1 = (float)(16 * (w & 1) + i16);
#pragma unroll
        for (int j = 0; j < 8; ++j) v[j] = cosr((float)(8 * g + j) * d1 * 0.03125f - ((w >= 2) ? 0.25f : 0.f));
        *(u32x4*)(tb + w * 1024 + l * 16) = pk8(v);
    } else {
        const int tau = w & 1;
        const float d1 = (float)(16 * tau + i16);
#pragma unroll
        for (int i = 0; i < 4; ++i)
            v[i] = cosr(d1 * (float)(4 * g + i) * (1.0f / 640.0f) - ((w >= 6) ? 0.25f : 0.f)) * (1.0f / (2048.0f * 640.0f));
        *(f32x4*)(tb + w * 1024 + l * 16) = f32x4{v[0], v[1], v[2], v[3]};
        if (w < 6) {
#pragma unroll
            for (int j = 0; j < 8; ++j) {
                const int k2 = 4 * g + (j & 3), d2 = 16 * tau + i16;
                const float wgt = (k2 > 10 || d2 >= 20) ? 0.f : ((k2 == 0 || k2 == 10) ? 1.f : 2.f);
                const float c = cosr((float)k2 * (float)d2 * 0.05f - ((j < 4) ? 0.f : 0.25f));
                v[j] = (j < 4) ? wgt * c : -wgt * c;
            }
            *(u32x4*)(tb + (8 + tau) * 1024 + l * 16) = pk8(v);
        }
    }
    if (w < 3) {
        const int a = i16 >> 1, plo = i16 & 1;
#pragma unroll
        for (int e = 0; e < 4; ++e) {
            const int kk = 2 * g + (e >> 1), pp = e & 1;
            const float c = cosr((float)kk * (float)(256 * a + ai + 96 * w) * (1.0f / 2048.0f) - ((pp != plo) ? 0.25f : 0.f));
            v[e] = (plo == 0 && pp == 1) ? -c : c;
        }
        *(u32x2*)(tb + 10240 + w * 512 + l * 8) = u32x2{pk(v[0], v[1]), pk(v[2], v[3])};
    }
}

DI void role_a(const Args& P, char* smem, const int t, const int w, const int ka, const int cg) {
    const int l = t & 63, g = l >> 4, i16 = l & 15;
    const int ai = ka * 12 + cg, nset = ai < 64 ? 3 : 2;
    const int colbase = min(28 * cg, 296);
    const rsrc_t rws = mkrs(P.ws, WS_USED);
    STAMP_DECL STAMP();
    Start st;
    p1_start(P, st, t, w);
    p1_consts(smem, t, w);
    const u32x2 fb = p1_rows(P, smem, st, t, w);
    const u32 pw = warm_w(P, t, w);
    fence();
    p1_finish(P, smem, fb, t, w);
    const u32 tag = st.gen + 1u;
    volatile LAS u32* word = (volatile LAS u32*)(smem + L_WORD);
    STAMP();
    u32x4 f1re, f1im, f2re, f2im;
    float t2c[4], t2s[4];
    if (w < 4) {
        const int wpl = w >> 1, dl = (ka + colbase) & 7;
        {
            float c[8], s[8];
#pragma unroll
            for (int j = 0; j < 8; ++j) {
                const float a = (float)(8 * (g & 1) + j) * (float)i16 * 0.0625f;
                const float co = cosr(a), si = sinr(a);
                c[j] = (g < 2) ? co : si;
                s[j] = (g < 2) ? -si : co;
            }
            f1re = pk8(c); f1im = pk8(s);
#pragma unroll
            for (int j = 0; j < 4; ++j) {
                const float pr = (float)(4 * g + j) * (float)i16;
                const float co = cosr(pr * 0.0625f), si = sinr(pr * 0.0625f);
                c[j] = co; c[4 + j] = si;
                s[j] = -si; s[4 + j] = co;
                t2c[j] = cosr(pr * (1.0f / 256.0f));
                t2s[j] = sinr(pr * (1.0f / 256.0f));
            }
            f2re = pk8(c); f2im = pk8(s);
        }
        const rsrc_t rw = mkrs(wpl ? P.wi : P.wr, 2048 * 642);
        u32x4 wv[2][8];
#pragma unroll
        for (int it = 0; it < 2; ++it) {
            const int task = min((t & 127) + 128 * it, 159), r = task / 5, ck = task - 5 * r;
#pragma unroll
            for (int e = 0; e < 8; ++e)
                wv[it][e] = ld16(rw, ((((ka + 8 * ((r & 15) + 16 * (8 * (r >> 4) + e))) * 321 + colbase) * 2) & ~15u) + ck * 16, 0);
        }
        fence();
        p3_consts(smem + L_P3C, w, l, ai);
#pragma unroll
        for (int it = 0; it < 2; ++it) {
            const int task = min((t & 127) + 128 * it, 159), r = task / 5, ck = task - 5 * r;
#pragma unroll
            for (int c = 0; c < 8; ++c) {
                const int col = 8 * ck + c - dl, m = col * 2 + wpl, d = c >> 1;
                const u32x4* x = wv[it];
                const u32x4 o = (c & 1) ? u32x4{permhi(x[0][d], x[1][d]), permhi(x[2][d], x[3][d]), permhi(x[4][d], x[5][d]), permhi(x[6][d], x[7][d])}
                                        : u32x4{permlo(x[0][d], x[1][d]), permlo(x[2][d], x[3][d]), permlo(x[4][d], x[5][d]), permlo(x[6][d], x[7][d])};
                const int dst = ((u32)col < 28u) ? L_W + m * 512 + ((((r & 15) * 2 + (r >> 4)) ^ ((m >> 3) & 7)) * 16) : L_DUMP + l * 16;
                *(u32x4*)(smem + dst) = o;
            }
        }
    } else {
        {
            float c[8], s[8];
#pragma unroll
            for (int j = 0; j < 8; ++j) {
                const float a = (float)(8 * (g & 1) + j) * (float)i16 * 0.0625f;
                const float co = cosr(a), si = sinr(a);
                c[j] = (g < 2) ? co : si;
                s[j] = (g < 2) ? -si : co;
            }
            f1re = pk8(c); f1im = pk8(s);
#pragma unroll
            for (int j = 0; j < 4; ++j) {
                const float pr = (float)(4 * g + j) * (float)i16;
                const float co = cosr(pr * 0.0625f), si = sinr(pr * 0.0625f);
                c[j] = co; c[4 + j] = si;
                s[j] = -si; s[4 + j] = co;
                t2c[j] = cosr(pr * (1.0f / 256.0f));
                t2s[j] = sinr(pr * (1.0f / 256.0f));
            }
            f2re = pk8(c); f2im = pk8(s);
        }
        const int task = min(t - 256, 223), oc = task / 7, ck = task - 7 * oc;
        const u32 base = OFF_X1 + (8 * oc) * X1_ROW + ai * 128 + ck * 16;
        u32x4 v[8];
        p3_consts(smem + L_P3C, w, l, ai);
        drain();
        if (w == 4) {
            wait_flags<256>(rws, F_X, tag, l);
            if (l == 0) {
                *word = 1u;
                if (ai == 0) __hip_atomic_store((GAS u32*)(P.ws + OFF_SYNC), tag, __ATOMIC_RELAXED, __HIP_MEMORY_SCOPE_AGENT);
            }
        }
        for (u32 spins = 0; spins < SPIN_MAX && *word == 0u; ++spins) __builtin_amdgcn_s_sleep(1);
        for (u32 spins = 0; spins < SPIN_MAX; ++spins) {
            fence();
            bool ok = true;
#pragma unroll
            for (int e = 0; e < 8; ++e) v[e] = ld16(rws, base + e * X1_ROW, 1);
#pragma unroll
            for (int e = 0; e < 8; ++e) ok &= fresh4(v[e]);
            if (__all(ok)) break;
            __builtin_amdgcn_s_sleep(1);
        }
#pragma unroll
        for (int c = 0; c < 8; ++c) {
            const int el = 8 * ck + c, pl = el >= 28, m = (el - 28 * pl) * 2 + pl, d = c >> 1;
            const u32x4 o = (c & 1) ? u32x4{permhi(v[0][d], v[1][d]), permhi(v[2][d], v[3][d]), permhi(v[4][d], v[5][d]), permhi(v[6][d], v[7][d])}
                                    : u32x4{permlo(v[0][d], v[1][d]), permlo(v[2][d], v[3][d]), permlo(v[4][d], v[5][d]), permlo(v[6][d], v[7][d])};
            *(u32x4*)(smem + L_C + m * 512 + ((oc ^ ((m >> 3) & 7)) * 16)) = o;
        }
    }
    const u32x4 ire = {f2re.x, f2re.y, f2re.z ^ 0x80008000u, f2re.w ^ 0x80008000u};
    const u32x4 iim = {f2im.x ^ 0x80008000u, f2im.y ^ 0x80008000u, f2im.z, f2im.w};
    asm volatile("" :: "v"(pw));
    STAMP();
    __syncthreads();
#pragma unroll
    for (int i = 0; i < 4; ++i) st16sc(rws, OFF_Y1 + ai * Y1_BLK + (t + 512 * i) * 16, u32x4{SENT, SENT, SENT, SENT});
    fence();
    {
        const int q = i16 >> 2, p = i16 & 3;
        const f32x4 zero = {0.f, 0.f, 0.f, 0.f};
        auto fetch = [&](int col, u32x4& a, u32x2& wre, u32x2& wim) {
            const int mi = col * 2 + (g >> 1), swi = (mi >> 3) & 7;
            const char* ib = smem + L_C + mi * 512 + (p & 1) * 8;
            const u32x2 a0 = trread(ib + (((2 * (8 * (g & 1) + q) + (p >> 1)) ^ swi) * 16));
            const u32x2 a1 = trread(ib + (((2 * (8 * (g & 1) + 4 + q) + (p >> 1)) ^ swi) * 16));
            a = u32x4{a0.x, a0.y, a1.x, a1.y};
            const int m0 = col * 2, sw = (m0 >> 3) & 7;
            const int woff = (((i16 * 2 + (g >> 1)) ^ sw) * 16) + (g & 1) * 8;
            wre = *(const u32x2*)(smem + L_W + m0 * 512 + woff);
            wim = *(const u32x2*)(smem + L_W + (m0 + 1) * 512 + woff);
        };
        auto columns = [&](auto nc, const u32x4* a, const u32x2* wre, const u32x2* wim, u32x2* ore, u32x2* oim) {
            constexpr int NC = decltype(nc)::value;
            f32x4 re[NC], im[NC];
            u32x4 d[NC];
#pragma unroll
            for (int c = 0; c < NC; ++c) { re[c] = mfma16(a[c], f1re, zero); im[c] = mfma16(a[c], f1im, zero); }
            __builtin_amdgcn_sched_barrier(0);
#pragma unroll
            for (int c = 0; c < NC; ++c) {
                float x[8];
#pragma unroll
                for (int i = 0; i < 4; ++i) {
                    x[i] = re[c][i] * t2c[i] + im[c][i] * t2s[i];
                    x[4 + i] = im[c][i] * t2c[i] - re[c][i] * t2s[i];
                }
                d[c] = pk8(x);
            }
            __builtin_amdgcn_sched_barrier(0);
#pragma unroll
            for (int c = 0; c < NC; ++c) { re[c] = mfma16(f2re, d[c], zero); im[c] = mfma16(f2im, d[c], zero); }
            __builtin_amdgcn_sched_barrier(0);
#pragma unroll
            for (int c = 0; c < NC; ++c) {
                float x[8];
#pragma unroll
                for (int i = 0; i < 4; ++i) {
                    const float a_ = (i & 1) ? bfhi(wre[c][i >> 1]) : bflo(wre[c][i >> 1]);
                    const float b_ = (i & 1) ? bfhi(wim[c][i >> 1]) : bflo(wim[c][i >> 1]);
                    x[i] = re[c][i] * a_ - im[c][i] * b_;
                    x[4 + i] = re[c][i] * b_ + im[c][i] * a_;
                }
                d[c] = pk8(x);
            }
            __builtin_amdgcn_sched_barrier(0);
#pragma unroll
            for (int c = 0; c < NC; ++c) { re[c] = mfma16(d[c], ire, zero); im[c] = mfma16(d[c], iim, zero); }
            __builtin_amdgcn_sched_barrier(0);
#pragma unroll
            for (int c = 0; c < NC; ++c) {
                float x[8];
#pragma unroll
                for (int i = 0; i < 4; ++i) {
                    x[i] = re[c][i] * t2c[i] - im[c][i] * t2s[i];
                    x[4 + i] = im[c][i] * t2c[i] + re[c][i] * t2s[i];
                }
                d[c] = pk8(x);
            }
            __builtin_amdgcn_sched_barrier(0);
#pragma unroll
            for (int c = 0; c < NC; ++c) { re[c] = mfma16(d[c], ire, zero); im[c] = mfma16(d[c], iim, zero); }
            __builtin_amdgcn_sched_barrier(0);
#pragma unroll
            for (int c = 0; c < NC; ++c) {
                ore[c] = u32x2{pk(re[c][0], re[c][1]), pk(re[c][2], re[c][3])};
                oim[c] = u32x2{pk(im[c][0], im[c][1]), pk(im[c][2], im[c][3])};
            }
        };
        auto put = [&](int col, const u32x2 ore, const u32x2 oim) {
            const int m0 = col * 2, sw = (m0 >> 3) & 7;
            const int ooff = (((2 * i16 + (g >> 1)) ^ sw) * 16) + (g & 1) * 8;
            *(u32x2*)(smem + L_C + m0 * 512 + ooff) = ore;
            *(u32x2*)(smem + L_C + (m0 + 1) * 512 + ooff) = oim;
        };
        u32x4 a[4];
        u32x2 wre[4], wim[4], ore[4], oim[4];
        if (w < 4) {
#pragma unroll
            for (int c = 0; c < 4; ++c) fetch(w + 8 * c, a[c], wre[c], wim[c]);
            columns(ic<4>{}, a, wre, wim, ore, oim);
#pragma unroll
            for (int c = 0; c < 4; ++c) put(w + 8 * c, ore[c], oim[c]);
        } else {
#pragma unroll
            for (int c = 0; c < 3; ++c) fetch(w + 8 * c, a[c], wre[c], wim[c]);
            columns(ic<3>{}, a, wre, wim, ore, oim);
#pragma unroll
            for (int c = 0; c < 3; ++c) put(w + 8 * c, ore[c], oim[c]);
        }
    }
    drain();
    STAMP();
    __syncthreads();
    if (t == 0) raise_flag(P.ws, F_Y, ai, tag);
    {
        const int q = i16 >> 2, p = i16 & 3;
        const int G = 2 * w + (g >> 1), pl = g & 1;
        u32x2 v[7];
#pragma unroll
        for (int it = 0; it < 7; ++it) {
            const int m = (4 * it + q) * 2 + pl;
            v[it] = trread(smem + L_C + m * 512 + (((2 * G + (p >> 1)) ^ ((m >> 3) & 7)) * 16) + (p & 1) * 8);
        }
#pragma unroll
        for (int it = 0; it < 7; ++it) {
            const int o = pl * 56 + it * 8;
            *(u32x2*)(smem + L_W + (16 * G + i16) * 128 + (((o >> 4) ^ (i16 & 7)) << 4) + (o & 8)) = v[it];
        }
    }
    __syncthreads();
#pragma unroll
    for (int i = 0; i < 4; ++i) {
        const int m = t + 512 * i;
        st16sc(rws, OFF_Y1 + ai * Y1_BLK + m * 16, sane(*(const u32x4*)(smem + L_W + (m >> 3) * 128 + (((m & 7) ^ ((m >> 3) & 7)) << 4))));
    }
    STAMP();

    u32x4 C[2], S[2], nsS[2], sC[2], F[2];
    float twc[8], tws[8];
    u32x2 fa[3];
    {
        const char* tb = smem + L_P3C;
        const u32 sgn = (g >= 2) ? 0x80008000u : 0u;
#pragma unroll
        for (int tau = 0; tau < 2; ++tau) {
            C[tau] = *(const u32x4*)(tb + tau * 1024 + l * 16);
            S[tau] = *(const u32x4*)(tb + (2 + tau) * 1024 + l * 16);
            nsS[tau] = S[tau] ^ (sgn ^ 0x80008000u);
            sC[tau] = C[tau] ^ sgn;
            const f32x4 a = *(const f32x4*)(tb + (4 + tau) * 1024 + l * 16), bq = *(const f32x4*)(tb + (6 + tau) * 1024 + l * 16);
#pragma unroll
            for (int i = 0; i < 4; ++i) { twc[4 * tau + i] = a[i]; tws[4 * tau + i] = bq[i]; }
            F[tau] = *(const u32x4*)(tb + (8 + tau) * 1024 + l * 16);
        }
#pragma unroll
        for (int j = 0; j < 3; ++j) fa[j] = *(const u32x2*)(tb + 10240 + j * 512 + l * 8);
    }
    drain();
    if (w == 7) {
        wait_flags<96>(rws, F_Y, tag, l);
        if (l == 0) *word = 2u;
    }
    STAMP();
    {
        u32 off[4];
#pragma unroll
        for (int i = 0; i < 4; ++i) {
            const int m = min(t + 512 * i, nset * 672 - 1), j = (m >= 672) + (m >= 1344), mm = m - 672 * j, ab = mm / 7;
            off[i] = OFF_Y1 + ab * Y1_BLK + (ai + 96 * j) * 128 + (mm - 7 * ab) * 16;
        }
        for (u32 spins = 0; spins < SPIN_MAX && *word != 2u; ++spins) __builtin_amdgcn_s_sleep(1);
        u32x4 v[4];
        for (u32 spins = 0; spins < SPIN_MAX; ++spins) {
            fence();
#pragma unroll
            for (int i = 0; i < 4; ++i) v[i] = ld16(rws, off[i], 1);
            if (__all(fresh4(v[0]) && fresh4(v[1]) && fresh4(v[2]) && fresh4(v[3]))) break;
            __builtin_amdgcn_s_sleep(1);
        }
#pragma unroll
        for (int i = 0; i < 4; ++i) *(u32x4*)(smem + L_X + min(t + 512 * i, nset * 672 - 1) * 16) = v[i];
    }
    STAMP();
    __syncthreads();
    {
        const int q = i16 >> 2, p = i16 & 3;
        const int kap = 2 * g + (q >> 1), pl = q & 1;
        u32x2 bfr[3][3];
#pragma unroll
        for (int it = 0; it < 3; ++it) {
            const int T = min(w + 8 * it, 20);
            const int k = min(16 * T + 4 * p, 320);
            const int cgk = min((k * 2341) >> 16, 11), idx = k - min(28 * cgk, 296);
            const int src = L_X + (kap * 12 + cgk) * 112 + pl * 56 + idx * 2;
#pragma unroll
            for (int j = 0; j < 3; ++j) bfr[it][j] = trread(smem + src + j * 10752);
        }
        u32x2 dv[3][3];
#pragma unroll
        for (int it = 0; it < 3; ++it)
#pragma unroll
            for (int j = 0; j < 3; ++j) {
                const f32x4 d = mfma16k16(fa[j], bfr[it][j], f32x4{0.f, 0.f, 0.f, 0.f});
                dv[it][j] = u32x2{pk(d[0], d[1]), pk(d[2], d[3])};
            }
#pragma unroll
        for (int it = 0; it < 3; ++it)
#pragma unroll
            for (int j = 0; j < 3; ++j)
                *(u32x2*)(smem + L_S + j * S_SZ + (16 * min(w + 8 * it, 20) + i16) * S_K + g * 8) = dv[it][j];
    }
    __syncthreads();
    STAMP();
    {
        const int k2 = i16 <= 10 ? i16 : 10;
        const int k0 = k2 + 160 * g;
        const int kofs = ((g < 2) ? k0 : 640 - k0) * S_K + w * 4, kstep = (g < 2) ? 20 * S_K : -20 * S_K;
        const rsrc_t ro = mkrs(P.out, 2048 * 2560);
        u32 dd[3][8];
#pragma unroll
        for (int j = 0; j < 3; ++j)
#pragma unroll
            for (int e = 0; e < 8; ++e) dd[j][e] = *(const u32*)(smem + L_S + j * S_SZ + kofs + e * kstep);
        const f32x4 zero = {0.f, 0.f, 0.f, 0.f};
        f32x4 vre[3][2], vim[3][2];
#pragma unroll
        for (int j = 0; j < 3; ++j) {
            const u32x4 are = {permlo(dd[j][0], dd[j][1]), permlo(dd[j][2], dd[j][3]), permlo(dd[j][4], dd[j][5]), permlo(dd[j][6], dd[j][7])};
            const u32x4 aim = {permhi(dd[j][0], dd[j][1]), permhi(dd[j][2], dd[j][3]), permhi(dd[j][4], dd[j][5]), permhi(dd[j][6], dd[j][7])};
#pragma unroll
            for (int tau = 0; tau < 2; ++tau) {
                vre[j][tau] = mfma16(aim, nsS[tau], mfma16(are, C[tau], zero));
                vim[j][tau] = mfma16(aim, sC[tau], mfma16(are, S[tau], zero));
            }
        }
        __builtin_amdgcn_sched_barrier(0);
        u32x4 va[3][2];
#pragma unroll
        for (int j = 0; j < 3; ++j)
#pragma unroll
            for (int tau = 0; tau < 2; ++tau) {
                float pr[8];
#pragma unroll
                for (int i = 0; i < 4; ++i) {
                    pr[i] = vre[j][tau][i] * twc[4 * tau + i] - vim[j][tau][i] * tws[4 * tau + i];
                    pr[4 + i] = vim[j][tau][i] * twc[4 * tau + i] + vre[j][tau][i] * tws[4 * tau + i];
                }
                va[j][tau] = pk8(pr);
            }
        __builtin_amdgcn_sched_barrier(0);
        f32x4 y[3][2][2];
#pragma unroll
        for (int j = 0; j < 3; ++j)
#pragma unroll
            for (int dl = 0; dl < 2; ++dl) {
                y[j][dl][0] = mfma16(va[j][0], F[dl], zero);
                y[j][dl][1] = mfma16(va[j][1], F[dl], zero);
            }
        __builtin_amdgcn_sched_barrier(0);
#pragma unroll
        for (int j = 0; j < 3; ++j) {
            const u32 orow = (ai + 96 * j + 256 * w) * 2560;
#pragma unroll
            for (int dl = 0; dl < 2; ++dl) {
                u32 a0 = pk(y[j][dl][0][0], y[j][dl][0][1]), a1 = pk(y[j][dl][0][2], y[j][dl][0][3]);
                u32 b0 = pk(y[j][dl][1][0], y[j][dl][1][1]), b1 = pk(y[j][dl][1][2], y[j][dl][1][3]);
                auto s0 = __builtin_amdgcn_permlane16_swap(a0, b0, false, false);
                auto s1 = __builtin_amdgcn_permlane16_swap(a1, b1, false, false);
                const int d2 = 16 * dl + i16;
                const u32 off = (d2 < 20 && j < nset) ? orow + (32 * d2 + (g & 1) * 16 + (g >> 1) * 8) * 2 : 0x80000000u;
                __builtin_amdgcn_raw_buffer_store_b128(u32x4{s0[0], s1[0], s0[1], s1[1]}, ro, off, 0, 0);
            }
        }
    }
    STAMP();
    { const bool PROBE_ROLE_OK = (false); STAMP_OUT(P.ws); }
}

DI void role_b(const Args& P, char* smem, const int t, const int w, const int chb) {
    const int l = t & 63, g = l >> 4, i16 = l & 15;
    const int ch = w & 3, rh = w >> 2;
    STAMP_DECL STAMP();
    const rsrc_t rws = mkrs(P.ws, WS_USED);
    Start st;
    p1_start(P, st, t, w);
    p1_consts(smem, t, w);
    const u32x2 fb = p1_rows(P, smem, st, t, w);
    u32 pv[12], ldt = 0, pu = 0;
    const rsrc_t ru = mkrs(P.u, 2048 * 1280);
    if (w < 4) {
        const u32 nb = 640 * 128 * 2;
        const rsrc_t r0 = mkrs(P.Are, nb), r1 = mkrs(P.Aim, nb), r2 = mkrs(P.Bre, nb), r3 = mkrs(P.Bim, nb), r4 = mkrs(P.Cre, nb), r5 = mkrs(P.Cim, nb);
#pragma unroll
        for (int h = 0; h < 2; ++h) {
            const u32 off = ((4 * chb + w) * 128 + 64 * h + l) * 2;
            pv[6 * h + 0] = ld2(r0, off); pv[6 * h + 1] = ld2(r1, off); pv[6 * h + 2] = ld2(r2, off);
            pv[6 * h + 3] = ld2(r3, off); pv[6 * h + 4] = ld2(r4, off); pv[6 * h + 5] = ld2(r5, off);
        }
        ldt = ld2(mkrs(P.logdt, 1280), (4 * chb + w) * 2);
    }
    const u32 dks = ld2(mkrs(P.dskip, 1280), (4 * chb + ch) * 2);
    if (w < 5) {
        const int xcd = blockIdx.x & 7, jx = blockIdx.x >> 3;
        const int b0 = (160 * xcd) & ~127, nl = (160 * xcd + 160 - b0 + 127) >> 7;
        const int m = min(t, 103 * 3 - 1), r = m / 3;
        pu = __builtin_amdgcn_raw_buffer_load_b32(ru, min(103 * jx + r, 2047) * 1280 + b0 + min(m - 3 * r, nl - 1) * 128, 0, 0);
    }
    const u32 pw = warm_w(P, t, w);
    fence();
    p1_finish(P, smem, fb, t, w);
    STAMP();
    if (w >= 4) {
        const int tt = t - 256;
        fence();
        u32x2 gv[8];
#pragma unroll
        for (int e = 0; e < 8; ++e) gv[e] = ld8(ru, (8 * tt + e) * 1280 + chb * 8, 0);
        fence();
        if (tt < 128) *(u32x4*)(smem + L_U + (tt >> 5) * PB_U + (tt & 31) * 16) = u32x4{0u, 0u, 0u, 0u};
        else if (tt < 144) {
            const int v = tt - 128;
            *(u32x4*)(smem + L_K + (v >> 2) * PB_KS + ((v >> 1) & 1) * PB_K1 + 4096 + (v & 1) * 16) = u32x4{0u, 0u, 0u, 0u};
        }
        u32x4 o[4];
        tr8x4(gv, o);
#pragma unroll
        for (int c = 0; c < 4; ++c) *(u32x4*)(smem + L_U + c * PB_U + 512 + tt * 16) = o[c];
    } else {
        char* sc = smem + w * KG_SZ;
        const float dt = rbf(expf(bf2f(ldt)));
        f32x4 kacc[3];
#pragma unroll
        for (int T = 0; T < 3; ++T) kacc[T] = f32x4{0.f, 0.f, 0.f, 0.f};
#pragma unroll 1
        for (int h = 0; h < 2; ++h) {
            const float lre = dt * bf2f(h ? pv[6] : pv[0]), lim = dt * bf2f(h ? pv[7] : pv[1]) * 0.15915494309189535f;
            const cf c = cmul(cf{bf2f(h ? pv[10] : pv[4]), bf2f(h ? pv[11] : pv[5])}, cf{dt * bf2f(h ? pv[8] : pv[2]), dt * bf2f(h ? pv[9] : pv[3])});
            char* rowp = sc + l * 4;
            const cf z1 = cexpk(lre, lim, 1.f), z8 = cexpk(lre, lim, 8.f), z128 = cexpk(lre, lim, 128.f);
            const cf z2 = cmul(z1, z1), z4 = cmul(z2, z2), z6 = cmul(z2, z4);
            const cf z8c = {z8.x, -z8.y};
            cf v = {1.f, 0.f}, r0 = c, r1 = cmul(c, z128);
            cf q = cmul(r1, z128);
#pragma unroll
            for (int j = 0; j < 8; ++j) {
                *(u32*)(rowp + KG_B + (2 * j) * KG_RS) = pk(v.x, v.y);
                v = cmul(v, z8c);
                *(u32*)(rowp + KG_B + (2 * j + 1) * KG_RS) = pk(v.x, v.y);
                v = cmul(v, z8c);
                *(u32*)(rowp + j * KG_RS) = pkc(r0);
                *(u32*)(rowp + (8 + j) * KG_RS) = pkc(r1);
                r0 = cmul(r0, z1);
                r1 = cmul(r1, z1);
#pragma unroll
                for (int e = 0; e < 2; ++e) {
                    const int hh = 2 + 2 * j + e;
                    if (hh < 4) {
                        const int r = 16 + (hh - 2) * 4;
                        *(u32*)(rowp + r * KG_RS) = pkc(q);
                        *(u32*)(rowp + (r + 1) * KG_RS) = pkc(cmul(q, z2));
                        *(u32*)(rowp + (r + 2) * KG_RS) = pkc(cmul(q, z4));
                        *(u32*)(rowp + (r + 3) * KG_RS) = pkc(cmul(q, z6));
                    } else if (hh < 8) {
                        const int r = 24 + (hh - 4) * 2;
                        *(u32*)(rowp + r * KG_RS) = pkc(q);
                        *(u32*)(rowp + (r + 1) * KG_RS) = pkc(cmul(q, z4));
                    } else if (hh <= 16) {
                        *(u32*)(rowp + (32 + hh - 8) * KG_RS) = pkc(q);
                    }
                    if (hh < 16) q = cmul(q, z128);
                }
            }
            u32x4 bf[4], af[3][4];
#pragma unroll
            for (int ks = 0; ks < 4; ++ks) {
                bf[ks] = *(const u32x4*)(sc + KG_B + i16 * KG_RS + g * 16 + ks * 64);
#pragma unroll
                for (int T = 0; T < 3; ++T) af[T][ks] = *(const u32x4*)(sc + (16 * T + i16) * KG_RS + g * 16 + ks * 64);
            }
#pragma unroll
            for (int T = 0; T < 3; ++T)
#pragma unroll
                for (int ks = 0; ks < 4; ++ks) kacc[T] = mfma16(af[T][ks], bf[ks], kacc[T]);
        }
        char* tbl = smem + L_T + w * KG_T;
#pragma unroll
        for (int T = 0; T < 3; ++T)
#pragma unroll
            for (int i = 0; i < 4; ++i) {
                const int row = 16 * T + 4 * g + i;
                int e;
                if (T == 0) e = 128 * (row >> 3) + (row & 7);
                else if (T == 1) e = (g < 2) ? 256 + 128 * g + 2 * i : 512 + 256 * (g - 2) + 128 * (i >> 1) + 4 * (i & 1);
                else e = 1024 + 128 * (row - 32);
                const bool ok = row < 40 || (row == 40 && i16 == 0);
                *(u16*)(tbl + (ok ? (e + 8 * i16) * 2 : 4110)) = (u16)(pk(kacc[T][i], kacc[T][i]) & 0xffffu);
            }
        u32 v[4][8], vm[4];
#pragma unroll
        for (int it = 0; it < 4; ++it) {
            const int tt = l + 64 * it, a = 255 - tt;
            const u32 code = (a < 32) ? 0x76543210u : (a < 64) ? 0x86444200u : (a < 128) ? 0x88444000u : ((a & 1) ? 0x88880000u : 0x88800000u);
            const int lastoff = (a <= 32) ? 7 : 8;
            const char* tb = tbl + 16 * a;
#pragma unroll
            for (int e = 0; e < 8; ++e) v[it][e] = *(const u16*)(tb + 2 * ((code >> (4 * e)) & 15));
            vm[it] = *(const u16*)(tb + ((a == 0) ? 0 : 2 * lastoff - 16));
            vm[it] = (a == 0) ? 0u : vm[it];
        }
#pragma unroll
        for (int it = 0; it < 4; ++it) {
            char* kb = smem + L_K + w * PB_KS + (l + 64 * it) * 16;
            const u32* x = v[it];
            *(u32x4*)(kb) = u32x4{x[7] | (x[6] << 16), x[5] | (x[4] << 16), x[3] | (x[2] << 16), x[1] | (x[0] << 16)};
            *(u32x4*)(kb + PB_K1) = u32x4{x[6] | (x[5] << 16), x[4] | (x[3] << 16), x[2] | (x[1] << 16), x[0] | (vm[it] << 16)};
        }
    }
    asm volatile("" :: "v"(pw ^ pu));
    STAMP();
    __syncthreads();
    STAMP();
    const int mu = g >> 1, hp = g & 1;
    const int x0 = 2047 - 16 * mu - i16 + 8 * hp;
    const char* ap = smem + L_K + ch * PB_KS + ((x0 & 1) ? PB_K1 + (x0 - 1) * 2 : x0 * 2);
    const char* bp = smem + L_U + ch * PB_U + (256 + 16 * (i16 - mu) + 8 * hp) * 2;
    f32x4 acc[8];
#pragma unroll
    for (int c = 0; c < 8; ++c) acc[c] = f32x4{0.f, 0.f, 0.f, 0.f};
    {
        u32x4 A[2][8], B[2][8];
        const char* ap0 = ap - 256 * rh;
        const char* bp0 = bp - 256 * rh;
#pragma unroll
        for (int p = 0; p < 8; ++p) {
            const u32* a = (const u32*)(ap0 - 512 * p);
            B[0][p] = *(const u32x4*)(bp0 + 512 * p);
            A[0][p] = u32x4{a[0], a[1], a[2], a[3]};
        }
#pragma unroll
        for (int r = 0; r < 4; ++r) {
            __builtin_amdgcn_sched_barrier(0);
            if (r < 3) {
#pragma unroll
                for (int p = 0; p < 8; ++p) {
                    const u32* a = (const u32*)(ap0 - 64 * (r + 1) - 512 * p);
                    B[(r + 1) & 1][p] = *(const u32x4*)(bp0 - 64 * (r + 1) + 512 * p);
                    A[(r + 1) & 1][p] = u32x4{a[0], a[1], a[2], a[3]};
                }
            }
#pragma unroll
            for (int p = 0; p < 8; ++p)
#pragma unroll
                for (int q = 0; q + p < 8; ++q) acc[p + q] = mfma16(A[r & 1][p], B[r & 1][q], acc[p + q]);
            if (r < 3) {
#pragma unroll
                for (int k = 0; k < 12; ++k) {
                    __builtin_amdgcn_sched_group_barrier(0x008, 3, 0);
                    __builtin_amdgcn_sched_group_barrier(0x100, 2, 0);
                }
            }
        }
        __builtin_amdgcn_sched_barrier(0);
    }
    STAMP();
    char* rb = smem + L_R;
#pragma unroll
    for (int c = 0; c < 4; ++c) *(f32x4*)(rb + (w * 4 + c) * 1024 + l * 16) = rh ? acc[c] : acc[4 + c];
    __syncthreads();
    char* yi = smem + L_Y;
    const float dsk = bf2f(dks);
    {
        f32x4 o[4];
        u32x2 uu[4];
#pragma unroll
        for (int c = 0; c < 4; ++c) {
            o[c] = *(const f32x4*)(rb + ((w ^ 4) * 4 + c) * 1024 + l * 16);
            uu[c] = *(const u32x2*)(smem + L_U + ch * PB_U + (256 + 256 * (4 * rh + c) + 16 * i16 + 4 * g) * 2);
        }
        u32x2 res[4];
#pragma unroll
        for (int c = 0; c < 4; ++c) {
            const f32x4 mine = rh ? acc[4 + c] : acc[c];
            float v[4];
#pragma unroll
            for (int i = 0; i < 4; ++i) {
                const float uf = (i & 1) ? bfhi(i < 2 ? uu[c].x : uu[c].y) : bflo(i < 2 ? uu[c].x : uu[c].y);
                v[i] = rbf(mine[i] + o[c][i]) + rbf(uf * dsk);
            }
            res[c] = u32x2{pk(v[0], v[1]), pk(v[2], v[3])};
        }
#pragma unroll
        for (int c = 0; c < 4; ++c) *(u32x2*)(yi + ch * PB_Y + (64 * (4 * rh + c) + 4 * i16 + (g ^ ((i16 >> 2) & 3))) * 8) = res[c];
    }
    __syncthreads();
    STAMP();
    {
        const rsrc_t ro = mkrs(P.out, 2048 * 2560);
        const int q = i16 >> 2, p = i16 & 3;
        u32x2 v[4];
#pragma unroll
        for (int i = 0; i < 4; ++i) {
            const int sg = ((256 * w + 64 * i + 16 * g) >> 2) + p;
            v[i] = trread(yi + q * PB_Y + (sg ^ ((sg >> 4) & 3)) * 8);
        }
#pragma unroll
        for (int i = 0; i < 4; ++i)
            __builtin_amdgcn_raw_buffer_store_b64(v[i], ro, (256 * w + 64 * i + 16 * g + i16) * 2560 + 1280 + chb * 8, 0, 0);
    }
    STAMP();
    { const bool PROBE_ROLE_OK = (chb == 21); STAMP_OUT(P.ws); }
}

__global__ void __launch_bounds__(512, 2) fused_kernel(Args P) {
    extern __shared__ __attribute__((aligned(16))) char smem[];
    const int t = threadIdx.x;
    const int w = __builtin_amdgcn_readfirstlane(t >> 6);
    const int b = blockIdx.x, xcd = b & 7, jx = b >> 3;
    if (jx < 20) role_b(P, smem, t, w, 20 * xcd + jx);
    else role_a(P, smem, t, w, xcd, jx - 20);
}

extern "C" void kernel_launch(void* const* d_in, const int* in_sizes, int n_in,
                              void* d_out, int out_size, void* d_ws, size_t ws_size,
                              hipStream_t stream) {
    Args a;
    a.x = (const u16*)d_in[0];
    a.wr = (const u16*)d_in[1];
    a.wi = (const u16*)d_in[2];
    a.u = (const u16*)d_in[3];
    a.Are = (const u16*)d_in[4];
    a.Aim = (const u16*)d_in[5];
    a.Bre = (const u16*)d_in[6];
    a.Bim = (const u16*)d_in[7];
    a.Cre = (const u16*)d_in[8];
    a.Cim = (const u16*)d_in[9];
    a.logdt = (const u16*)d_in[10];
    a.dskip = (const u16*)d_in[11];
    a.ws = (char*)d_ws;
    a.out = (u16*)d_out;
    (void)hipFuncSetAttribute((const void*)fused_kernel, hipFuncAttributeMaxDynamicSharedMemorySize, LDS_BYTES);
    fused_kernel<<<256, 512, LDS_BYTES, stream>>>(a);
}
```

```cpp
#include <hip/hip_runtime.h>
#include <hip/hip_bf16.h>

typedef unsigned short u16;
typedef unsigned int u32;
typedef __attribute__((ext_vector_type(2))) __bf16 bf16x2;
typedef __attribute__((ext_vector_type(8))) __bf16 bf16x8;
typedef __attribute__((ext_vector_type(2))) float f32x2;
typedef __attribute__((ext_vector_type(4))) float f32x4;
typedef __attribute__((ext_vector_type(4))) u32 u32x4;
typedef __attribute__((ext_vector_type(2))) u32 u32x2;
typedef __attribute__((ext_vector_type(4))) short s16x4;
typedef __amdgpu_buffer_rsrc_t rsrc_t;
#define LAS __attribute__((address_space(3)))
#define GAS __attribute__((address_space(1)))
#define DI __device__ __forceinline__
#define SC1 16

constexpr u32 X1_ROW = 12288;
constexpr u32 OFF_X1 = 0;
constexpr u32 Y1_BLK = 32768;
constexpr u32 OFF_Y1 = 256 * X1_ROW;
constexpr u32 OFF_SYNC = OFF_Y1 + 96 * Y1_BLK;
constexpr u32 F_X = 1024;
constexpr u32 F_Y = F_X + 1024;
constexpr u32 WS_USED = OFF_SYNC + F_Y + 1024;
constexpr u32 SENT = 0xFFFFFFFFu;
constexpr u32 SPIN_MAX = 1u << 16;

constexpr int S_K = 40, S_SZ = 336 * S_K;
constexpr int P_X = 80448;
constexpr int P_S = P_X + 12800;
constexpr int P_C = 32768;
constexpr int L_W = 0;
constexpr int L_C = L_W + 32768;
constexpr int L_DUMP = L_C + 28672;
constexpr int L_X = L_DUMP + 1024;
constexpr int L_S = L_X + 3 * 10752;
constexpr int L_P3C = L_S + 3 * S_SZ;
constexpr int KG_RS = 272, KG_B = 41 * KG_RS, KG_SZ = 57 * KG_RS;
constexpr int PB_U = 4608, PB_K1 = 4160, PB_KS = 8320, PB_Y = 4128, KG_T = 4112;
constexpr int L_U = 4 * KG_SZ;
constexpr int L_K = P_S + S_SZ;
constexpr int L_T = L_K + 4 * PB_KS;
constexpr int L_R = 0;
constexpr int L_Y = L_R + 32768;
constexpr int L_WORD = L_T + 4 * KG_T;
constexpr int LDS_BYTES = L_WORD + 16;
static_assert(L_P3C + 11776 <= L_WORD && L_U + 4 * PB_U <= P_X && L_Y + 4 * PB_Y <= L_U && LDS_BYTES <= 163840, "LDS map");

DI u32 pk(float a, float b) {
    f32x2 v = {a, b};
    return __builtin_bit_cast(u32, __builtin_convertvector(v, bf16x2));
}
DI u32x4 pk8(const float* v) { return u32x4{pk(v[0], v[1]), pk(v[2], v[3]), pk(v[4], v[5]), pk(v[6], v[7])}; }
DI float bflo(u32 v) { return __uint_as_float(v << 16); }
DI float bfhi(u32 v) { return __uint_as_float(v & 0xffff0000u); }
DI float bf2f(u32 v) { return __uint_as_float(v << 16); }
DI float rbf(float a) { return bflo(pk(a, a)); }
DI float cosr(float rev) { return __builtin_amdgcn_cosf(rev); }
DI float sinr(float rev) { return __builtin_amdgcn_sinf(rev); }
DI f32x4 mfma16(u32x4 a, u32x4 b, f32x4 c) {
    return __builtin_amdgcn_mfma_f32_16x16x32_bf16(__builtin_bit_cast(bf16x8, a), __builtin_bit_cast(bf16x8, b), c, 0, 0, 0);
}
DI f32x4 mfma16k16(u32x2 a, u32x2 b, f32x4 c) {
    return __builtin_amdgcn_mfma_f32_16x16x16bf16_1k(__builtin_bit_cast(s16x4, a), __builtin_bit_cast(s16x4, b), c, 0, 0, 0);
}
DI u32x2 trread(const char* p) {
    return __builtin_bit_cast(u32x2, __builtin_amdgcn_ds_read_tr16_b64_v4i16((LAS s16x4*)(p)));
}
DI u32 permlo(u32 a, u32 b) { return __builtin_amdgcn_perm(b, a, 0x05040100u); }
DI u32 permhi(u32 a, u32 b) { return __builtin_amdgcn_perm(b, a, 0x07060302u); }
DI rsrc_t mkrs(const void* p, u32 bytes) { return __builtin_amdgcn_make_buffer_rsrc((void*)p, 0, bytes, 0x00020000); }
DI u32x4 ld16(rsrc_t r, u32 off, int aux) { return aux ? __builtin_amdgcn_raw_buffer_load_b128(r, off, 0, SC1) : __builtin_amdgcn_raw_buffer_load_b128(r, off, 0, 0); }
DI u32x2 ld8(rsrc_t r, u32 off, int aux) { return aux ? __builtin_amdgcn_raw_buffer_load_b64(r, off, 0, SC1) : __builtin_amdgcn_raw_buffer_load_b64(r, off, 0, 0); }
DI u32 ld2(rsrc_t r, u32 off) { return __builtin_amdgcn_raw_buffer_load_b16(r, off, 0, 0); }
DI void st16sc(rsrc_t r, u32 off, u32x4 v) { __builtin_amdgcn_raw_buffer_store_b128(v, r, off, 0, SC1); }
DI u32x4 sane(u32x4 v) { return u32x4{min(v.x, SENT - 1), min(v.y, SENT - 1), min(v.z, SENT - 1), min(v.w, SENT - 1)}; }
DI bool fresh4(u32x4 v) { return max(max(v.x, v.y), max(v.z, v.w)) != SENT; }
DI void fence() { asm volatile("" ::: "memory"); }
DI void drain() { asm volatile("s_waitcnt vmcnt(0)" ::: "memory"); }
DI void tr8x4(const u32x2* v, u32x4* o) {
    o[0] = u32x4{permlo(v[0].x, v[1].x), permlo(v[2].x, v[3].x), permlo(v[4].x, v[5].x), permlo(v[6].x, v[7].x)};
    o[1] = u32x4{permhi(v[0].x, v[1].x), permhi(v[2].x, v[3].x), permhi(v[4].x, v[5].x), permhi(v[6].x, v[7].x)};
    o[2] = u32x4{permlo(v[0].y, v[1].y), permlo(v[2].y, v[3].y), permlo(v[4].y, v[5].y), permlo(v[6].y, v[7].y)};
    o[3] = u32x4{permhi(v[0].y, v[1].y), permhi(v[2].y, v[3].y), permhi(v[4].y, v[5].y), permhi(v[6].y, v[7].y)};
}
DI void raise_flag(char* ws, u32 set, int i, u32 tag) {
    __hip_atomic_store((GAS u32*)(ws + OFF_SYNC + set) + i, tag, __ATOMIC_RELAXED, __HIP_MEMORY_SCOPE_AGENT);
}
template <int NF>
DI void wait_flags(rsrc_t rws, u32 set, u32 tag, int l) {
    for (u32 spins = 0; spins < SPIN_MAX; ++spins) {
        fence();
        const u32x4 f = ld16(rws, OFF_SYNC + set + min(l, NF / 4 - 1) * 16, 1);
        if (__all(f.x == tag && f.y == tag && f.z == tag && f.w == tag)) break;
        __builtin_amdgcn_s_sleep(4);
    }
}

template <int N> struct ic { static constexpr int value = N; };
typedef f32x2 cf;
DI cf cmul(cf a, cf b) { return __builtin_elementwise_fma(cf{a.y, a.y}, cf{-b.y, b.x}, cf{a.x, a.x} * b); }
DI cf cexpk(float lre, float lim_rev, float k) {
    const float m = __builtin_amdgcn_exp2f(k * lre * 1.44269504088896341f);
    const float a = __builtin_amdgcn_fractf(k * lim_rev);
    return cf{m * cosr(a), m * sinr(a)};
}
DI u32 pkc(cf v) { return pk(v.x, v.y); }

struct Args {
    const u16 *x, *wr, *wi, *u, *Are, *Aim, *Bre, *Bim, *Cre, *Cim, *logdt, *dskip;
    char* ws;
    u16* out;
};

struct Start { u32x4 xv[2]; u32 gen; };
DI void p1_start(const Args& P, Start& st, const int t, const int w) {
    const int l = t & 63, b = blockIdx.x;
    const rsrc_t rws = mkrs(P.ws, WS_USED);
    st.gen = __builtin_amdgcn_raw_buffer_load_b32(rws, OFF_SYNC, 0, SC1);
    fence();
    const rsrc_t rx = mkrs(P.x, 2048 * 1280);
    const u32 ro = (b + 256 * w) * 1280;
    st.xv[0] = ld16(rx, ro + l * 16, 0);
    st.xv[1] = ld16(rx, ro + min(64 + l, 79) * 16, 0);
    fence();
#pragma unroll
    for (int i = 0; i < 2; ++i) st16sc(rws, OFF_X1 + b * X1_ROW + min(t + 512 * i, 767) * 16, u32x4{SENT, SENT, SENT, SENT});
    fence();
}
DI u32 warm_w(const Args& P, const int t, const int w) {
    const int l = t & 63, b = blockIdx.x;
    u32 pw = 0;
    if (w >= 6) {
        const int m = min(l, 47);
        pw = __builtin_amdgcn_raw_buffer_load_b32(mkrs(w == 6 ? P.wr : P.wi, 2048 * 642),
                                                  ((((b & 7) + 8 * (8 * (b >> 3) + m / 6)) * 642) & ~127u) + (m % 6) * 128, 0, 0);
    }
    return pw;
}
DI void p1_consts(char* smem, const int t, const int w) {
    const int l = t & 63, g = l >> 4, i16 = l & 15, b = blockIdx.x;
    if (t < 15) *(u32x2*)(smem + P_S + 321 * S_K + t * 8) = u32x2{0u, 0u};
    {
        float v[8];
        if (w < 4) {
#pragma unroll
            for (int j = 0; j < 8; ++j) {
                const int d = (j < 4) ? 4 * g + j : 16 + 4 * g + j - 4;
                const float c = cosr((float)d * (float)i16 * (w < 2 ? 0.05f : 0.03125f) - ((w & 1) ? 0.25f : 0.f));
                v[j] = (w >= 2) ? c : ((d < 20 && i16 <= 10) ? ((w & 1) ? -c : c) : 0.f);
            }
            *(u32x4*)(smem + P_C + w * 1024 + l * 16) = pk8(v);
        } else {
#pragma unroll
            for (int i = 0; i < 4; ++i)
                v[i] = cosr((float)(16 * (w & 1) + 4 * g + i) * (float)i16 * (1.0f / 640.0f) - ((w >= 6) ? 0.25f : 0.f));
            *(f32x4*)(smem + P_C + w * 1024 + l * 16) = f32x4{v[0], v[1], v[2], v[3]};
            if (w == 4) {
                const int kap = i16 >> 1, plo = i16 & 1;
#pragma unroll
                for (int e = 0; e < 4; ++e) {
                    const int a = 2 * g + (e >> 1), pl = e & 1;
                    const float c = cosr((float)kap * (float)(256 * a + b) * (1.0f / 2048.0f) - ((pl != plo) ? 0.25f : 0.f));
                    v[e] = (plo == 1 && pl == 0) ? -c : c;
                }
                *(u32x2*)(smem + P_C + 8192 + l * 8) = u32x2{pk(v[0], v[1]), pk(v[2], v[3])};
            }
        }
    }
    if (t == 0) *(u32x4*)(smem + L_WORD) = u32x4{0u, 0u, 0u, 0u};
    __syncthreads();
}
DI u32x2 p1_rows(const Args& P, char* smem, const Start& st, const int t, const int w) {
    const int l = t & 63, g = l >> 4, i16 = l & 15, b = blockIdx.x;
    const u32x4* xv = st.xv;
    const u32x4 f20c = *(const u32x4*)(smem + P_C + l * 16), f20s = *(const u32x4*)(smem + P_C + 1024 + l * 16);
    const u32x4 c32 = *(const u32x4*)(smem + P_C + 2048 + l * 16), s32 = *(const u32x4*)(smem + P_C + 3072 + l * 16);
    float twc[8], tws[8];
#pragma unroll
    for (int i = 0; i < 2; ++i) {
        const f32x4 a = *(const f32x4*)(smem + P_C + (4 + i) * 1024 + l * 16), bq = *(const f32x4*)(smem + P_C + (6 + i) * 1024 + l * 16);
#pragma unroll
        for (int e = 0; e < 4; ++e) { twc[4 * i + e] = a[e]; tws[4 * i + e] = bq[e]; }
    }
    const u32x2 fb = *(const u32x2*)(smem + P_C + 8192 + l * 8);
    const u32x4 ns32 = s32 ^ 0x80008000u, c32b = c32 ^ 0x80000000u, s32b = s32 ^ 0x80000000u, nc32b = c32b ^ 0x80008000u;
    {
        const int q = i16 >> 2, p = i16 & 3;
        const int d2a = 4 * g + q, d2b = (g == 0) ? 16 + q : d2a;
        const int offa = 64 * d2a + 8 * p, swa = ((d2a >> 2) & 1) << 5;
        const int offb = 64 * d2b + 8 * p, swb = ((d2b >> 2) & 1) << 5;
        char* xr = smem + P_X + w * 1280;
        *(u32x4*)(xr + ((l * 16) ^ (((l >> 4) & 1) << 5))) = xv[0];
        *(u32x4*)(xr + min(64 + l, 79) * 16) = xv[1];
        f32x4 tre[2], tim[2];
        const f32x4 zero = {0.f, 0.f, 0.f, 0.f};
        u32x4 xa[2];
#pragma unroll
        for (int tau = 0; tau < 2; ++tau) {
            const u32x2 a0 = trread(xr + offa + ((32 * tau) ^ swa));
            const u32x2 a1 = trread(xr + offb + ((32 * tau) ^ swb));
            xa[tau] = u32x4{a0.x, a0.y, a1.x, a1.y};
        }
#pragma unroll
        for (int tau = 0; tau < 2; ++tau) {
            tre[tau] = mfma16(xa[tau], f20c, zero);
            tim[tau] = mfma16(xa[tau], f20s, zero);
        }
        float pr[8], pi[8];
#pragma unroll
        for (int e = 0; e < 8; ++e) {
            const float a = tre[e >> 2][e & 3], bb = tim[e >> 2][e & 3];
            pr[e] = a * twc[e] + bb * tws[e];
            pi[e] = bb * twc[e] - a * tws[e];
        }
        const u32x4 bre = pk8(pr), bim = pk8(pi);
        const f32x4 z0 = mfma16(s32, bim, mfma16(c32, bre, zero));
        const f32x4 z1 = mfma16(ns32, bre, mfma16(c32, bim, zero));
        const f32x4 z2 = mfma16(s32b, bim, mfma16(c32b, bre, zero));
        const f32x4 z3 = mfma16(s32b, bre, mfma16(nc32b, bim, zero));
        const bool okA = i16 <= 10, okB = (i16 >= 1 && i16 <= 9), okB0 = okB || (i16 == 0 && g == 0);
        const int kA = i16 + 80 * g, kB = 320 - i16 - 80 * g;
        const int oA0 = (okA ? kA : 335) * S_K, oAs = okA ? 20 * S_K : 0;
        const int oB0 = (okB0 ? kB : 335) * S_K, oB1 = (okB ? kB - 20 : 335) * S_K, oBs = okB ? 20 * S_K : 0;
        char* st = smem + P_S + w * 4;
#pragma unroll
        for (int i = 0; i < 4; ++i) *(u32*)(st + oA0 + i * oAs) = pk(z0[i], z1[i]);
        *(u32*)(st + oB0) = pk(z2[0], z3[0]);
#pragma unroll
        for (int i = 1; i < 4; ++i) *(u32*)(st + oB1 - (i - 1) * oBs) = pk(z2[i], z3[i]);
    }
    drain();
    __syncthreads();
    if (t == 0) raise_flag(P.ws, F_X, b, st.gen + 1u);
    return fb;
}
DI void p1_finish(const Args& P, char* smem, const u32x2 fb, const int t, const int w) {
    const int l = t & 63, g = l >> 4, i16 = l & 15, b = blockIdx.x;
    {
        u32x2 av[3], dv[3];
#pragma unroll
        for (int it = 0; it < 3; ++it)
            av[it] = *(const u32x2*)(smem + P_S + (16 * min(w + 8 * it, 20) + i16) * S_K + g * 8);
#pragma unroll
        for (int it = 0; it < 3; ++it) {
            const f32x4 d = mfma16k16(av[it], fb, f32x4{0.f, 0.f, 0.f, 0.f});
            dv[it] = u32x2{pk(d[0], d[1]), pk(d[2], d[3])};
        }
        const int kap = i16 >> 1, po = (i16 & 1) * 56;
#pragma unroll
        for (int it = 0; it < 3; ++it) {
            const int k = 16 * min(w + 8 * it, 20) + 4 * g;
            const int cgk = min((k * 2341) >> 16, 11);
            const int o0 = po + (k - min(28 * cgk, 296)) * 2, o1 = po + (k - 296) * 2;
            const int d0 = (k < 324) ? (kap * 12 + cgk) * 128 + (((o0 >> 4) ^ kap) << 4) + (o0 & 8) : 12288 + l * 8;
            const int d1 = (k >= 296 && k < 308) ? (kap * 12 + 11) * 128 + (((o1 >> 4) ^ kap) << 4) + (o1 & 8) : d0;
            *(u32x2*)(smem + P_X + d0) = dv[it];
            *(u32x2*)(smem + P_X + d1) = dv[it];
        }
    }
    __syncthreads();
    const rsrc_t rws = mkrs(P.ws, WS_USED);
#pragma unroll
    for (int i = 0; i < 2; ++i) {
        const int m = min(t + 512 * i, 767), pc = m >> 3, kp = (pc * 43) >> 9;
        st16sc(rws, OFF_X1 + b * X1_ROW + m * 16, sane(*(const u32x4*)(smem + P_X + pc * 128 + (((m & 7) ^ kp) << 4))));
    }
}

DI void p3_consts(char* tb, const int w, const int l, const int ai) {
    const int g = l >> 4, i16 = l & 15;
    float v[8];
    if (w < 4) {
        const float d1 = (float)(16 * (w & 1) + i16);
#pragma unroll
        for (int j = 0; j < 8; ++j) v[j] = cosr((float)(8 * g + j) * d1 * 0.03125f - ((w >= 2) ? 0.25f : 0.f));
        *(u32x4*)(tb + w * 1024 + l * 16) = pk8(v);
    } else {
        const int tau = w & 1;
        const float d1 = (float)(16 * tau + i16);
#pragma unroll
        for (int i = 0; i < 4; ++i)
            v[i] = cosr(d1 * (float)(4 * g + i) * (1.0f / 640.0f) - ((w >= 6) ? 0.25f : 0.f)) * (1.0f / (2048.0f * 640.0f));
        *(f32x4*)(tb + w * 1024 + l * 16) = f32x4{v[0], v[1], v[2], v[3]};
        if (w < 6) {
#pragma unroll
            for (int j = 0; j < 8; ++j) {
                const int k2 = 4 * g + (j & 3), d2 = 16 * tau + i16;
                const float wgt = (k2 > 10 || d2 >= 20) ? 0.f : ((k2 == 0 || k2 == 10) ? 1.f : 2.f);
                const float c = cosr((float)k2 * (float)d2 * 0.05f - ((j < 4) ? 0.f : 0.25f));
                v[j] = (j < 4) ? wgt * c : -wgt * c;
            }
            *(u32x4*)(tb + (8 + tau) * 1024 + l * 16) = pk8(v);
        }
    }
    if (w < 3) {
        const int a = i16 >> 1, plo = i16 & 1;
#pragma unroll
        for (int e = 0; e < 4; ++e) {
            const int kk = 2 * g + (e >> 1), pp = e & 1;
            const float c = cosr((float)kk * (float)(256 * a + ai + 96 * w) * (1.0f / 2048.0f) - ((pp != plo) ? 0.25f : 0.f));
            v[e] = (plo == 0 && pp == 1) ? -c : c;
        }
        *(u32x2*)(tb + 10240 + w * 512 + l * 8) = u32x2{pk(v[0], v[1]), pk(v[2], v[3])};
    }
}

DI void role_a(const Args& P, char* smem, const int t, const int w, const int ka, const int cg) {
    const int l = t & 63, g = l >> 4, i16 = l & 15;
    const int ai = ka * 12 + cg, nset = ai < 64 ? 3 : 2;
    const int colbase = min(28 * cg, 296);
    const rsrc_t rws = mkrs(P.ws, WS_USED);
    Start st;
    p1_start(P, st, t, w);
    p1_consts(smem, t, w);
    const u32x2 fb = p1_rows(P, smem, st, t, w);
    const u32 pw = warm_w(P, t, w);
    fence();
    p1_finish(P, smem, fb, t, w);
    const u32 tag = st.gen + 1u;
    volatile LAS u32* word = (volatile LAS u32*)(smem + L_WORD);
    u32x4 f1re, f1im, f2re, f2im;
    float t2c[4], t2s[4];
    if (w < 4) {
        const int wpl = w >> 1, dl = (ka + colbase) & 7;
        {
            float c[8], s[8];
#pragma unroll
            for (int j = 0; j < 8; ++j) {
                const float a = (float)(8 * (g & 1) + j) * (float)i16 * 0.0625f;
                const float co = cosr(a), si = sinr(a);
                c[j] = (g < 2) ? co : si;
                s[j] = (g < 2) ? -si : co;
            }
            f1re = pk8(c); f1im = pk8(s);
#pragma unroll
            for (int j = 0; j < 4; ++j) {
                const float pr = (float)(4 * g + j) * (float)i16;
                const float co = cosr(pr * 0.0625f), si = sinr(pr * 0.0625f);
                c[j] = co; c[4 + j] = si;
                s[j] = -si; s[4 + j] = co;
                t2c[j] = cosr(pr * (1.0f / 256.0f));
                t2s[j] = sinr(pr * (1.0f / 256.0f));
            }
            f2re = pk8(c); f2im = pk8(s);
        }
        const rsrc_t rw = mkrs(wpl ? P.wi : P.wr, 2048 * 642);
        u32x4 wv[2][8];
#pragma unroll
        for (int it = 0; it < 2; ++it) {
            const int task = min((t & 127) + 128 * it, 159), r = task / 5, ck = task - 5 * r;
#pragma unroll
            for (int e = 0; e < 8; ++e)
                wv[it][e] = ld16(rw, ((((ka + 8 * ((r & 15) + 16 * (8 * (r >> 4) + e))) * 321 + colbase) * 2) & ~15u) + ck * 16, 0);
        }
        fence();
        p3_consts(smem + L_P3C, w, l, ai);
#pragma unroll
        for (int it = 0; it < 2; ++it) {
            const int task = min((t & 127) + 128 * it, 159), r = task / 5, ck = task - 5 * r;
#pragma unroll
            for (int c = 0; c < 8; ++c) {
                const int col = 8 * ck + c - dl, m = col * 2 + wpl, d = c >> 1;
                const u32x4* x = wv[it];
                const u32x4 o = (c & 1) ? u32x4{permhi(x[0][d], x[1][d]), permhi(x[2][d], x[3][d]), permhi(x[4][d], x[5][d]), permhi(x[6][d], x[7][d])}
                                        : u32x4{permlo(x[0][d], x[1][d]), permlo(x[2][d], x[3][d]), permlo(x[4][d], x[5][d]), permlo(x[6][d], x[7][d])};
                const int dst = ((u32)col < 28u) ? L_W + m * 512 + ((((r & 15) * 2 + (r >> 4)) ^ ((m >> 3) & 7)) * 16) : L_DUMP + l * 16;
                *(u32x4*)(smem + dst) = o;
            }
        }
    } else {
        {
            float c[8], s[8];
#pragma unroll
            for (int j = 0; j < 8; ++j) {
                const float a = (float)(8 * (g & 1) + j) * (float)i16 * 0.0625f;
                const float co = cosr(a), si = sinr(a);
                c[j] = (g < 2) ? co : si;
                s[j] = (g < 2) ? -si : co;
            }
            f1re = pk8(c); f1im = pk8(s);
#pragma unroll
            for (int j = 0; j < 4; ++j) {
                const float pr = (float)(4 * g + j) * (float)i16;
                const float co = cosr(pr * 0.0625f), si = sinr(pr * 0.0625f);
                c[j] = co; c[4 + j] = si;
                s[j] = -si; s[4 + j] = co;
                t2c[j] = cosr(pr * (1.0f / 256.0f));
                t2s[j] = sinr(pr * (1.0f / 256.0f));
            }
            f2re = pk8(c); f2im = pk8(s);
        }
        const int task = min(t - 256, 223), oc = task / 7, ck = task - 7 * oc;
        const u32 base = OFF_X1 + (8 * oc) * X1_ROW + ai * 128 + ck * 16;
        u32x4 v[8];
        p3_consts(smem + L_P3C, w, l, ai);
        drain();
        if (w == 4) {
            wait_flags<256>(rws, F_X, tag, l);
            if (l == 0) {
                *word = 1u;
                if (ai == 0) __hip_atomic_store((GAS u32*)(P.ws + OFF_SYNC), tag, __ATOMIC_RELAXED, __HIP_MEMORY_SCOPE_AGENT);
            }
        }
        for (u32 spins = 0; spins < SPIN_MAX && *word == 0u; ++spins) __builtin_amdgcn_s_sleep(1);
        for (u32 spins = 0; spins < SPIN_MAX; ++spins) {
            fence();
            bool ok = true;
#pragma unroll
            for (int e = 0; e < 8; ++e) v[e] = ld16(rws, base + e * X1_ROW, 1);
#pragma unroll
            for (int e = 0; e < 8; ++e) ok &= fresh4(v[e]);
            if (__all(ok)) break;
            __builtin_amdgcn_s_sleep(1);
        }
#pragma unroll
        for (int c = 0; c < 8; ++c) {
            const int el = 8 * ck + c, pl = el >= 28, m = (el - 28 * pl) * 2 + pl, d = c >> 1;
            const u32x4 o = (c & 1) ? u32x4{permhi(v[0][d], v[1][d]), permhi(v[2][d], v[3][d]), permhi(v[4][d], v[5][d]), permhi(v[6][d], v[7][d])}
                                    : u32x4{permlo(v[0][d], v[1][d]), permlo(v[2][d], v[3][d]), permlo(v[4][d], v[5][d]), permlo(v[6][d], v[7][d])};
            *(u32x4*)(smem + L_C + m * 512 + ((oc ^ ((m >> 3) & 7)) * 16)) = o;
        }
    }
    const u32x4 ire = {f2re.x, f2re.y, f2re.z ^ 0x80008000u, f2re.w ^ 0x80008000u};
    const u32x4 iim = {f2im.x ^ 0x80008000u, f2im.y ^ 0x80008000u, f2im.z, f2im.w};
    asm volatile("" :: "v"(pw));
    __syncthreads();
#pragma unroll
    for (int i = 0; i < 4; ++i) st16sc(rws, OFF_Y1 + ai * Y1_BLK + (t + 512 * i) * 16, u32x4{SENT, SENT, SENT, SENT});
    fence();
    {
        const int q = i16 >> 2, p = i16 & 3;
        const f32x4 zero = {0.f, 0.f, 0.f, 0.f};
        auto fetch = [&](int col, u32x4& a, u32x2& wre, u32x2& wim) {
            const int mi = col * 2 + (g >> 1), swi = (mi >> 3) & 7;
            const char* ib = smem + L_C + mi * 512 + (p & 1) * 8;
            const u32x2 a0 = trread(ib + (((2 * (8 * (g & 1) + q) + (p >> 1)) ^ swi) * 16));
            const u32x2 a1 = trread(ib + (((2 * (8 * (g & 1) + 4 + q) + (p >> 1)) ^ swi) * 16));
            a = u32x4{a0.x, a0.y, a1.x, a1.y};
            const int m0 = col * 2, sw = (m0 >> 3) & 7;
            const int woff = (((i16 * 2 + (g >> 1)) ^ sw) * 16) + (g & 1) * 8;
            wre = *(const u32x2*)(smem + L_W + m0 * 512 + woff);
            wim = *(const u32x2*)(smem + L_W + (m0 + 1) * 512 + woff);
        };
        auto columns = [&](auto nc, const u32x4* a, const u32x2* wre, const u32x2* wim, u32x2* ore, u32x2* oim) {
            constexpr int NC = decltype(nc)::value;
            f32x4 re[NC], im[NC];
            u32x4 d[NC];
#pragma unroll
            for (int c = 0; c < NC; ++c) { re[c] = mfma16(a[c], f1re, zero); im[c] = mfma16(a[c], f1im, zero); }
            __builtin_amdgcn_sched_barrier(0);
#pragma unroll
            for (int c = 0; c < NC; ++c) {
                float x[8];
#pragma unroll
                for (int i = 0; i < 4; ++i) {
                    x[i] = re[c][i] * t2c[i] + im[c][i] * t2s[i];
                    x[4 + i] = im[c][i] * t2c[i] - re[c][i] * t2s[i];
                }
                d[c] = pk8(x);
            }
            __builtin_amdgcn_sched_barrier(0);
#pragma unroll
            for (int c = 0; c < NC; ++c) { re[c] = mfma16(f2re, d[c], zero); im[c] = mfma16(f2im, d[c], zero); }
            __builtin_amdgcn_sched_barrier(0);
#pragma unroll
            for (int c = 0; c < NC; ++c) {
                float x[8];
#pragma unroll
                for (int i = 0; i < 4; ++i) {
                    const float a_ = (i & 1) ? bfhi(wre[c][i >> 1]) : bflo(wre[c][i >> 1]);
                    const float b_ = (i & 1) ? bfhi(wim[c][i >> 1]) : bflo(wim[c][i >> 1]);
                    x[i] = re[c][i] * a_ - im[c][i] * b_;
                    x[4 + i] = re[c][i] * b_ + im[c][i] * a_;
                }
                d[c] = pk8(x);
            }
            __builtin_amdgcn_sched_barrier(0);
#pragma unroll
            for (int c = 0; c < NC; ++c) { re[c] = mfma16(d[c], ire, zero); im[c] = mfma16(d[c], iim, zero); }
            __builtin_amdgcn_sched_barrier(0);
#pragma unroll
            for (int c = 0; c < NC; ++c) {
                float x[8];
#pragma unroll
                for (int i = 0; i < 4; ++i) {
                    x[i] = re[c][i] * t2c[i] - im[c][i] * t2s[i];
                    x[4 + i] = im[c][i] * t2c[i] + re[c][i] * t2s[i];
                }
                d[c] = pk8(x);
            }
            __builtin_amdgcn_sched_barrier(0);
#pragma unroll
            for (int c = 0; c < NC; ++c) { re[c] = mfma16(d[c], ire, zero); im[c] = mfma16(d[c], iim, zero); }
            __builtin_amdgcn_sched_barrier(0);
#pragma unroll
            for (int c = 0; c < NC; ++c) {
                ore[c] = u32x2{pk(re[c][0], re[c][1]), pk(re[c][2], re[c][3])};
                oim[c] = u32x2{pk(im[c][0], im[c][1]), pk(im[c][2], im[c][3])};
            }
        };
        auto put = [&](int col, const u32x2 ore, const u32x2 oim) {
            const int m0 = col * 2, sw = (m0 >> 3) & 7;
            const int ooff = (((2 * i16 + (g >> 1)) ^ sw) * 16) + (g & 1) * 8;
            *(u32x2*)(smem + L_C + m0 * 512 + ooff) = ore;
            *(u32x2*)(smem + L_C + (m0 + 1) * 512 + ooff) = oim;
        };
        u32x4 a[4];
        u32x2 wre[4], wim[4], ore[4], oim[4];
        if (w < 4) {
#pragma unroll
            for (int c = 0; c < 4; ++c) fetch(w + 8 * c, a[c], wre[c], wim[c]);
            columns(ic<4>{}, a, wre, wim, ore, oim);
#pragma unroll
            for (int c = 0; c < 4; ++c) put(w + 8 * c, ore[c], oim[c]);
        } else {
#pragma unroll
            for (int c = 0; c < 3; ++c) fetch(w + 8 * c, a[c], wre[c], wim[c]);
            columns(ic<3>{}, a, wre, wim, ore, oim);
#pragma unroll
            for (int c = 0; c < 3; ++c) put(w + 8 * c, ore[c], oim[c]);
        }
    }
    drain();
    __syncthreads();
    if (t == 0) raise_flag(P.ws, F_Y, ai, tag);
    {
        const int q = i16 >> 2, p = i16 & 3;
        const int G = 2 * w + (g >> 1), pl = g & 1;
        u32x2 v[7];
#pragma unroll
        for (int it = 0; it < 7; ++it) {
            const int m = (4 * it + q) * 2 + pl;
            v[it] = trread(smem + L_C + m * 512 + (((2 * G + (p >> 1)) ^ ((m >> 3) & 7)) * 16) + (p & 1) * 8);
        }
#pragma unroll
        for (int it = 0; it < 7; ++it) {
            const int o = pl * 56 + it * 8;
            *(u32x2*)(smem + L_W + (16 * G + i16) * 128 + (((o >> 4) ^ (i16 & 7)) << 4) + (o & 8)) = v[it];
        }
    }
    __syncthreads();
#pragma unroll
    for (int i = 0; i < 4; ++i) {
        const int m = t + 512 * i;
        st16sc(rws, OFF_Y1 + ai * Y1_BLK + m * 16, sane(*(const u32x4*)(smem + L_W + (m >> 3) * 128 + (((m & 7) ^ ((m >> 3) & 7)) << 4))));
    }

    u32x4 C[2], S[2], nsS[2], sC[2], F[2];
    float twc[8], tws[8];
    u32x2 fa[3];
    {
        const char* tb = smem + L_P3C;
        const u32 sgn = (g >= 2) ? 0x80008000u : 0u;
#pragma unroll
        for (int tau = 0; tau < 2; ++tau) {
            C[tau] = *(const u32x4*)(tb + tau * 1024 + l * 16);
            S[tau] = *(const u32x4*)(tb + (2 + tau) * 1024 + l * 16);
            nsS[tau] = S[tau] ^ (sgn ^ 0x80008000u);
            sC[tau] = C[tau] ^ sgn;
            const f32x4 a = *(const f32x4*)(tb + (4 + tau) * 1024 + l * 16), bq = *(const f32x4*)(tb + (6 + tau) * 1024 + l * 16);
#pragma unroll
            for (int i = 0; i < 4; ++i) { twc[4 * tau + i] = a[i]; tws[4 * tau + i] = bq[i]; }
            F[tau] = *(const u32x4*)(tb + (8 + tau) * 1024 + l * 16);
        }
#pragma unroll
        for (int j = 0; j < 3; ++j) fa[j] = *(const u32x2*)(tb + 10240 + j * 512 + l * 8);
    }
    drain();
    if (w == 7) {
        wait_flags<96>(rws, F_Y, tag, l);
        if (l == 0) *word = 2u;
    }
    {
        u32 off[4];
#pragma unroll
        for (int i = 0; i < 4; ++i) {
            const int m = min(t + 512 * i, nset * 672 - 1), j = (m >= 672) + (m >= 1344), mm = m - 672 * j, ab = mm / 7;
            off[i] = OFF_Y1 + ab * Y1_BLK + (ai + 96 * j) * 128 + (mm - 7 * ab) * 16;
        }
        for (u32 spins = 0; spins < SPIN_MAX && *word != 2u; ++spins) __builtin_amdgcn_s_sleep(1);
        u32x4 v[4];
        for (u32 spins = 0; spins < SPIN_MAX; ++spins) {
            fence();
#pragma unroll
            for (int i = 0; i < 4; ++i) v[i] = ld16(rws, off[i], 1);
            if (__all(fresh4(v[0]) && fresh4(v[1]) && fresh4(v[2]) && fresh4(v[3]))) break;
            __builtin_amdgcn_s_sleep(1);
        }
#pragma unroll
        for (int i = 0; i < 4; ++i) *(u32x4*)(smem + L_X + min(t + 512 * i, nset * 672 - 1) * 16) = v[i];
    }
    __syncthreads();
    {
        const int q = i16 >> 2, p = i16 & 3;
        const int kap = 2 * g + (q >> 1), pl = q & 1;
        u32x2 bfr[3][3];
#pragma unroll
        for (int it = 0; it < 3; ++it) {
            const int T = min(w + 8 * it, 20);
            const int k = min(16 * T + 4 * p, 320);
            const int cgk = min((k * 2341) >> 16, 11), idx = k - min(28 * cgk, 296);
            const int src = L_X + (kap * 12 + cgk) * 112 + pl * 56 + idx * 2;
#pragma unroll
            for (int j = 0; j < 3; ++j) bfr[it][j] = trread(smem + src + j * 10752);
        }
        u32x2 dv[3][3];
#pragma unroll
        for (int it = 0; it < 3; ++it)
#pragma unroll
            for (int j = 0; j < 3; ++j) {
                const f32x4 d = mfma16k16(fa[j], bfr[it][j], f32x4{0.f, 0.f, 0.f, 0.f});
                dv[it][j] = u32x2{pk(d[0], d[1]), pk(d[2], d[3])};
            }
#pragma unroll
        for (int it = 0; it < 3; ++it)
#pragma unroll
            for (int j = 0; j < 3; ++j)
                *(u32x2*)(smem + L_S + j * S_SZ + (16 * min(w + 8 * it, 20) + i16) * S_K + g * 8) = dv[it][j];
    }
    __syncthreads();
    {
        const int k2 = i16 <= 10 ? i16 : 10;
        const int k0 = k2 + 160 * g;
        const int kofs = ((g < 2) ? k0 : 640 - k0) * S_K + w * 4, kstep = (g < 2) ? 20 * S_K : -20 * S_K;
        const rsrc_t ro = mkrs(P.out, 2048 * 2560);
        u32 dd[3][8];
#pragma unroll
        for (int j = 0; j < 3; ++j)
#pragma unroll
            for (int e = 0; e < 8; ++e) dd[j][e] = *(const u32*)(smem + L_S + j * S_SZ + kofs + e * kstep);
        const f32x4 zero = {0.f, 0.f, 0.f, 0.f};
        f32x4 vre[3][2], vim[3][2];
#pragma unroll
        for (int j = 0; j < 3; ++j) {
            const u32x4 are = {permlo(dd[j][0], dd[j][1]), permlo(dd[j][2], dd[j][3]), permlo(dd[j][4], dd[j][5]), permlo(dd[j][6], dd[j][7])};
            const u32x4 aim = {permhi(dd[j][0], dd[j][1]), permhi(dd[j][2], dd[j][3]), permhi(dd[j][4], dd[j][5]), permhi(dd[j][6], dd[j][7])};
#pragma unroll
            for (int tau = 0; tau < 2; ++tau) {
                vre[j][tau] = mfma16(aim, nsS[tau], mfma16(are, C[tau], zero));
                vim[j][tau] = mfma16(aim, sC[tau], mfma16(are, S[tau], zero));
            }
        }
        __builtin_amdgcn_sched_barrier(0);
        u32x4 va[3][2];
#pragma unroll
        for (int j = 0; j < 3; ++j)
#pragma unroll
            for (int tau = 0; tau < 2; ++tau) {
                float pr[8];
#pragma unroll
                for (int i = 0; i < 4; ++i) {
                    pr[i] = vre[j][tau][i] * twc[4 * tau + i] - vim[j][tau][i] * tws[4 * tau + i];
                    pr[4 + i] = vim[j][tau][i] * twc[4 * tau + i] + vre[j][tau][i] * tws[4 * tau + i];
                }
                va[j][tau] = pk8(pr);
            }
        __builtin_amdgcn_sched_barrier(0);
        f32x4 y[3][2][2];
#pragma unroll
        for (int j = 0; j < 3; ++j)
#pragma unroll
            for (int dl = 0; dl < 2; ++dl) {
                y[j][dl][0] = mfma16(va[j][0], F[dl], zero);
                y[j][dl][1] = mfma16(va[j][1], F[dl], zero);
            }
        __builtin_amdgcn_sched_barrier(0);
#pragma unroll
        for (int j = 0; j < 3; ++j) {
            const u32 orow = (ai + 96 * j + 256 * w) * 2560;
#pragma unroll
            for (int dl = 0; dl < 2; ++dl) {
                u32 a0 = pk(y[j][dl][0][0], y[j][dl][0][1]), a1 = pk(y[j][dl][0][2], y[j][dl][0][3]);
                u32 b0 = pk(y[j][dl][1][0], y[j][dl][1][1]), b1 = pk(y[j][dl][1][2], y[j][dl][1][3]);
                auto s0 = __builtin_amdgcn_permlane16_swap(a0, b0, false, false);
                auto s1 = __builtin_amdgcn_permlane16_swap(a1, b1, false, false);
                const int d2 = 16 * dl + i16;
                const u32 off = (d2 < 20 && j < nset) ? orow + (32 * d2 + (g & 1) * 16 + (g >> 1) * 8) * 2 : 0x80000000u;
                __builtin_amdgcn_raw_buffer_store_b128(u32x4{s0[0], s1[0], s0[1], s1[1]}, ro, off, 0, 0);
            }
        }
    }
}

DI void role_b(const Args& P, char* smem, const int t, const int w, const int chb) {
    const int l = t & 63, g = l >> 4, i16 = l & 15;
    const int ch = w & 3, rh = w >> 2;
    const rsrc_t rws = mkrs(P.ws, WS_USED);
    Start st;
    p1_start(P, st, t, w);
    p1_consts(smem, t, w);
    const u32x2 fb = p1_rows(P, smem, st, t, w);
    u32 pv[12], ldt = 0, pu = 0;
    const rsrc_t ru = mkrs(P.u, 2048 * 1280);
    if (w < 4) {
        const u32 nb = 640 * 128 * 2;
        const rsrc_t r0 = mkrs(P.Are, nb), r1 = mkrs(P.Aim, nb), r2 = mkrs(P.Bre, nb), r3 = mkrs(P.Bim, nb), r4 = mkrs(P.Cre, nb), r5 = mkrs(P.Cim, nb);
#pragma unroll
        for (int h = 0; h < 2; ++h) {
            const u32 off = ((4 * chb + w) * 128 + 64 * h + l) * 2;
            pv[6 * h + 0] = ld2(r0, off); pv[6 * h + 1] = ld2(r1, off); pv[6 * h + 2] = ld2(r2, off);
            pv[6 * h + 3] = ld2(r3, off); pv[6 * h + 4] = ld2(r4, off); pv[6 * h + 5] = ld2(r5, off);
        }
        ldt = ld2(mkrs(P.logdt, 1280), (4 * chb + w) * 2);
    }
    const u32 dks = ld2(mkrs(P.dskip, 1280), (4 * chb + ch) * 2);
    if (w < 5) {
        const int xcd = blockIdx.x & 7, jx = blockIdx.x >> 3;
        const int b0 = (160 * xcd) & ~127, nl = (160 * xcd + 160 - b0 + 127) >> 7;
        const int m = min(t, 103 * 3 - 1), r = m / 3;
        pu = __builtin_amdgcn_raw_buffer_load_b32(ru, min(103 * jx + r, 2047) * 1280 + b0 + min(m - 3 * r, nl - 1) * 128, 0, 0);
    }
    const u32 pw = warm_w(P, t, w);
    fence();
    p1_finish(P, smem, fb, t, w);
    if (w >= 4) {
        const int tt = t - 256;
        fence();
        u32x2 gv[8];
#pragma unroll
        for (int e = 0; e < 8; ++e) gv[e] = ld8(ru, (8 * tt + e) * 1280 + chb * 8, 0);
        fence();
        if (tt < 128) *(u32x4*)(smem + L_U + (tt >> 5) * PB_U + (tt & 31) * 16) = u32x4{0u, 0u, 0u, 0u};
        else if (tt < 144) {
            const int v = tt - 128;
            *(u32x4*)(smem + L_K + (v >> 2) * PB_KS + ((v >> 1) & 1) * PB_K1 + 4096 + (v & 1) * 16) = u32x4{0u, 0u, 0u, 0u};
        }
        u32x4 o[4];
        tr8x4(gv, o);
#pragma unroll
        for (int c = 0; c < 4; ++c) *(u32x4*)(smem + L_U + c * PB_U + 512 + tt * 16) = o[c];
    } else {
        char* sc = smem + w * KG_SZ;
        const float dt = rbf(expf(bf2f(ldt)));
        f32x4 kacc[3];
#pragma unroll
        for (int T = 0; T < 3; ++T) kacc[T] = f32x4{0.f, 0.f, 0.f, 0.f};
#pragma unroll 1
        for (int h = 0; h < 2; ++h) {
            const float lre = dt * bf2f(h ? pv[6] : pv[0]), lim = dt * bf2f(h ? pv[7] : pv[1]) * 0.15915494309189535f;
            const cf c = cmul(cf{bf2f(h ? pv[10] : pv[4]), bf2f(h ? pv[11] : pv[5])}, cf{dt * bf2f(h ? pv[8] : pv[2]), dt * bf2f(h ? pv[9] : pv[3])});
            char* rowp = sc + l * 4;
            const cf z1 = cexpk(lre, lim, 1.f), z8 = cexpk(lre, lim, 8.f), z128 = cexpk(lre, lim, 128.f);
            const cf z2 = cmul(z1, z1), z4 = cmul(z2, z2), z6 = cmul(z2, z4);
            const cf z8c = {z8.x, -z8.y};
            cf v = {1.f, 0.f}, r0 = c, r1 = cmul(c, z128);
            cf q = cmul(r1, z128);
#pragma unroll
            for (int j = 0; j < 8; ++j) {
                *(u32*)(rowp + KG_B + (2 * j) * KG_RS) = pk(v.x, v.y);
                v = cmul(v, z8c);
                *(u32*)(rowp + KG_B + (2 * j + 1) * KG_RS) = pk(v.x, v.y);
                v = cmul(v, z8c);
                *(u32*)(rowp + j * KG_RS) = pkc(r0);
                *(u32*)(rowp + (8 + j) * KG_RS) = pkc(r1);
                r0 = cmul(r0, z1);
                r1 = cmul(r1, z1);
#pragma unroll
                for (int e = 0; e < 2; ++e) {
                    const int hh = 2 + 2 * j + e;
                    if (hh < 4) {
                        const int r = 16 + (hh - 2) * 4;
                        *(u32*)(rowp + r * KG_RS) = pkc(q);
                        *(u32*)(rowp + (r + 1) * KG_RS) = pkc(cmul(q, z2));
                        *(u32*)(rowp + (r + 2) * KG_RS) = pkc(cmul(q, z4));
                        *(u32*)(rowp + (r + 3) * KG_RS) = pkc(cmul(q, z6));
                    } else if (hh < 8) {
                        const int r = 24 + (hh - 4) * 2;
                        *(u32*)(rowp + r * KG_RS) = pkc(q);
                        *(u32*)(rowp + (r + 1) * KG_RS) = pkc(cmul(q, z4));
                    } else if (hh <= 16) {
                        *(u32*)(rowp + (32 + hh - 8) * KG_RS) = pkc(q);
                    }
                    if (hh < 16) q = cmul(q, z128);
                }
            }
            u32x4 bf[4], af[3][4];
#pragma unroll
            for (int ks = 0; ks < 4; ++ks) {
                bf[ks] = *(const u32x4*)(sc + KG_B + i16 * KG_RS + g * 16 + ks * 64);
#pragma unroll
                for (int T = 0; T < 3; ++T) af[T][ks] = *(const u32x4*)(sc + (16 * T + i16) * KG_RS + g * 16 + ks * 64);
            }
#pragma unroll
            for (int T = 0; T < 3; ++T)
#pragma unroll
                for (int ks = 0; ks < 4; ++ks) kacc[T] = mfma16(af[T][ks], bf[ks], kacc[T]);
        }
        char* tbl = smem + L_T + w * KG_T;
#pragma unroll
        for (int T = 0; T < 3; ++T)
#pragma unroll
            for (int i = 0; i < 4; ++i) {
                const int row = 16 * T + 4 * g + i;
                int e;
                if (T == 0) e = 128 * (row >> 3) + (row & 7);
                else if (T == 1) e = (g < 2) ? 256 + 128 * g + 2 * i : 512 + 256 * (g - 2) + 128 * (i >> 1) + 4 * (i & 1);
                else e = 1024 + 128 * (row - 32);
                const bool ok = row < 40 || (row == 40 && i16 == 0);
                *(u16*)(tbl + (ok ? (e + 8 * i16) * 2 : 4110)) = (u16)(pk(kacc[T][i], kacc[T][i]) & 0xffffu);
            }
        u32 v[4][8], vm[4];
#pragma unroll
        for (int it = 0; it < 4; ++it) {
            const int tt = l + 64 * it, a = 255 - tt;
            const u32 code = (a < 32) ? 0x76543210u : (a < 64) ? 0x86444200u : (a < 128) ? 0x88444000u : ((a & 1) ? 0x88880000u : 0x88800000u);
            const int lastoff = (a <= 32) ? 7 : 8;
            const char* tb = tbl + 16 * a;
#pragma unroll
            for (int e = 0; e < 8; ++e) v[it][e] = *(const u16*)(tb + 2 * ((code >> (4 * e)) & 15));
            vm[it] = *(const u16*)(tb + ((a == 0) ? 0 : 2 * lastoff - 16));
            vm[it] = (a == 0) ? 0u : vm[it];
        }
#pragma unroll
        for (int it = 0; it < 4; ++it) {
            char* kb = smem + L_K + w * PB_KS + (l + 64 * it) * 16;
            const u32* x = v[it];
            *(u32x4*)(kb) = u32x4{x[7] | (x[6] << 16), x[5] | (x[4] << 16), x[3] | (x[2] << 16), x[1] | (x[0] << 16)};
            *(u32x4*)(kb + PB_K1) = u32x4{x[6] | (x[5] << 16), x[4] | (x[3] << 16), x[2] | (x[1] << 16), x[0] | (vm[it] << 16)};
        }
    }
    asm volatile("" :: "v"(pw ^ pu));
    __syncthreads();
    const int mu = g >> 1, hp = g & 1;
    const int x0 = 2047 - 16 * mu - i16 + 8 * hp;
    const char* ap = smem + L_K + ch * PB_KS + ((x0 & 1) ? PB_K1 + (x0 - 1) * 2 : x0 * 2);
    const char* bp = smem + L_U + ch * PB_U + (256 + 16 * (i16 - mu) + 8 * hp) * 2;
    f32x4 acc[8];
#pragma unroll
    for (int c = 0; c < 8; ++c) acc[c] = f32x4{0.f, 0.f, 0.f, 0.f};
    {
        u32x4 A[2][8], B[2][8];
        const char* ap0 = ap - 256 * rh;
        const char* bp0 = bp - 256 * rh;
#pragma unroll
        for (int p = 0; p < 8; ++p) {
            const u32* a = (const u32*)(ap0 - 512 * p);
            B[0][p] = *(const u32x4*)(bp0 + 512 * p);
            A[0][p] = u32x4{a[0], a[1], a[2], a[3]};
        }
#pragma unroll
        for (int r = 0; r < 4; ++r) {
            __builtin_amdgcn_sched_barrier(0);
            if (r < 3) {
#pragma unroll
                for (int p = 0; p < 8; ++p) {
                    const u32* a = (const u32*)(ap0 - 64 * (r + 1) - 512 * p);
                    B[(r + 1) & 1][p] = *(const u32x4*)(bp0 - 64 * (r + 1) + 512 * p);
                    A[(r + 1) & 1][p] = u32x4{a[0], a[1], a[2], a[3]};
                }
            }
#pragma unroll
            for (int p = 0; p < 8; ++p)
#pragma unroll
                for (int q = 0; q + p < 8; ++q) acc[p + q] = mfma16(A[r & 1][p], B[r & 1][q], acc[p + q]);
            if (r < 3) {
#pragma unroll
                for (int k = 0; k < 12; ++k) {
                    __builtin_amdgcn_sched_group_barrier(0x008, 3, 0);
                    __builtin_amdgcn_sched_group_barrier(0x100, 2, 0);
                }
            }
        }
        __builtin_amdgcn_sched_barrier(0);
    }
    char* rb = smem + L_R;
#pragma unroll
    for (int c = 0; c < 4; ++c) *(f32x4*)(rb + (w * 4 + c) * 1024 + l * 16) = rh ? acc[c] : acc[4 + c];
    __syncthreads();
    char* yi = smem + L_Y;
    const float dsk = bf2f(dks);
    {
        f32x4 o[4];
        u32x2 uu[4];
#pragma unroll
        for (int c = 0; c < 4; ++c) {
            o[c] = *(const f32x4*)(rb + ((w ^ 4) * 4 + c) * 1024 + l * 16);
            uu[c] = *(const u32x2*)(smem + L_U + ch * PB_U + (256 + 256 * (4 * rh + c) + 16 * i16 + 4 * g) * 2);
        }
        u32x2 res[4];
#pragma unroll
        for (int c = 0; c < 4; ++c) {
            const f32x4 mine = rh ? acc[4 + c] : acc[c];
            float v[4];
#pragma unroll
            for (int i = 0; i < 4; ++i) {
                const float uf = (i & 1) ? bfhi(i < 2 ? uu[c].x : uu[c].y) : bflo(i < 2 ? uu[c].x : uu[c].y);
                v[i] = rbf(mine[i] + o[c][i]) + rbf(uf * dsk);
            }
            res[c] = u32x2{pk(v[0], v[1]), pk(v[2], v[3])};
        }
#pragma unroll
        for (int c = 0; c < 4; ++c) *(u32x2*)(yi + ch * PB_Y + (64 * (4 * rh + c) + 4 * i16 + (g ^ ((i16 >> 2) & 3))) * 8) = res[c];
    }
    __syncthreads();
    {
        const rsrc_t ro = mkrs(P.out, 2048 * 2560);
        const int q = i16 >> 2, p = i16 & 3;
        u32x2 v[4];
#pragma unroll
        for (int i = 0; i < 4; ++i) {
            const int sg = ((256 * w + 64 * i + 16 * g) >> 2) + p;
            v[i] = trread(yi + q * PB_Y + (sg ^ ((sg >> 4) & 3)) * 8);
        }
#pragma unroll
        for (int i = 0; i < 4; ++i)
            __builtin_amdgcn_raw_buffer_store_b64(v[i], ro, (256 * w + 64 * i + 16 * g + i16) * 2560 + 1280 + chb * 8, 0, 0);
    }
}

__global__ void __launch_bounds__(512, 2) fused_kernel(Args P) {
    extern __shared__ __attribute__((aligned(16))) char smem[];
    const int t = threadIdx.x;
    const int w = __builtin_amdgcn_readfirstlane(t >> 6);
    const int b = blockIdx.x, xcd = b & 7, jx = b >> 3;
    if (jx < 20) role_b(P, smem, t, w, 20 * xcd + jx);
    else role_a(P, smem, t, w, xcd, jx - 20);
}

extern "C" void kernel_launch(void* const* d_in, const int* in_sizes, int n_in,
                              void* d_out, int out_size, void* d_ws, size_t ws_size,
                              hipStream_t stream) {
    Args a;
    a.x = (const u16*)d_in[0];
    a.wr = (const u16*)d_in[1];
    a.wi = (const u16*)d_in[2];
    a.u = (const u16*)d_in[3];
    a.Are = (const u16*)d_in[4];
    a.Aim = (const u16*)d_in[5];
    a.Bre = (const u16*)d_in[6];
    a.Bim = (const u16*)d_in[7];
    a.Cre = (const u16*)d_in[8];
    a.Cim = (const u16*)d_in[9];
    a.logdt = (const u16*)d_in[10];
    a.dskip = (const u16*)d_in[11];
    a.ws = (char*)d_ws;
    a.out = (u16*)d_out;
    (void)hipFuncSetAttribute((const void*)fused_kernel, hipFuncAttributeMaxDynamicSharedMemorySize, LDS_BYTES);
    fused_kernel<<<256, 512, LDS_BYTES, stream>>>(a);
}
```
